```python
import torch

def _softmax(x, axis=-1):
    return torch.softmax(x, dim=axis)
def _gelu(x):
    return torch.nn.functional.gelu(x, approximate="tanh")
def _silu(x):
    return torch.nn.functional.silu(x)
def _sigmoid(x):
    return torch.sigmoid(x)
def _layernorm(x, axis=-1, eps=1e-5):
    m = x.mean(dim=axis, keepdim=True)
    v = x.var(dim=axis, keepdim=True, unbiased=False)
    return (x - m) / torch.sqrt(v + eps)
def _scatter_add(tgt, idx, src, axis=0):
    return tgt.clone().index_add_(axis, idx, src)
def _segment_sum(src, seg, n):
    out = torch.zeros((n,) + src.shape[1:], dtype=src.dtype)
    return out.index_add_(0, seg, src)
def _pad(x, pw):
    flat = tuple(v for pair in reversed(pw) for v in pair)
    return torch.nn.functional.pad(x, flat)

kernel_name = 'parallel_sum__gqa_block_cached__mixer_channel_mlp__decode'

def setup_inputs():

    _w = (lambda a: (lambda t: t.to(torch.bfloat16) if t.is_floating_point() else t)(torch.as_tensor(a)))
    def _A_setup():
        B, Sq, D = 4, 1, 1024
        Hq, Hkv, Sk, Dh = 32, 4, 16384, 128
        assert Hq % Hkv == 0, (Hq, Hkv)
        return {
            "x": _w((lambda *s: torch.randn(*s, dtype=torch.bfloat16) * 0.05)(B, Sq, D)),
            "k_cache": _w((lambda *s: torch.randn(*s, dtype=torch.bfloat16) * 3.2)(B, Hkv, Sk, Dh)),
            "v_cache": _w((lambda *s: torch.randn(*s, dtype=torch.bfloat16) * 0.4)(B, Hkv, Sk, Dh)),
            "wq": _w((lambda *s: torch.randn(*s, dtype=torch.bfloat16) * 3.2)(D, Hq * Dh) * (D ** -0.5)),
            "wo": _w((lambda *s: torch.randn(*s, dtype=torch.bfloat16) * 0.4)(Hq * Dh, D) * ((Hq * Dh) ** -0.5)),
        }
    _A_in = _A_setup()

    def _B_setup():
        B, S, D, F = 4, 1, 1024, 3584
        return {
            "x": _w((lambda *s: torch.randn(*s, dtype=torch.bfloat16) * 0.05)(B, S, D)),
            "w1": _w((lambda *s: torch.randn(*s, dtype=torch.bfloat16) * 3.2)(D, F) * (D ** -0.5)),
            "b1": _w((lambda *s: torch.randn(*s, dtype=torch.bfloat16) * 1.6)(F)),
            "w2": _w((lambda *s: torch.randn(*s, dtype=torch.bfloat16) * 0.1)(F, D) * (F ** -0.5)),
            "b2": _w((lambda *s: torch.randn(*s, dtype=torch.bfloat16) * 0.1)(D)),
        }
    _B_in = _B_setup()

    return {
        "A_x": _A_in["x"],
        "A_k_cache": _A_in["k_cache"],
        "A_v_cache": _A_in["v_cache"],
        "A_wq": _A_in["wq"],
        "A_wo": _A_in["wo"],
        "B_x": _B_in["x"],
        "B_w1": _B_in["w1"],
        "B_b1": _B_in["b1"],
        "B_w2": _B_in["w2"],
        "B_b2": _B_in["b2"],
    }

def reference(A_x, A_k_cache, A_v_cache, A_wq, A_wo, B_x, B_w1, B_b1, B_w2, B_b2):
    def _A_ref(x, k_cache, v_cache, wq, wo):
        B, Sq, D = x.shape
        Hq, Dh = 32, 128
        Hkv = k_cache.shape[1]
        rep = Hq // Hkv
        q = (lambda x, *d: x.permute(*d))((x @ wq).reshape(B, Sq, Hq, Dh), 0, 2, 1, 3)
        k = (lambda x, n, axis: torch.repeat_interleave(x, n, dim=axis))(k_cache, rep, axis=1)
        v = (lambda x, n, axis: torch.repeat_interleave(x, n, dim=axis))(v_cache, rep, axis=1)
        scores = torch.einsum("bhqd,bhkd->bhqk", q, k) * (Dh ** -0.5)
        p = _softmax(scores, axis=-1)
        o = torch.einsum("bhqk,bhkd->bhqd", p, v)
        return (lambda x, *d: x.permute(*d))(o, 0, 2, 1, 3).reshape(B, Sq, Hq * Dh) @ wo

    def _B_ref(x, w1, b1, w2, b2):
        y = _layernorm(x, axis=-1)
        h = _gelu(torch.einsum("bsd,df->bsf", y, w1) + b1)
        return torch.einsum("bsf,fd->bsd", h, w2) + b2

    x = A_x
    xn = _layernorm(x, axis=-1)
    return x + _A_ref(xn, A_k_cache, A_v_cache, A_wq, A_wo) + _B_ref(xn, B_w1, B_b1, B_w2, B_b2)
```

```cpp
#include <hip/hip_runtime.h>
#include <hip/hip_bf16.h>
#include <stdint.h>

#define MIMIC 1

typedef unsigned short u16;
typedef unsigned v4u __attribute__((ext_vector_type(4)));
constexpr int NB = 4, DM = 1024, HQ = 32, HKV = 4, SK = 16384, DH = 128, FF = 3584;
constexpr int NSPLIT = 64;

__device__ __forceinline__ float bf2f(u16 b) { return __uint_as_float(((unsigned)b) << 16); }
__device__ __forceinline__ u16 f2bf(float f) { unsigned u = __float_as_uint(f); u += 0x7FFFu + ((u >> 16) & 1u); return (u16)(u >> 16); }
__device__ __forceinline__ float rb(float f) { return MIMIC ? bf2f(f2bf(f)) : f; }

__device__ float block_sum(float v, float* red) {
  for (int o = 32; o >= 1; o >>= 1) v += __shfl_xor(v, o);
  __syncthreads();
  if ((threadIdx.x & 63) == 0) red[threadIdx.x >> 6] = v;
  __syncthreads();
  float s = 0.f;
  for (int i = 0; i < (int)(blockDim.x >> 6); ++i) s += red[i];
  return s;
}

__global__ void __launch_bounds__(256) k1(const u16* x, const u16* wq, const u16* w1, const u16* b1, float* q, float* h) {
  __shared__ float xn[NB][DM];
  __shared__ float red[8];
  __shared__ float part[8][NB][32];
  const int t = threadIdx.x;
  const bool isq = blockIdx.x < (HQ * DH / 32);
  for (int b = 0; b < NB; ++b) {
    float v[4];
    for (int i = 0; i < 4; ++i) v[i] = bf2f(x[b * DM + t + 256 * i]);
    for (int pass = 0; pass < (isq ? 1 : 2); ++pass) {
      float s = block_sum(v[0] + v[1] + v[2] + v[3], red);
      float mf = s / DM;
      float m = rb(mf);
      float ss = 0.f;
      for (int i = 0; i < 4; ++i) ss += (v[i] - mf) * (v[i] - mf);
      float var = rb(block_sum(ss, red) / DM);
      float den = rb(sqrtf(rb(var + 1e-5f)));
      for (int i = 0; i < 4; ++i) v[i] = rb(rb(v[i] - m) / den);
    }
    for (int i = 0; i < 4; ++i) xn[b][t + 256 * i] = v[i];
  }
  __syncthreads();
  const int c = t & 31, g = t >> 5;
  const u16* w = isq ? wq : w1;
  const int ld = isq ? HQ * DH : FF;
  const int col = (isq ? blockIdx.x : blockIdx.x - HQ * DH / 32) * 32 + c;
  float acc[NB] = {0, 0, 0, 0};
  for (int k = g; k < DM; k += 8) {
    float wv = bf2f(w[(size_t)k * ld + col]);
    for (int b = 0; b < NB; ++b) acc[b] += xn[b][k] * wv;
  }
  for (int b = 0; b < NB; ++b) part[g][b][c] = acc[b];
  __syncthreads();
  if (t < 128) {
    int b = t >> 5;
    float s = 0.f;
    for (int i = 0; i < 8; ++i) s += part[i][b][c];
    if (isq) q[b * HQ * DH + col] = rb(s);
    else {
      float u = rb(rb(s) + bf2f(b1[col]));
      float gl = 0.5f * u * (1.f + tanhf(0.7978845608028654f * (u + 0.044715f * u * u * u)));
      h[b * FF + col] = rb(gl);
    }
  }
}

__global__ void __launch_bounds__(256) k2(const u16* kc, const u16* vc, const float* q, float* pm, float* pl, float* po) {
  __shared__ float qs[8][DH];
  __shared__ float p[8][256];
  __shared__ float red[8];
  __shared__ float ox[2][8][DH];
  const int s = blockIdx.x, kvh = blockIdx.y, b = blockIdx.z, t = threadIdx.x;
  for (int i = t; i < 8 * DH; i += 256) qs[i >> 7][i & 127] = q[b * HQ * DH + kvh * 8 * DH + i];
  __syncthreads();
  const size_t base = ((size_t)(b * HKV + kvh) * SK + s * 256) * DH;
  const v4u* kp = (const v4u*)(kc + base + (size_t)t * DH);
  float sc[8] = {0, 0, 0, 0, 0, 0, 0, 0};
  for (int c = 0; c < 16; ++c) {
    v4u kv = kp[c];
    for (int j = 0; j < 4; ++j) {
      float lo = __uint_as_float(kv[j] << 16), hi = __uint_as_float(kv[j] & 0xffff0000u);
      for (int hh = 0; hh < 8; ++hh) sc[hh] += lo * qs[hh][c * 8 + 2 * j] + hi * qs[hh][c * 8 + 2 * j + 1];
    }
  }
  for (int hh = 0; hh < 8; ++hh) {
    float v = rb(rb(sc[hh]) * 0.08838834764831845f);
    float m = v;
    for (int o = 32; o >= 1; o >>= 1) m = fmaxf(m, __shfl_xor(m, o));
    __syncthreads();
    if ((t & 63) == 0) red[t >> 6] = m;
    __syncthreads();
    m = fmaxf(fmaxf(red[0], red[1]), fmaxf(red[2], red[3]));
    float e = __expf(v - m);
    p[hh][t] = e;
    float l = block_sum(e, red);
    if (t == 0) { pm[((b * HQ + kvh * 8 + hh)) * NSPLIT + s] = m; pl[((b * HQ + kvh * 8 + hh)) * NSPLIT + s] = l; }
  }
  __syncthreads();
  const int d = t & 127, half = t >> 7;
  float acc[8] = {0, 0, 0, 0, 0, 0, 0, 0};
  for (int k = half * 128; k < half * 128 + 128; ++k) {
    float vv = bf2f(vc[base + (size_t)k * DH + d]);
    for (int hh = 0; hh < 8; ++hh) acc[hh] += p[hh][k] * vv;
  }
  for (int hh = 0; hh < 8; ++hh) ox[half][hh][d] = acc[hh];
  __syncthreads();
  for (int i = t; i < 8 * DH; i += 256) {
    int hh = i >> 7, dd = i & 127;
    po[((size_t)(b * HQ + kvh * 8 + hh) * NSPLIT + s) * DH + dd] = ox[0][hh][dd] + ox[1][hh][dd];
  }
}

__global__ void __launch_bounds__(128) k3(const float* pm, const float* pl, const float* po, float* o) {
  const int bh = blockIdx.x, d = threadIdx.x;
  float M = -INFINITY;
  for (int s = 0; s < NSPLIT; ++s) M = fmaxf(M, pm[bh * NSPLIT + s]);
  float L = 0.f, a = 0.f;
  for (int s = 0; s < NSPLIT; ++s) {
    float w = __expf(pm[bh * NSPLIT + s] - M);
    L += w * pl[bh * NSPLIT + s];
    a += w * po[((size_t)bh * NSPLIT + s) * DH + d];
  }
  o[bh * DH + d] = rb(a / L);
}

__global__ void __launch_bounds__(256) k4(const u16* x, const float* o, const float* h, const u16* wo, const u16* w2, const u16* b2, u16* out) {
  __shared__ float pa[64][NB][4], pb[64][NB][4];
  const int t = threadIdx.x, c = t & 3, g = t >> 2;
  const int col = blockIdx.x * 4 + c;
  float a[NB] = {0, 0, 0, 0}, bb[NB] = {0, 0, 0, 0};
  for (int k = g; k < HQ * DH; k += 64) { float w = bf2f(wo[(size_t)k * DM + col]); for (int b = 0; b < NB; ++b) a[b] += o[b * HQ * DH + k] * w; }
  for (int k = g; k < FF; k += 64) { float w = bf2f(w2[(size_t)k * DM + col]); for (int b = 0; b < NB; ++b) bb[b] += h[b * FF + k] * w; }
  for (int b = 0; b < NB; ++b) { pa[g][b][c] = a[b]; pb[g][b][c] = bb[b]; }
  __syncthreads();
  if (t < 16) {
    int b = t >> 2;
    float sa = 0.f, sb = 0.f;
    for (int i = 0; i < 64; ++i) { sa += pa[i][b][c]; sb += pb[i][b][c]; }
    float A = rb(sa), Bv = rb(rb(sb) + bf2f(b2[col]));
    float r = rb(bf2f(x[b * DM + col]) + A) + Bv;
    out[b * DM + col] = f2bf(r);
  }
}

extern "C" void kernel_launch(void* const* d_in, const int* in_sizes, int n_in, void* d_out, int out_size, void* d_ws, size_t ws_size, hipStream_t stream) {
  const u16* x = (const u16*)d_in[0];
  const u16* kc = (const u16*)d_in[1];
  const u16* vc = (const u16*)d_in[2];
  const u16* wq = (const u16*)d_in[3];
  const u16* wo = (const u16*)d_in[4];
  const u16* w1 = (const u16*)d_in[6];
  const u16* b1 = (const u16*)d_in[7];
  const u16* w2 = (const u16*)d_in[8];
  const u16* b2 = (const u16*)d_in[9];
  float* ws = (float*)d_ws;
  float* q = ws;
  float* h = q + NB * HQ * DH;
  float* pm = h + NB * FF;
  float* pl = pm + NB * HQ * NSPLIT;
  float* po = pl + NB * HQ * NSPLIT;
  float* o = po + (size_t)NB * HQ * NSPLIT * DH;
  k1<<<(HQ * DH + FF) / 32, 256, 0, stream>>>(x, wq, w1, b1, q, h);
  k2<<<dim3(NSPLIT, HKV, NB), 256, 0, stream>>>(kc, vc, q, pm, pl, po);
  k3<<<NB * HQ, 128, 0, stream>>>(pm, pl, po, o);
  k4<<<DM / 4, 256, 0, stream>>>(x, o, h, wo, w2, b2, (u16*)d_out);
}
```

```cpp
#include <hip/hip_runtime.h>
#include <hip/hip_bf16.h>
#include <stdint.h>

#define STAMPS 0
#define NPRE 0
#define CODEPF 0
#define FLPOS 21

namespace {
typedef unsigned v4u __attribute__((ext_vector_type(4)));
typedef unsigned v2u __attribute__((ext_vector_type(2)));
typedef float f32x4 __attribute__((ext_vector_type(4)));
typedef short bf16x8 __attribute__((ext_vector_type(8)));
typedef __bf16 bf16x2 __attribute__((ext_vector_type(2)));
typedef float f32x2 __attribute__((ext_vector_type(2)));
typedef __amdgpu_buffer_rsrc_t rsrc_t;
#define GAS __attribute__((address_space(1)))
#define LAS __attribute__((address_space(3)))

constexpr int NWG = 256, NTHR = 512;
constexpr int DM = 1024, NQ = 4096, FF = 3584;
constexpr int WS_E = 0;
constexpr int WS_FLAG = 4096;
constexpr int WS_Q = 8192;
constexpr int WS_H = WS_Q + 4 * 32 * 8 * 512;
constexpr int WS_O = WS_H + 8 * 4 * FF * 4;
constexpr int WS_ML = WS_O + 32 * 4 * 16 * 512;
constexpr int WS_F = WS_ML + 32 * 4 * 16 * 8;
constexpr int WS_END = WS_F + 256 * 32 * 64;
constexpr int WS_STAMP = 8 << 20;
constexpr int OOB = 0x7f000000;
constexpr int AUX_NT = 2, AUX_SC1 = 16;
constexpr unsigned POISON = 0xFFFFFFFFu;
constexpr int SPIN_CAP = 1 << 14;
constexpr int L_XN = 0;
constexpr int L_Q = L_XN + 2048;
constexpr int L_M = L_Q + 2048;
constexpr int L_L = L_M + 256;
constexpr int L_RED = L_L + 256;
constexpr int RED_W = 8 * 18 * 16;
constexpr int L_ACC = L_RED + 8 * RED_W;
constexpr int L_O = L_ACC + 4 * 8192;
constexpr int O_W = 8 * 132 * 4;
constexpr int L_H = L_O + 4 * O_W;
constexpr int L_OA = L_H + 1024;
constexpr int L_END = L_OA + 1024;

struct Params {
  const void *x, *kc, *vc, *wq, *wo, *w1, *b1, *w2, *b2;
  void *out, *ws;
};

__device__ __forceinline__ rsrc_t mk(const void* p, int bytes) { return __builtin_amdgcn_make_buffer_rsrc((void*)p, 0, bytes, 0x00020000); }
template <int AUX> __device__ __forceinline__ v4u ld16(rsrc_t r, int voff, int soff) { return __builtin_amdgcn_raw_buffer_load_b128(r, voff, soff, AUX); }
template <int AUX> __device__ __forceinline__ v2u ld8(rsrc_t r, int voff, int soff) { return __builtin_amdgcn_raw_buffer_load_b64(r, voff, soff, AUX); }
__device__ __forceinline__ void st16(v4u v, rsrc_t r, int voff, int soff) { __builtin_amdgcn_raw_buffer_store_b128(v, r, voff, soff, AUX_SC1); }
__device__ __forceinline__ void st8(v2u v, rsrc_t r, int voff, int soff) { __builtin_amdgcn_raw_buffer_store_b64(v, r, voff, soff, AUX_SC1); }
__device__ __forceinline__ f32x4 mfma(v4u a, v4u b, f32x4 c) {
  return __builtin_amdgcn_mfma_f32_16x16x32_bf16(__builtin_bit_cast(bf16x8, a), __builtin_bit_cast(bf16x8, b), c, 0, 0, 0);
}
__device__ __forceinline__ float u2f(unsigned u) { return __builtin_bit_cast(float, u); }
__device__ __forceinline__ unsigned f2u(float f) { return __builtin_bit_cast(unsigned, f); }
__device__ __forceinline__ unsigned pk(float lo, float hi) {
  f32x2 v = {lo, hi};
  return __builtin_bit_cast(unsigned, __builtin_convertvector(v, bf16x2));
}
__device__ __forceinline__ float blo(unsigned u) { return u2f(u << 16); }
__device__ __forceinline__ float bhi(unsigned u) { return u2f(u & 0xffff0000u); }
__device__ __forceinline__ float rbf(float f) { return blo(pk(f, f)); }
template <int N> __device__ __forceinline__ float ror(float x) { return u2f(__builtin_amdgcn_mov_dpp(f2u(x), 0x120 + N, 0xf, 0xf, true)); }
template <int N> __device__ __forceinline__ unsigned roru(unsigned x) { return __builtin_amdgcn_mov_dpp(x, 0x120 + N, 0xf, 0xf, true); }
__device__ __forceinline__ float xrow_sum(float x) {
  auto s = __builtin_amdgcn_permlane16_swap(f2u(x), f2u(x), false, false);
  x = u2f(s[0]) + u2f(s[1]);
  auto t = __builtin_amdgcn_permlane32_swap(f2u(x), f2u(x), false, false);
  return u2f(t[0]) + u2f(t[1]);
}
__device__ __forceinline__ float xrow_max(float x) {
  auto s = __builtin_amdgcn_permlane16_swap(f2u(x), f2u(x), false, false);
  x = fmaxf(u2f(s[0]), u2f(s[1]));
  auto t = __builtin_amdgcn_permlane32_swap(f2u(x), f2u(x), false, false);
  return fmaxf(u2f(t[0]), u2f(t[1]));
}
__device__ __forceinline__ float wave_sum(float x) {
  x += ror<8>(x); x += ror<4>(x); x += ror<2>(x); x += ror<1>(x);
  return xrow_sum(x);
}
__device__ __forceinline__ unsigned umax4(v4u v) { return max(max(v.x, v.y), max(v.z, v.w)); }
__device__ __forceinline__ v4u nopoison(f32x4 v) {
  v4u u = __builtin_bit_cast(v4u, v);
  return v4u{min(u.x, POISON - 1), min(u.y, POISON - 1), min(u.z, POISON - 1), min(u.w, POISON - 1)};
}
#define PIN() __builtin_amdgcn_sched_barrier(0)
#define BAR() do { PIN(); asm volatile("s_waitcnt lgkmcnt(0)\n\ts_barrier" ::: "memory"); PIN(); } while (0)
#define REREAD() asm volatile("" ::: "memory")
__device__ __forceinline__ v4u tr8(const v4u* w, int j0) {
  const unsigned sel = (j0 & 1) ? 0x07060302u : 0x05040100u;
  const int c = j0 >> 1;
  return v4u{__builtin_amdgcn_perm(w[1][c], w[0][c], sel), __builtin_amdgcn_perm(w[3][c], w[2][c], sel),
             __builtin_amdgcn_perm(w[5][c], w[4][c], sel), __builtin_amdgcn_perm(w[7][c], w[6][c], sel)};
}
__device__ __forceinline__ void layernorm16(float (&v)[16]) {
  float s = 0.f;
#pragma unroll
  for (int i = 0; i < 16; ++i) s += v[i];
  const float mf = wave_sum(s) * (1.0f / DM);
  float ss = 0.f;
#pragma unroll
  for (int i = 0; i < 16; ++i) { const float d = v[i] - mf; ss = fmaf(d, d, ss); }
  const float var = rbf(wave_sum(ss) * (1.0f / DM));
  const float den = rbf(__builtin_amdgcn_sqrtf(rbf(var + 1e-5f)));
  const float m = rbf(mf), inv = __builtin_amdgcn_rcpf(den);
#pragma unroll
  for (int i = 0; i < 16; i += 2) {
    const unsigned d = pk(v[i] - m, v[i + 1] - m);
    const unsigned o = pk(blo(d) * inv, bhi(d) * inv);
    v[i] = blo(o); v[i + 1] = bhi(o);
  }
}

#define STAMP(i)
#define STAMPV(i, v)
#define STAMP_FLUSH()

__global__ void __launch_bounds__(NTHR, 2) fused_decode(Params p) {
  __shared__ __attribute__((aligned(16))) unsigned char smem[L_END];
  LAS unsigned char* L = (LAS unsigned char*)smem;
  const int tid = threadIdx.x, lane = tid & 63;
  const int w = __builtin_amdgcn_readfirstlane(tid >> 6);
  const int wl = w & 3;
  const bool isA = w < 4;
  const int xs = blockIdx.x & 7, ci = blockIdx.x >> 3;
  const int n16 = lane & 15, g = lane >> 4;
  const rsrc_t rws = mk(p.ws, WS_END);
  const int gidx = 2 * xs + (ci >> 4), ab = gidx >> 2, kvh = gidx & 3, sp = ci & 15;
  const int hd = 4 * xs + (ci >> 3), cg = ci & 7;
  const int cu = xs * 32 + ci;

  asm volatile("" :: "s"(p.x), "s"(p.kc), "s"(p.vc), "s"(p.wq), "s"(p.wo), "s"(p.w1), "s"(p.b1), "s"(p.w2), "s"(p.b2), "s"(p.out), "s"(p.ws));
  if (isA) __builtin_amdgcn_s_setprio(1); else __builtin_amdgcn_s_setprio(2);
  int zoff = WS_E;
  asm volatile("" : "+v"(zoff));
  const v2u gen = ld8<AUX_SC1>(rws, zoff, 0);
  v4u xr[2];
  if (!isA) {
    const rsrc_t rx = mk(p.x, 4 * DM * 2);
    const int xo = wl * 2048 + lane * 32;
    xr[0] = ld16<0>(rx, xo, 0);
    xr[1] = ld16<0>(rx, xo + 16, 0);
  }
  PIN();
  unsigned cpf = 0;
  PIN();
  if (w == 7) {
    const v4u pz = {POISON, POISON, POISON, POISON};
    const int hf = lane >> 5, c4 = lane & 31;
#pragma unroll
    for (int i = 0; i < 2; ++i) st16(pz, rws, WS_Q + (((2 * i + hf) * 32 + ci) * 8 + xs) * 512 + c4 * 16, 0);
#pragma unroll
    for (int i = 0; i < 2; ++i) st16(pz, rws, ci < 28 ? WS_H + ((xs * 4 + 2 * i + hf) * FF + ci * 128) * 4 + c4 * 16 : OOB, 0);
#pragma unroll
    for (int i = 0; i < 4; ++i) st16(pz, rws, WS_O + (((8 * kvh + 2 * i + hf) * 4 + ab) * 16 + sp) * 512 + c4 * 16, 0);
    st8(v2u{POISON, POISON}, rws, lane < 8 ? WS_ML + (((8 * kvh + lane) * 4 + ab) * 16 + sp) * 8 : OOB, 0);
#pragma unroll
    for (int i = 0; i < 2; ++i) st16(pz, rws, WS_F + ((32 * cg + 16 * i + (lane >> 2)) * 32 + hd) * 64 + (lane & 3) * 16, 0);
  }
  PIN();
  v4u wv[8];
  {
    const int ldw = isA ? NQ * 2 : FF * 2;
    const rsrc_t rw = mk(isA ? p.wq : p.w1, DM * ldw);
    const int wo_ = (isA || ci < 28) ? (128 * xs + 32 * wl + 8 * g) * ldw + ci * 256 + n16 * 16 : OOB;
#pragma unroll
    for (int j = 0; j < 8; ++j) wv[j] = ld16<AUX_NT>(rw, wo_, j * ldw);
  }
  PIN();
  STAMP(14);
  BAR();
#define MAKE_TAG() v2u gen_ = gen; asm volatile("" : "+v"(gen_)); \
  const unsigned long long e1 = (((unsigned long long)gen_.y << 32) | gen_.x) + 1ull, tag64 = e1 * 0x9E3779B97F4A7C15ull; \
  const unsigned tag0 = (unsigned)tag64, tag1 = (unsigned)(tag64 >> 32)

#define P1_MFMA() do { \
    const v4u act = *(const LAS v4u*)(L + L_XN + (isA ? 0 : 1024) + (n16 & 3) * 256 + (32 * wl + 8 * g) * 2); \
    f32x4 d_[8]; \
    _Pragma("unroll") for (int j0 = 0; j0 < 8; ++j0) d_[j0] = mfma(act, tr8(wv, j0), f32x4{0.f, 0.f, 0.f, 0.f}); \
    if (lane < 16) { _Pragma("unroll") for (int j0 = 0; j0 < 8; ++j0) *(LAS f32x4*)(L + L_RED + w * RED_W + (j0 * 17 + lane) * 16) = d_[j0]; } \
    } while (0)

  if (!isA) {
    float v[16];
#pragma unroll
    for (int i = 0; i < 8; ++i) { v[2 * i] = blo(xr[i >> 2][i & 3]); v[2 * i + 1] = bhi(xr[i >> 2][i & 3]); }
    if (w == 7) {
      MAKE_TAG();
      asm volatile("s_waitcnt vmcnt(8)" ::: "memory");
      st8(v2u{tag0, tag1}, rws, lane == 0 ? WS_FLAG + cu * 8 : OOB, 0);
    }
    asm volatile("" : "+v"(v[0]), "+v"(v[15]));
    STAMP(1);
#pragma unroll
    for (int pass = 0; pass < 2; ++pass) {
      layernorm16(v);
      if ((lane >> 3) == xs) {
        LAS v4u* d = (LAS v4u*)(L + L_XN + pass * 1024 + wl * 256 + (lane & 7) * 32);
        d[0] = v4u{pk(v[0], v[1]), pk(v[2], v[3]), pk(v[4], v[5]), pk(v[6], v[7])};
        d[1] = v4u{pk(v[8], v[9]), pk(v[10], v[11]), pk(v[12], v[13]), pk(v[14], v[15])};
      }
    }
    STAMP(2);
    BAR();
    STAMP(4);
    P1_MFMA();
    STAMP(5);
    BAR();
    const int mat = wl >> 1, b = lane & 3, jh = (lane >> 2) & 1, nn = (wl & 1) * 8 + (lane >> 3);
    f32x4 sum = {0.f, 0.f, 0.f, 0.f};
#pragma unroll
    for (int ww = 0; ww < 4; ++ww)
#pragma unroll
      for (int k = 0; k < 4; ++k) sum[k] += *(const LAS float*)(L + L_RED + (mat * 4 + ww) * RED_W + ((4 * jh + k) * 17 + nn) * 16 + b * 4);
    const int co = (8 * nn + 4 * jh) * 4;
    const int off = mat == 0 ? WS_Q + ((b * 32 + ci) * 8 + xs) * 512 + co : (ci < 28 ? WS_H + ((xs * 4 + b) * FF + ci * 128) * 4 + co : OOB);
    st16(nopoison(sum), rws, off, 0);
    PIN();
    __builtin_amdgcn_s_setprio(0);
    STAMP(3);
  }
  PIN();
  const size_t kvbase = ((size_t)gidx * 16 + sp) * 262144;
  const rsrc_t rk = mk((const char*)p.kc + kvbase, 262144), rv = mk((const char*)p.vc + kvbase, 262144);
  const int koff = (w >> 2) * 131072 + wl * 4096 + ((lane >> 2) & 3) * 256 + g * 64 + (lane & 3) * 16;
  const int voff = (w >> 2) * 131072 + wl * 4096 + lane * 16;
  v4u r[32];
#define KLD(i) do { r[i] = ld16<AUX_NT>(rk, koff, ((i) >> 2) * 16384 + ((i) & 3) * 1024); PIN(); } while (0)
#define VLD(i) do { r[i] = ld16<AUX_NT>(rv, voff, ((i) >> 2) * 16384 + ((i) & 3) * 1024); PIN(); } while (0)
#pragma unroll
  for (int i = 0; i < 16; ++i) KLD(i);
  PIN();
  if (isA) {
    STAMP(1);
    BAR();
    P1_MFMA();
    asm volatile("" :: "v"(cpf));
    BAR();
    STAMP(2);
  }
  PIN();
#pragma unroll
  for (int i = 16; i < FLPOS; ++i) KLD(i);
  PIN();
  v4u fl[2];
  if (isA) {
    fl[0] = ld16<AUX_SC1>(rws, WS_FLAG + lane * 16, 0);
    fl[1] = ld16<AUX_SC1>(rws, WS_FLAG + lane * 16, 1024);
  }
  PIN();
#pragma unroll
  for (int i = FLPOS; i < 32; ++i) KLD(i);
  PIN();
  if (isA) {
    STAMP(3);
    MAKE_TAG();
    int spins = 0;
#define FLAGS_BAD(a, b) __any((((a).x ^ tag0) | ((a).y ^ tag1) | ((a).z ^ tag0) | ((a).w ^ tag1) | ((b).x ^ tag0) | ((b).y ^ tag1) | ((b).z ^ tag0) | ((b).w ^ tag1)) != 0u)
    if (FLAGS_BAD(fl[0], fl[1])) {
      _Pragma("nounroll") while (true) {
        REREAD();
        __builtin_amdgcn_s_sleep(4);
        const v4u f0 = ld16<AUX_SC1>(rws, WS_FLAG + lane * 16, 0), f1 = ld16<AUX_SC1>(rws, WS_FLAG + lane * 16, 1024);
        if (++spins > SPIN_CAP || !FLAGS_BAD(f0, f1)) break;
      }
    }
    REREAD();
    STAMPV(18, spins);
    STAMP(4);
    if (blockIdx.x == 0 && w == 0) {
      st8(v2u{(unsigned)e1, (unsigned)(e1 >> 32)}, rws, lane == 0 ? WS_E : OOB, 0);
    }
    const int qo = WS_Q + ((ab * 32 + 8 * kvh + 2 * wl + (lane >> 5)) * 8) * 512 + (lane & 31) * 16;
    const int hc = lane & 7, hbt = (lane >> 3) & 3, hb = 112 * hd + 28 * wl + 4 * hc;
    const int ho = hc < 7 ? WS_H + ((16 * (lane >> 5) + hbt) * FF + hb) * 4 : OOB;
    const v2u bia = ld8<0>(mk(p.b1, FF * 2), hc < 7 ? hb * 2 : OOB, 0);
    v4u qp[8], hp[4];
    spins = 0;
    _Pragma("nounroll") while (true) {
      REREAD();
#pragma unroll
      for (int s = 0; s < 8; ++s) qp[s] = ld16<AUX_SC1>(rws, qo, s * 512);
#pragma unroll
      for (int s = 0; s < 4; ++s) hp[s] = ld16<AUX_SC1>(rws, ho, s * 4 * FF * 4);
      PIN();
      unsigned mx = 0;
#pragma unroll
      for (int s = 0; s < 8; ++s) mx = max(mx, umax4(qp[s]));
#pragma unroll
      for (int s = 0; s < 4; ++s) mx = max(mx, umax4(hp[s]));
      if (!__any(mx == POISON) || ++spins > SPIN_CAP) break;
      __builtin_amdgcn_s_sleep(8);
    }
    STAMPV(19, spins);
    STAMP(5);
    f32x4 qs = __builtin_bit_cast(f32x4, qp[0]);
#pragma unroll
    for (int s = 1; s < 8; ++s) qs += __builtin_bit_cast(f32x4, qp[s]);
    *(LAS v2u*)(L + L_Q + (2 * wl + (lane >> 5)) * 256 + (lane & 31) * 8) = v2u{pk(qs[0], qs[1]), pk(qs[2], qs[3])};
    float hv[4];
#pragma unroll
    for (int j = 0; j < 4; ++j) {
      const float t0 = (u2f(hp[0][j]) + u2f(hp[1][j])) + (u2f(hp[2][j]) + u2f(hp[3][j]));
      auto sw = __builtin_amdgcn_permlane32_swap(f2u(t0), f2u(t0), false, false);
      const float t = u2f(sw[0]) + u2f(sw[1]);
      const float u = rbf(rbf(t) + ((j & 1) ? bhi(bia[j >> 1]) : blo(bia[j >> 1])));
      const float z2 = u * fmaf(u * u, 0.044715f, 1.0f) * (-2.0f * 0.7978845608028654f * 1.4426950408889634f);
      hv[j] = u * __builtin_amdgcn_rcpf(1.0f + __builtin_amdgcn_exp2f(z2));
    }
    if (lane < 32) *(LAS v2u*)(L + L_H + wl * 256 + hbt * 64 + hc * 8) = v2u{pk(hv[0], hv[1]), pk(hv[2], hv[3])};
  }
  STAMP(6);
  BAR();
  STAMP(15);
  const int hl = n16 & 7, wb = n16 >> 3;
  const v4u qb0 = *(const LAS v4u*)(L + L_Q + hl * 256 + (4 * g + wb) * 16);
  const v4u qb1 = *(const LAS v4u*)(L + L_Q + hl * 256 + (4 * g + 2 + wb) * 16);
  float s[16];
#pragma unroll
  for (int u = 0; u < 8; ++u) {
    float t0[4], t1[4];
#pragma unroll
    for (int k = 0; k < 4; ++k) {
      const f32x4 d0 = mfma(r[4 * u + k], qb0, f32x4{0.f, 0.f, 0.f, 0.f});
      const f32x4 d1 = mfma(r[4 * u + k], qb1, f32x4{0.f, 0.f, 0.f, 0.f});
      t0[k] = d0[0] + d1[2];
      t1[k] = d0[1] + d1[3];
    }
#pragma unroll
    for (int k = 0; k < 4; k += 2) {
      const float xx = wb ? t1[k + 1] : t0[k], yy = wb ? t1[k] : t0[k + 1];
      s[2 * u + (k >> 1)] = xx + ror<8>(yy);
    }
    PIN();
#pragma unroll
    for (int k = 0; k < 4; ++k) VLD(4 * u + k);
    PIN();
  }
  STAMP(7);
  float m = -INFINITY;
#pragma unroll
  for (int i = 0; i < 16; i += 2) {
    const unsigned a = pk(s[i], s[i + 1]);
    const unsigned c = pk(blo(a) * 0.08838834764831845f, bhi(a) * 0.08838834764831845f);
    s[i] = blo(c); s[i + 1] = bhi(c);
    m = fmaxf(m, fmaxf(s[i], s[i + 1]));
  }
  m = fmaxf(m, ror<8>(m));
  m = xrow_max(m);
  if (lane < 8) *(LAS float*)(L + L_M + (w * 8 + lane) * 4) = m;
  f32x4 acc[8];
  float lsum = 0.f;
#pragma unroll
  for (int j = 0; j < 8; ++j) acc[j] = f32x4{0.f, 0.f, 0.f, 0.f};
  constexpr float LOG2E = 1.4426950408889634f;
  if (!isA) {
    __builtin_amdgcn_s_setprio(2);
    BAR();
    const float mA = *(const LAS float*)(L + L_M + (wl * 8 + hl) * 4);
#pragma unroll
    for (int ww = 0; ww < 8; ++ww) m = fmaxf(m, *(const LAS float*)(L + L_M + (ww * 8 + hl) * 4));
    const float f = __builtin_amdgcn_exp2f((mA - m) * LOG2E);
    lsum = f * *(const LAS float*)(L + L_L + (wl * 8 + hl) * 4);
#pragma unroll
    for (int j = 0; j < 8; ++j) acc[j] = f * *(const LAS f32x4*)(L + L_ACC + wl * 8192 + j * 1024 + lane * 16);
  }
  PIN();
  unsigned pb[16];
  {
    float l = 0.f;
    const unsigned msk = wb ? 0xffff0000u : 0x0000ffffu;
#pragma unroll
    for (int i = 0; i < 16; ++i) {
      const float e = __builtin_amdgcn_exp2f((s[i] - m) * LOG2E);
      l += e;
      const unsigned t = pk(e, e) & msk;
      pb[i] = t | roru<8>(t);
    }
    l += ror<8>(l);
    lsum += xrow_sum(l);
    if (lane < 8) *(LAS float*)(L + L_L + (w * 8 + lane) * 4) = lsum;
  }
  PIN();
#pragma unroll
  for (int T = 0; T < 4; ++T) {
    const v4u pop = {pb[4 * T], pb[4 * T + 1], pb[4 * T + 2], pb[4 * T + 3]};
#pragma unroll
    for (int j0 = 0; j0 < 8; ++j0) acc[j0] = mfma(tr8(&r[8 * T], j0), pop, acc[j0]);
    PIN();
  }
  STAMP(8);
  if (isA) {
#pragma unroll
    for (int j = 0; j < 8; ++j) *(LAS f32x4*)(L + L_ACC + wl * 8192 + j * 1024 + lane * 16) = acc[j];
    BAR();
    const rsrc_t r2 = mk(p.w2, FF * DM * 2), ro = mk(p.wo, NQ * DM * 2);
    const int o2 = (112 * hd + 28 * wl + 8 * g) * 2048 + cg * 256 + n16 * 16;
    const int o2b = g < 3 ? o2 : OOB;
    const int oo = (128 * hd + 32 * wl + 8 * g) * 2048 + cg * 256 + n16 * 16;
    v4u tw[16];
#define TLD(j) do { tw[j] = (j) < 8 ? ld16<AUX_NT>(r2, (j) < 4 ? o2 : o2b, (j) * 2048) : ld16<AUX_NT>(ro, oo, ((j) - 8) * 2048); PIN(); } while (0)
#pragma unroll
    for (int j = 0; j < NPRE; ++j) TLD(j);
    STAMP(9);
    BAR();
#pragma unroll
    for (int j = NPRE; j < 16; ++j) TLD(j);
    const v4u hact = *(const LAS v4u*)(L + L_H + wl * 256 + (n16 & 3) * 64 + g * 16);
    f32x4 d[8];
#pragma unroll
    for (int j0 = 0; j0 < 8; ++j0) d[j0] = mfma(hact, tr8(tw, j0), f32x4{0.f, 0.f, 0.f, 0.f});
    v4u wt[8];
#pragma unroll
    for (int j0 = 0; j0 < 8; ++j0) { wt[j0] = tr8(tw + 8, j0); asm volatile("" : "+v"(wt[j0])); }
    STAMP(10);
    BAR();
    STAMP(11);
    const v4u oact = *(const LAS v4u*)(L + L_OA + (n16 & 3) * 256 + (32 * wl + 8 * g) * 2);
#pragma unroll
    for (int j0 = 0; j0 < 8; ++j0) d[j0] = mfma(oact, wt[j0], d[j0]);
    if (lane < 16) {
#pragma unroll
      for (int j0 = 0; j0 < 8; ++j0) *(LAS f32x4*)(L + L_RED + wl * RED_W + (j0 * 18 + lane) * 16) = d[j0];
    }
    BAR();
    STAMP(12);
    asm volatile(".Lfd_code_end:");
  } else {
#pragma unroll
    for (int i = 0; i < 4; ++i) {
      const f32x4 t = {wb ? acc[4][i] : acc[0][i], wb ? acc[5][i] : acc[1][i], wb ? acc[6][i] : acc[2][i], wb ? acc[7][i] : acc[3][i]};
      *(LAS f32x4*)(L + L_O + wl * O_W + (hl * 132 + 8 * (4 * g + i) + 4 * wb) * 4) = t;
    }
    const int fb = (lane >> 2) & 3, fc = 4 * cu + (lane & 3);
    const unsigned xres = __builtin_amdgcn_raw_buffer_load_b16(mk(p.x, 4 * DM * 2), (fb * DM + fc) * 2, 0, 0);
    const unsigned b2v = __builtin_amdgcn_raw_buffer_load_b16(mk(p.b2, DM * 2), fc * 2, 0, 0);
    BAR();
    STAMP(9);
    {
      const int hh = 2 * wl + (lane >> 5), c4 = lane & 31;
      f32x4 t = *(const LAS f32x4*)(L + L_O + (hh * 132 + 4 * c4) * 4);
#pragma unroll
      for (int ww = 1; ww < 4; ++ww) t += *(const LAS f32x4*)(L + L_O + ww * O_W + (hh * 132 + 4 * c4) * 4);
      st16(nopoison(t), rws, WS_O + (((8 * kvh + hh) * 4 + ab) * 16 + sp) * 512 + c4 * 16, 0);
      if (w == 7) {
        float l = 0.f;
#pragma unroll
        for (int ww = 4; ww < 8; ++ww) l += *(const LAS float*)(L + L_L + (ww * 8 + hl) * 4);
        st8(v2u{min(f2u(m), POISON - 1), min(f2u(l), POISON - 1)}, rws, lane < 8 ? WS_ML + (((8 * kvh + lane) * 4 + ab) * 16 + sp) * 8 : OOB, 0);
      }
    }
    STAMP(10);
    {
      const int hf = lane >> 5;
      const int rec = (hd * 4 + wl) * 16;
      const int oo_ = WS_O + (rec + 8 * hf) * 512 + (lane & 31) * 16, mo = WS_ML + (rec + ((n16 + 8 * hf) & 15)) * 8;
      v4u op[8];
      v2u ml;
      int spins = 0;
      __builtin_amdgcn_s_setprio(0);
      _Pragma("nounroll") while (true) {
        REREAD();
        ml = ld8<AUX_SC1>(rws, mo, 0);
#pragma unroll
        for (int k = 0; k < 8; ++k) op[k] = ld16<AUX_SC1>(rws, oo_, k * 512);
        PIN();
        unsigned mx = max(ml.x, ml.y);
#pragma unroll
        for (int k = 0; k < 8; ++k) mx = max(mx, umax4(op[k]));
        if (!__any(mx == POISON) || ++spins > SPIN_CAP) break;
        __builtin_amdgcn_s_sleep(1);
      }
      STAMPV(18, spins);
      STAMP(13);
      float M = u2f(ml.x);
      M = fmaxf(M, ror<8>(M)); M = fmaxf(M, ror<4>(M)); M = fmaxf(M, ror<2>(M)); M = fmaxf(M, ror<1>(M));
      const float e = __builtin_amdgcn_exp2f((u2f(ml.x) - M) * LOG2E);
      float Ls = e * u2f(ml.y);
      Ls += ror<8>(Ls); Ls += ror<4>(Ls); Ls += ror<2>(Ls); Ls += ror<1>(Ls);
      const float f = e * __builtin_amdgcn_rcpf(Ls);
      f32x4 t = {0.f, 0.f, 0.f, 0.f};
#define OACC(k) t += u2f(__builtin_amdgcn_mov_dpp(f2u(f), 0x150 + (k), 0xf, 0xf, true)) * __builtin_bit_cast(f32x4, op[k])
      OACC(0); OACC(1); OACC(2); OACC(3); OACC(4); OACC(5); OACC(6); OACC(7);
#pragma unroll
      for (int k = 0; k < 4; ++k) { auto sw = __builtin_amdgcn_permlane32_swap(f2u(t[k]), f2u(t[k]), false, false); t[k] = u2f(sw[0]) + u2f(sw[1]); }
      if (lane < 32) *(LAS v2u*)(L + L_OA + wl * 256 + lane * 8) = v2u{pk(t[0], t[1]), pk(t[2], t[3])};
      if (wl < 2) __builtin_amdgcn_s_setprio(2);
    }
    BAR();
    BAR();
    if (wl < 2) {
      const int j0 = lane & 7, nn = wl * 8 + (lane >> 3);
      f32x4 t = *(const LAS f32x4*)(L + L_RED + (j0 * 18 + nn) * 16);
#pragma unroll
      for (int ww = 1; ww < 4; ++ww) t += *(const LAS f32x4*)(L + L_RED + ww * RED_W + (j0 * 18 + nn) * 16);
      st16(nopoison(t), rws, WS_F + ((32 * cg + 2 * nn + (j0 >> 2)) * 32 + hd) * 64 + (j0 & 3) * 16, 0);
      STAMP(11);
    } else if (wl == 2) {
      v4u f0, f1;
      int spins = 0;
      _Pragma("nounroll") while (true) {
        REREAD();
        f0 = ld16<AUX_SC1>(rws, WS_F + cu * 2048 + lane * 16, 0);
        f1 = ld16<AUX_SC1>(rws, WS_F + cu * 2048 + lane * 16, 1024);
        if (!__any(max(umax4(f0), umax4(f1)) == POISON) || ++spins > SPIN_CAP) break;
        __builtin_amdgcn_s_sleep(1);
      }
      STAMPV(17, spins);
      STAMP(11);
      f32x4 t = __builtin_bit_cast(f32x4, f0) + __builtin_bit_cast(f32x4, f1);
#pragma unroll
      for (int k = 0; k < 4; ++k) { float y = t[k]; y += ror<4>(y); y += ror<8>(y); t[k] = xrow_sum(y); }
      const float y = (fb == 0 ? t[0] : fb == 1 ? t[1] : fb == 2 ? t[2] : t[3]) + blo(xres) + blo(b2v);
      __builtin_amdgcn_raw_buffer_store_b16((unsigned short)pk(y, y), mk(p.out, 4 * DM * 2), lane < 16 ? (fb * DM + fc) * 2 : OOB, 0, 0);
      STAMP(12);
    }
  }
  STAMP_FLUSH();
}
}

extern "C" void kernel_launch(void* const* d_in, const int* in_sizes, int n_in, void* d_out, int out_size, void* d_ws, size_t ws_size,
                              hipStream_t stream) {
  Params p;
  p.x = d_in[0]; p.kc = d_in[1]; p.vc = d_in[2]; p.wq = d_in[3]; p.wo = d_in[4];
  p.w1 = d_in[6]; p.b1 = d_in[7]; p.w2 = d_in[8]; p.b2 = d_in[9];
  p.out = d_out; p.ws = d_ws;
  hipLaunchKernelGGL(fused_decode, dim3(NWG), dim3(NTHR), 0, stream, p);
}
```

```cpp
#include <hip/hip_runtime.h>
#include <hip/hip_bf16.h>
#include <stdint.h>

#define STAMPS 0
#define NPRE 0
#define CODEPF 1
#define AUXK 2
#define AUXV 2
#define AUXW1 0
#define AUXW2 0
#define FLPOS 21

namespace {
typedef unsigned v4u __attribute__((ext_vector_type(4)));
typedef unsigned v2u __attribute__((ext_vector_type(2)));
typedef float f32x4 __attribute__((ext_vector_type(4)));
typedef short bf16x8 __attribute__((ext_vector_type(8)));
typedef __bf16 bf16x2 __attribute__((ext_vector_type(2)));
typedef float f32x2 __attribute__((ext_vector_type(2)));
typedef __amdgpu_buffer_rsrc_t rsrc_t;
#define GAS __attribute__((address_space(1)))
#define LAS __attribute__((address_space(3)))

constexpr int NWG = 256, NTHR = 512;
constexpr int DM = 1024, NQ = 4096, FF = 3584;
constexpr int WS_E = 0;
constexpr int WS_FLAG = 4096;
constexpr int WS_Q = 8192;
constexpr int WS_H = WS_Q + 4 * 32 * 8 * 512;
constexpr int WS_O = WS_H + 8 * 4 * FF * 4;
constexpr int WS_ML = WS_O + 32 * 4 * 16 * 512;
constexpr int WS_F = WS_ML + 32 * 4 * 16 * 8;
constexpr int WS_END = WS_F + 256 * 32 * 64;
constexpr int WS_STAMP = 8 << 20;
constexpr int OOB = 0x7f000000;
constexpr int AUX_NT = 2, AUX_SC1 = 16;
constexpr unsigned POISON = 0xFFFFFFFFu;
constexpr int SPIN_CAP = 1 << 14;
constexpr int L_XN = 0;
constexpr int L_Q = L_XN + 2048;
constexpr int L_M = L_Q + 2048;
constexpr int L_L = L_M + 256;
constexpr int L_RED = L_L + 256;
constexpr int RED_W = 8 * 18 * 16;
constexpr int L_ACC = L_RED + 8 * RED_W;
constexpr int L_O = L_ACC + 4 * 8192;
constexpr int O_W = 8 * 132 * 4;
constexpr int L_H = L_O + 4 * O_W;
constexpr int L_OA = L_H + 1024;
constexpr int L_END = L_OA + 1024;

struct Params {
  const void *x, *kc, *vc, *wq, *wo, *w1, *b1, *w2, *b2;
  void *out, *ws;
};

__device__ __forceinline__ rsrc_t mk(const void* p, int bytes) { return __builtin_amdgcn_make_buffer_rsrc((void*)p, 0, bytes, 0x00020000); }
template <int AUX> __device__ __forceinline__ v4u ld16(rsrc_t r, int voff, int soff) { return __builtin_amdgcn_raw_buffer_load_b128(r, voff, soff, AUX); }
template <int AUX> __device__ __forceinline__ v2u ld8(rsrc_t r, int voff, int soff) { return __builtin_amdgcn_raw_buffer_load_b64(r, voff, soff, AUX); }
__device__ __forceinline__ void st16(v4u v, rsrc_t r, int voff, int soff) { __builtin_amdgcn_raw_buffer_store_b128(v, r, voff, soff, AUX_SC1); }
__device__ __forceinline__ void st8(v2u v, rsrc_t r, int voff, int soff) { __builtin_amdgcn_raw_buffer_store_b64(v, r, voff, soff, AUX_SC1); }
__device__ __forceinline__ f32x4 mfma(v4u a, v4u b, f32x4 c) {
  return __builtin_amdgcn_mfma_f32_16x16x32_bf16(__builtin_bit_cast(bf16x8, a), __builtin_bit_cast(bf16x8, b), c, 0, 0, 0);
}
__device__ __forceinline__ float u2f(unsigned u) { return __builtin_bit_cast(float, u); }
__device__ __forceinline__ unsigned f2u(float f) { return __builtin_bit_cast(unsigned, f); }
__device__ __forceinline__ unsigned pk(float lo, float hi) {
  f32x2 v = {lo, hi};
  return __builtin_bit_cast(unsigned, __builtin_convertvector(v, bf16x2));
}
__device__ __forceinline__ float blo(unsigned u) { return u2f(u << 16); }
__device__ __forceinline__ float bhi(unsigned u) { return u2f(u & 0xffff0000u); }
__device__ __forceinline__ float rbf(float f) { return blo(pk(f, f)); }
template <int N> __device__ __forceinline__ float ror(float x) { return u2f(__builtin_amdgcn_mov_dpp(f2u(x), 0x120 + N, 0xf, 0xf, true)); }
template <int N> __device__ __forceinline__ unsigned roru(unsigned x) { return __builtin_amdgcn_mov_dpp(x, 0x120 + N, 0xf, 0xf, true); }
__device__ __forceinline__ float xrow_sum(float x) {
  auto s = __builtin_amdgcn_permlane16_swap(f2u(x), f2u(x), false, false);
  x = u2f(s[0]) + u2f(s[1]);
  auto t = __builtin_amdgcn_permlane32_swap(f2u(x), f2u(x), false, false);
  return u2f(t[0]) + u2f(t[1]);
}
__device__ __forceinline__ float xrow_max(float x) {
  auto s = __builtin_amdgcn_permlane16_swap(f2u(x), f2u(x), false, false);
  x = fmaxf(u2f(s[0]), u2f(s[1]));
  auto t = __builtin_amdgcn_permlane32_swap(f2u(x), f2u(x), false, false);
  return fmaxf(u2f(t[0]), u2f(t[1]));
}
__device__ __forceinline__ float wave_sum(float x) {
  x += ror<8>(x); x += ror<4>(x); x += ror<2>(x); x += ror<1>(x);
  return xrow_sum(x);
}
__device__ __forceinline__ unsigned umax4(v4u v) { return max(max(v.x, v.y), max(v.z, v.w)); }
__device__ __forceinline__ v4u nopoison(f32x4 v) {
  v4u u = __builtin_bit_cast(v4u, v);
  return v4u{min(u.x, POISON - 1), min(u.y, POISON - 1), min(u.z, POISON - 1), min(u.w, POISON - 1)};
}
#define PIN() __builtin_amdgcn_sched_barrier(0)
#define BAR() do { PIN(); asm volatile("s_waitcnt lgkmcnt(0)\n\ts_barrier" ::: "memory"); PIN(); } while (0)
#define REREAD() asm volatile("" ::: "memory")
__device__ __forceinline__ v4u tr8(const v4u* w, int j0) {
  const unsigned sel = (j0 & 1) ? 0x07060302u : 0x05040100u;
  const int c = j0 >> 1;
  return v4u{__builtin_amdgcn_perm(w[1][c], w[0][c], sel), __builtin_amdgcn_perm(w[3][c], w[2][c], sel),
             __builtin_amdgcn_perm(w[5][c], w[4][c], sel), __builtin_amdgcn_perm(w[7][c], w[6][c], sel)};
}
__device__ __forceinline__ void layernorm16(float (&v)[16]) {
  float s = 0.f;
#pragma unroll
  for (int i = 0; i < 16; ++i) s += v[i];
  const float mf = wave_sum(s) * (1.0f / DM);
  float ss = 0.f;
#pragma unroll
  for (int i = 0; i < 16; ++i) { const float d = v[i] - mf; ss = fmaf(d, d, ss); }
  const float var = rbf(wave_sum(ss) * (1.0f / DM));
  const float den = rbf(__builtin_amdgcn_sqrtf(rbf(var + 1e-5f)));
  const float m = rbf(mf), inv = __builtin_amdgcn_rcpf(den);
#pragma unroll
  for (int i = 0; i < 16; i += 2) {
    const unsigned d = pk(v[i] - m, v[i + 1] - m);
    const unsigned o = pk(blo(d) * inv, bhi(d) * inv);
    v[i] = blo(o); v[i + 1] = bhi(o);
  }
}

#define STAMP(i)
#define STAMPV(i, v)
#define STAMP_FLUSH()

__global__ void __launch_bounds__(NTHR, 2) fused_decode(Params p) {
  __shared__ __attribute__((aligned(16))) unsigned char smem[L_END];
  LAS unsigned char* L = (LAS unsigned char*)smem;
  const int tid = threadIdx.x, lane = tid & 63;
  const int w = __builtin_amdgcn_readfirstlane(tid >> 6);
  const int wl = w & 3;
  const bool isA = w < 4;
  const int xs = blockIdx.x & 7, ci = blockIdx.x >> 3;
  const int n16 = lane & 15, g = lane >> 4;
  const rsrc_t rws = mk(p.ws, WS_END);
  const int gidx = 2 * xs + (ci >> 4), ab = gidx >> 2, kvh = gidx & 3, sp = ci & 15;
  const int hd = 4 * xs + (ci >> 3), cg = ci & 7;
  const int cu = xs * 32 + ci;

  asm volatile("" :: "s"(p.x), "s"(p.kc), "s"(p.vc), "s"(p.wq), "s"(p.wo), "s"(p.w1), "s"(p.b1), "s"(p.w2), "s"(p.b2), "s"(p.out), "s"(p.ws));
  if (isA) __builtin_amdgcn_s_setprio(1); else __builtin_amdgcn_s_setprio(2);
  int zoff = WS_E;
  asm volatile("" : "+v"(zoff));
  const v2u gen = ld8<AUX_SC1>(rws, zoff, 0);
  v4u xr[2];
  if (!isA) {
    const rsrc_t rx = mk(p.x, 4 * DM * 2);
    const int xo = wl * 2048 + lane * 32;
    xr[0] = ld16<0>(rx, xo, 0);
    xr[1] = ld16<0>(rx, xo + 16, 0);
  }
  PIN();
  unsigned cpf = 0;
  if (isA) {
    unsigned long long pc; int len;
    asm volatile("s_getpc_b64 %0\n.Lpf_base%=:\n\ts_mov_b32 %1, .Lfd_code_end-.Lpf_base%=" : "=s"(pc), "=s"(len));
    cpf = __builtin_amdgcn_raw_buffer_load_b32(mk((const void*)pc, len), tid * 128, 0, 0);
  }
  PIN();
  if (w == 7) {
    const v4u pz = {POISON, POISON, POISON, POISON};
    const int hf = lane >> 5, c4 = lane & 31;
#pragma unroll
    for (int i = 0; i < 2; ++i) st16(pz, rws, WS_Q + (((2 * i + hf) * 32 + ci) * 8 + xs) * 512 + c4 * 16, 0);
#pragma unroll
    for (int i = 0; i < 2; ++i) st16(pz, rws, ci < 28 ? WS_H + ((xs * 4 + 2 * i + hf) * FF + ci * 128) * 4 + c4 * 16 : OOB, 0);
#pragma unroll
    for (int i = 0; i < 4; ++i) st16(pz, rws, WS_O + (((8 * kvh + 2 * i + hf) * 4 + ab) * 16 + sp) * 512 + c4 * 16, 0);
    st8(v2u{POISON, POISON}, rws, lane < 8 ? WS_ML + (((8 * kvh + lane) * 4 + ab) * 16 + sp) * 8 : OOB, 0);
#pragma unroll
    for (int i = 0; i < 2; ++i) st16(pz, rws, WS_F + ((32 * cg + 16 * i + (lane >> 2)) * 32 + hd) * 64 + (lane & 3) * 16, 0);
  }
  PIN();
  v4u wv[8];
  {
    const int ldw = isA ? NQ * 2 : FF * 2;
    const rsrc_t rw = mk(isA ? p.wq : p.w1, DM * ldw);
    const int wo_ = (isA || ci < 28) ? (128 * xs + 32 * wl + 8 * g) * ldw + ci * 256 + n16 * 16 : OOB;
#pragma unroll
    for (int j = 0; j < 8; ++j) wv[j] = ld16<AUXW1>(rw, wo_, j * ldw);
  }
  PIN();
  STAMP(14);
  BAR();
#define MAKE_TAG() v2u gen_ = gen; asm volatile("" : "+v"(gen_)); \
  const unsigned long long e1 = (((unsigned long long)gen_.y << 32) | gen_.x) + 1ull, tag64 = e1 * 0x9E3779B97F4A7C15ull; \
  const unsigned tag0 = (unsigned)tag64, tag1 = (unsigned)(tag64 >> 32)

#define P1_MFMA() do { \
    const v4u act = *(const LAS v4u*)(L + L_XN + (isA ? 0 : 1024) + (n16 & 3) * 256 + (32 * wl + 8 * g) * 2); \
    f32x4 d_[8]; \
    _Pragma("unroll") for (int j0 = 0; j0 < 8; ++j0) d_[j0] = mfma(act, tr8(wv, j0), f32x4{0.f, 0.f, 0.f, 0.f}); \
    if (lane < 16) { _Pragma("unroll") for (int j0 = 0; j0 < 8; ++j0) *(LAS f32x4*)(L + L_RED + w * RED_W + (j0 * 17 + lane) * 16) = d_[j0]; } \
    } while (0)

  if (!isA) {
    float v[16];
#pragma unroll
    for (int i = 0; i < 8; ++i) { v[2 * i] = blo(xr[i >> 2][i & 3]); v[2 * i + 1] = bhi(xr[i >> 2][i & 3]); }
    if (w == 7) {
      MAKE_TAG();
      asm volatile("s_waitcnt vmcnt(8)" ::: "memory");
      st8(v2u{tag0, tag1}, rws, lane == 0 ? WS_FLAG + cu * 8 : OOB, 0);
    }
    asm volatile("" : "+v"(v[0]), "+v"(v[15]));
    STAMP(1);
#pragma unroll
    for (int pass = 0; pass < 2; ++pass) {
      layernorm16(v);
      if ((lane >> 3) == xs) {
        LAS v4u* d = (LAS v4u*)(L + L_XN + pass * 1024 + wl * 256 + (lane & 7) * 32);
        d[0] = v4u{pk(v[0], v[1]), pk(v[2], v[3]), pk(v[4], v[5]), pk(v[6], v[7])};
        d[1] = v4u{pk(v[8], v[9]), pk(v[10], v[11]), pk(v[12], v[13]), pk(v[14], v[15])};
      }
    }
    STAMP(2);
    BAR();
    STAMP(4);
    P1_MFMA();
    STAMP(5);
    BAR();
    const int mat = wl >> 1, b = lane & 3, jh = (lane >> 2) & 1, nn = (wl & 1) * 8 + (lane >> 3);
    f32x4 sum = {0.f, 0.f, 0.f, 0.f};
#pragma unroll
    for (int ww = 0; ww < 4; ++ww)
#pragma unroll
      for (int k = 0; k < 4; ++k) sum[k] += *(const LAS float*)(L + L_RED + (mat * 4 + ww) * RED_W + ((4 * jh + k) * 17 + nn) * 16 + b * 4);
    const int co = (8 * nn + 4 * jh) * 4;
    const int off = mat == 0 ? WS_Q + ((b * 32 + ci) * 8 + xs) * 512 + co : (ci < 28 ? WS_H + ((xs * 4 + b) * FF + ci * 128) * 4 + co : OOB);
    st16(nopoison(sum), rws, off, 0);
    PIN();
    __builtin_amdgcn_s_setprio(0);
    STAMP(3);
  }
  PIN();
  const size_t kvbase = ((size_t)gidx * 16 + sp) * 262144;
  const rsrc_t rk = mk((const char*)p.kc + kvbase, 262144), rv = mk((const char*)p.vc + kvbase, 262144);
  const int koff = (w >> 2) * 131072 + wl * 4096 + ((lane >> 2) & 3) * 256 + g * 64 + (lane & 3) * 16;
  const int voff = (w >> 2) * 131072 + wl * 4096 + lane * 16;
  v4u r[32];
#define KLD(i) do { r[i] = ld16<AUXK>(rk, koff, ((i) >> 2) * 16384 + ((i) & 3) * 1024); PIN(); } while (0)
#define VLD(i) do { r[i] = ld16<AUXV>(rv, voff, ((i) >> 2) * 16384 + ((i) & 3) * 1024); PIN(); } while (0)
#pragma unroll
  for (int i = 0; i < 16; ++i) KLD(i);
  PIN();
  if (isA) {
    STAMP(1);
    BAR();
    P1_MFMA();
    asm volatile("" :: "v"(cpf));
    BAR();
    STAMP(2);
  }
  PIN();
#pragma unroll
  for (int i = 16; i < FLPOS; ++i) KLD(i);
  PIN();
  v4u fl[2];
  if (isA) {
    fl[0] = ld16<AUX_SC1>(rws, WS_FLAG + lane * 16, 0);
    fl[1] = ld16<AUX_SC1>(rws, WS_FLAG + lane * 16, 1024);
  }
  PIN();
#pragma unroll
  for (int i = FLPOS; i < 32; ++i) KLD(i);
  PIN();
  if (isA) {
    STAMP(3);
    MAKE_TAG();
    int spins = 0;
#define FLAGS_BAD(a, b) __any((((a).x ^ tag0) | ((a).y ^ tag1) | ((a).z ^ tag0) | ((a).w ^ tag1) | ((b).x ^ tag0) | ((b).y ^ tag1) | ((b).z ^ tag0) | ((b).w ^ tag1)) != 0u)
    if (FLAGS_BAD(fl[0], fl[1])) {
      _Pragma("nounroll") while (true) {
        REREAD();
        __builtin_amdgcn_s_sleep(4);
        const v4u f0 = ld16<AUX_SC1>(rws, WS_FLAG + lane * 16, 0), f1 = ld16<AUX_SC1>(rws, WS_FLAG + lane * 16, 1024);
        if (++spins > SPIN_CAP || !FLAGS_BAD(f0, f1)) break;
      }
    }
    REREAD();
    STAMPV(18, spins);
    STAMP(4);
    if (blockIdx.x == 0 && w == 0) {
      st8(v2u{(unsigned)e1, (unsigned)(e1 >> 32)}, rws, lane == 0 ? WS_E : OOB, 0);
    }
    const int qo = WS_Q + ((ab * 32 + 8 * kvh + 2 * wl + (lane >> 5)) * 8) * 512 + (lane & 31) * 16;
    const int hc = lane & 7, hbt = (lane >> 3) & 3, hb = 112 * hd + 28 * wl + 4 * hc;
    const int ho = hc < 7 ? WS_H + ((16 * (lane >> 5) + hbt) * FF + hb) * 4 : OOB;
    const v2u bia = ld8<0>(mk(p.b1, FF * 2), hc < 7 ? hb * 2 : OOB, 0);
    v4u qp[8], hp[4];
    spins = 0;
    _Pragma("nounroll") while (true) {
      REREAD();
#pragma unroll
      for (int s = 0; s < 8; ++s) qp[s] = ld16<AUX_SC1>(rws, qo, s * 512);
#pragma unroll
      for (int s = 0; s < 4; ++s) hp[s] = ld16<AUX_SC1>(rws, ho, s * 4 * FF * 4);
      PIN();
      unsigned mx = 0;
#pragma unroll
      for (int s = 0; s < 8; ++s) mx = max(mx, umax4(qp[s]));
#pragma unroll
      for (int s = 0; s < 4; ++s) mx = max(mx, umax4(hp[s]));
      if (!__any(mx == POISON) || ++spins > SPIN_CAP) break;
      __builtin_amdgcn_s_sleep(8);
    }
    STAMPV(19, spins);
    STAMP(5);
    f32x4 qs = __builtin_bit_cast(f32x4, qp[0]);
#pragma unroll
    for (int s = 1; s < 8; ++s) qs += __builtin_bit_cast(f32x4, qp[s]);
    *(LAS v2u*)(L + L_Q + (2 * wl + (lane >> 5)) * 256 + (lane & 31) * 8) = v2u{pk(qs[0], qs[1]), pk(qs[2], qs[3])};
    float hv[4];
#pragma unroll
    for (int j = 0; j < 4; ++j) {
      const float t0 = (u2f(hp[0][j]) + u2f(hp[1][j])) + (u2f(hp[2][j]) + u2f(hp[3][j]));
      auto sw = __builtin_amdgcn_permlane32_swap(f2u(t0), f2u(t0), false, false);
      const float t = u2f(sw[0]) + u2f(sw[1]);
      const float u = rbf(rbf(t) + ((j & 1) ? bhi(bia[j >> 1]) : blo(bia[j >> 1])));
      const float z2 = u * fmaf(u * u, 0.044715f, 1.0f) * (-2.0f * 0.7978845608028654f * 1.4426950408889634f);
      hv[j] = u * __builtin_amdgcn_rcpf(1.0f + __builtin_amdgcn_exp2f(z2));
    }
    if (lane < 32) *(LAS v2u*)(L + L_H + wl * 256 + hbt * 64 + hc * 8) = v2u{pk(hv[0], hv[1]), pk(hv[2], hv[3])};
  }
  STAMP(6);
  BAR();
  STAMP(15);
  const int hl = n16 & 7, wb = n16 >> 3;
  const v4u qb0 = *(const LAS v4u*)(L + L_Q + hl * 256 + (4 * g + wb) * 16);
  const v4u qb1 = *(const LAS v4u*)(L + L_Q + hl * 256 + (4 * g + 2 + wb) * 16);
  float s[16];
#pragma unroll
  for (int u = 0; u < 8; ++u) {
    float t0[4], t1[4];
#pragma unroll
    for (int k = 0; k < 4; ++k) {
      const f32x4 d0 = mfma(r[4 * u + k], qb0, f32x4{0.f, 0.f, 0.f, 0.f});
      const f32x4 d1 = mfma(r[4 * u + k], qb1, f32x4{0.f, 0.f, 0.f, 0.f});
      t0[k] = d0[0] + d1[2];
      t1[k] = d0[1] + d1[3];
    }
#pragma unroll
    for (int k = 0; k < 4; k += 2) {
      const float xx = wb ? t1[k + 1] : t0[k], yy = wb ? t1[k] : t0[k + 1];
      s[2 * u + (k >> 1)] = xx + ror<8>(yy);
    }
    PIN();
#pragma unroll
    for (int k = 0; k < 4; ++k) VLD(4 * u + k);
    PIN();
  }
  STAMP(7);
  float m = -INFINITY;
#pragma unroll
  for (int i = 0; i < 16; i += 2) {
    const unsigned a = pk(s[i], s[i + 1]);
    const unsigned c = pk(blo(a) * 0.08838834764831845f, bhi(a) * 0.08838834764831845f);
    s[i] = blo(c); s[i + 1] = bhi(c);
    m = fmaxf(m, fmaxf(s[i], s[i + 1]));
  }
  m = fmaxf(m, ror<8>(m));
  m = xrow_max(m);
  if (lane < 8) *(LAS float*)(L + L_M + (w * 8 + lane) * 4) = m;
  f32x4 acc[8];
  float lsum = 0.f;
#pragma unroll
  for (int j = 0; j < 8; ++j) acc[j] = f32x4{0.f, 0.f, 0.f, 0.f};
  constexpr float LOG2E = 1.4426950408889634f;
  if (!isA) {
    __builtin_amdgcn_s_setprio(2);
    BAR();
    const float mA = *(const LAS float*)(L + L_M + (wl * 8 + hl) * 4);
#pragma unroll
    for (int ww = 0; ww < 8; ++ww) m = fmaxf(m, *(const LAS float*)(L + L_M + (ww * 8 + hl) * 4));
    const float f = __builtin_amdgcn_exp2f((mA - m) * LOG2E);
    lsum = f * *(const LAS float*)(L + L_L + (wl * 8 + hl) * 4);
#pragma unroll
    for (int j = 0; j < 8; ++j) acc[j] = f * *(const LAS f32x4*)(L + L_ACC + wl * 8192 + j * 1024 + lane * 16);
  }
  PIN();
  unsigned pb[16];
  {
    float l = 0.f;
    const unsigned msk = wb ? 0xffff0000u : 0x0000ffffu;
#pragma unroll
    for (int i = 0; i < 16; ++i) {
      const float e = __builtin_amdgcn_exp2f((s[i] - m) * LOG2E);
      l += e;
      const unsigned t = pk(e, e) & msk;
      pb[i] = t | roru<8>(t);
    }
    l += ror<8>(l);
    lsum += xrow_sum(l);
    if (lane < 8) *(LAS float*)(L + L_L + (w * 8 + lane) * 4) = lsum;
  }
  PIN();
#pragma unroll
  for (int T = 0; T < 4; ++T) {
    const v4u pop = {pb[4 * T], pb[4 * T + 1], pb[4 * T + 2], pb[4 * T + 3]};
#pragma unroll
    for (int j0 = 0; j0 < 8; ++j0) acc[j0] = mfma(tr8(&r[8 * T], j0), pop, acc[j0]);
    PIN();
  }
  STAMP(8);
  if (isA) {
#pragma unroll
    for (int j = 0; j < 8; ++j) *(LAS f32x4*)(L + L_ACC + wl * 8192 + j * 1024 + lane * 16) = acc[j];
    BAR();
    const rsrc_t r2 = mk(p.w2, FF * DM * 2), ro = mk(p.wo, NQ * DM * 2);
    const int o2 = (112 * hd + 28 * wl + 8 * g) * 2048 + cg * 256 + n16 * 16;
    const int o2b = g < 3 ? o2 : OOB;
    const int oo = (128 * hd + 32 * wl + 8 * g) * 2048 + cg * 256 + n16 * 16;
    v4u tw[16];
#define TLD(j) do { tw[j] = (j) < 8 ? ld16<AUXW2>(r2, (j) < 4 ? o2 : o2b, (j) * 2048) : ld16<AUXW2>(ro, oo, ((j) - 8) * 2048); PIN(); } while (0)
#pragma unroll
    for (int j = 0; j < NPRE; ++j) TLD(j);
    STAMP(9);
    BAR();
#pragma unroll
    for (int j = NPRE; j < 16; ++j) TLD(j);
    const v4u hact = *(const LAS v4u*)(L + L_H + wl * 256 + (n16 & 3) * 64 + g * 16);
    f32x4 d[8];
#pragma unroll
    for (int j0 = 0; j0 < 8; ++j0) d[j0] = mfma(hact, tr8(tw, j0), f32x4{0.f, 0.f, 0.f, 0.f});
    v4u wt[8];
#pragma unroll
    for (int j0 = 0; j0 < 8; ++j0) { wt[j0] = tr8(tw + 8, j0); asm volatile("" : "+v"(wt[j0])); }
    STAMP(10);
    BAR();
    STAMP(11);
    const v4u oact = *(const LAS v4u*)(L + L_OA + (n16 & 3) * 256 + (32 * wl + 8 * g) * 2);
#pragma unroll
    for (int j0 = 0; j0 < 8; ++j0) d[j0] = mfma(oact, wt[j0], d[j0]);
    if (lane < 16) {
#pragma unroll
      for (int j0 = 0; j0 < 8; ++j0) *(LAS f32x4*)(L + L_RED + wl * RED_W + (j0 * 18 + lane) * 16) = d[j0];
    }
    BAR();
    STAMP(12);
    asm volatile(".Lfd_code_end:");
  } else {
#pragma unroll
    for (int i = 0; i < 4; ++i) {
      const f32x4 t = {wb ? acc[4][i] : acc[0][i], wb ? acc[5][i] : acc[1][i], wb ? acc[6][i] : acc[2][i], wb ? acc[7][i] : acc[3][i]};
      *(LAS f32x4*)(L + L_O + wl * O_W + (hl * 132 + 8 * (4 * g + i) + 4 * wb) * 4) = t;
    }
    const int fb = (lane >> 2) & 3, fc = 4 * cu + (lane & 3);
    const unsigned xres = __builtin_amdgcn_raw_buffer_load_b16(mk(p.x, 4 * DM * 2), (fb * DM + fc) * 2, 0, 0);
    const unsigned b2v = __builtin_amdgcn_raw_buffer_load_b16(mk(p.b2, DM * 2), fc * 2, 0, 0);
    BAR();
    STAMP(9);
    {
      const int hh = 2 * wl + (lane >> 5), c4 = lane & 31;
      f32x4 t = *(const LAS f32x4*)(L + L_O + (hh * 132 + 4 * c4) * 4);
#pragma unroll
      for (int ww = 1; ww < 4; ++ww) t += *(const LAS f32x4*)(L + L_O + ww * O_W + (hh * 132 + 4 * c4) * 4);
      st16(nopoison(t), rws, WS_O + (((8 * kvh + hh) * 4 + ab) * 16 + sp) * 512 + c4 * 16, 0);
      if (w == 7) {
        float l = 0.f;
#pragma unroll
        for (int ww = 4; ww < 8; ++ww) l += *(const LAS float*)(L + L_L + (ww * 8 + hl) * 4);
        st8(v2u{min(f2u(m), POISON - 1), min(f2u(l), POISON - 1)}, rws, lane < 8 ? WS_ML + (((8 * kvh + lane) * 4 + ab) * 16 + sp) * 8 : OOB, 0);
      }
    }
    STAMP(10);
    {
      const int hf = lane >> 5;
      const int rec = (hd * 4 + wl) * 16;
      const int oo_ = WS_O + (rec + 8 * hf) * 512 + (lane & 31) * 16, mo = WS_ML + (rec + ((n16 + 8 * hf) & 15)) * 8;
      v4u op[8];
      v2u ml;
      int spins = 0;
      __builtin_amdgcn_s_setprio(0);
      _Pragma("nounroll") while (true) {
        REREAD();
        ml = ld8<AUX_SC1>(rws, mo, 0);
#pragma unroll
        for (int k = 0; k < 8; ++k) op[k] = ld16<AUX_SC1>(rws, oo_, k * 512);
        PIN();
        unsigned mx = max(ml.x, ml.y);
#pragma unroll
        for (int k = 0; k < 8; ++k) mx = max(mx, umax4(op[k]));
        if (!__any(mx == POISON) || ++spins > SPIN_CAP) break;
        __builtin_amdgcn_s_sleep(1);
      }
      STAMPV(18, spins);
      STAMP(13);
      float M = u2f(ml.x);
      M = fmaxf(M, ror<8>(M)); M = fmaxf(M, ror<4>(M)); M = fmaxf(M, ror<2>(M)); M = fmaxf(M, ror<1>(M));
      const float e = __builtin_amdgcn_exp2f((u2f(ml.x) - M) * LOG2E);
      float Ls = e * u2f(ml.y);
      Ls += ror<8>(Ls); Ls += ror<4>(Ls); Ls += ror<2>(Ls); Ls += ror<1>(Ls);
      const float f = e * __builtin_amdgcn_rcpf(Ls);
      f32x4 t = {0.f, 0.f, 0.f, 0.f};
#define OACC(k) t += u2f(__builtin_amdgcn_mov_dpp(f2u(f), 0x150 + (k), 0xf, 0xf, true)) * __builtin_bit_cast(f32x4, op[k])
      OACC(0); OACC(1); OACC(2); OACC(3); OACC(4); OACC(5); OACC(6); OACC(7);
#pragma unroll
      for (int k = 0; k < 4; ++k) { auto sw = __builtin_amdgcn_permlane32_swap(f2u(t[k]), f2u(t[k]), false, false); t[k] = u2f(sw[0]) + u2f(sw[1]); }
      if (lane < 32) *(LAS v2u*)(L + L_OA + wl * 256 + lane * 8) = v2u{pk(t[0], t[1]), pk(t[2], t[3])};
      if (wl < 2) __builtin_amdgcn_s_setprio(2);
    }
    BAR();
    BAR();
    if (wl < 2) {
      const int j0 = lane & 7, nn = wl * 8 + (lane >> 3);
      f32x4 t = *(const LAS f32x4*)(L + L_RED + (j0 * 18 + nn) * 16);
#pragma unroll
      for (int ww = 1; ww < 4; ++ww) t += *(const LAS f32x4*)(L + L_RED + ww * RED_W + (j0 * 18 + nn) * 16);
      st16(nopoison(t), rws, WS_F + ((32 * cg + 2 * nn + (j0 >> 2)) * 32 + hd) * 64 + (j0 & 3) * 16, 0);
      STAMP(11);
    } else if (wl == 2) {
      v4u f0, f1;
      int spins = 0;
      _Pragma("nounroll") while (true) {
        REREAD();
        f0 = ld16<AUX_SC1>(rws, WS_F + cu * 2048 + lane * 16, 0);
        f1 = ld16<AUX_SC1>(rws, WS_F + cu * 2048 + lane * 16, 1024);
        if (!__any(max(umax4(f0), umax4(f1)) == POISON) || ++spins > SPIN_CAP) break;
        __builtin_amdgcn_s_sleep(1);
      }
      STAMPV(17, spins);
      STAMP(11);
      f32x4 t = __builtin_bit_cast(f32x4, f0) + __builtin_bit_cast(f32x4, f1);
#pragma unroll
      for (int k = 0; k < 4; ++k) { float y = t[k]; y += ror<4>(y); y += ror<8>(y); t[k] = xrow_sum(y); }
      const float y = (fb == 0 ? t[0] : fb == 1 ? t[1] : fb == 2 ? t[2] : t[3]) + blo(xres) + blo(b2v);
      __builtin_amdgcn_raw_buffer_store_b16((unsigned short)pk(y, y), mk(p.out, 4 * DM * 2), lane < 16 ? (fb * DM + fc) * 2 : OOB, 0, 0);
      STAMP(12);
    }
  }
  STAMP_FLUSH();
}
}

extern "C" void kernel_launch(void* const* d_in, const int* in_sizes, int n_in, void* d_out, int out_size, void* d_ws, size_t ws_size,
                              hipStream_t stream) {
  Params p;
  p.x = d_in[0]; p.kc = d_in[1]; p.vc = d_in[2]; p.wq = d_in[3]; p.wo = d_in[4];
  p.w1 = d_in[6]; p.b1 = d_in[7]; p.w2 = d_in[8]; p.b2 = d_in[9];
  p.out = d_out; p.ws = d_ws;
  hipLaunchKernelGGL(fused_decode, dim3(NWG), dim3(NTHR), 0, stream, p);
}
```

```cpp
#define STAMPS 1
#define DIAG 0
#define DIAGW 6
#define DIAGB -1
#include <hip/hip_runtime.h>
#include <hip/hip_bf16.h>
#include <stdint.h>

#define NPRE 0
#define CODEPF 1
#define AUXK 2
#define AUXV 2
#define AUXW1 2
#define AUXW2 2
#define FLPOS 21

namespace {
typedef unsigned v4u __attribute__((ext_vector_type(4)));
typedef unsigned v2u __attribute__((ext_vector_type(2)));
typedef float f32x4 __attribute__((ext_vector_type(4)));
typedef short bf16x8 __attribute__((ext_vector_type(8)));
typedef __bf16 bf16x2 __attribute__((ext_vector_type(2)));
typedef float f32x2 __attribute__((ext_vector_type(2)));
typedef __amdgpu_buffer_rsrc_t rsrc_t;
#define GAS __attribute__((address_space(1)))
#define LAS __attribute__((address_space(3)))

constexpr int NWG = 256, NTHR = 512;
constexpr int DM = 1024, NQ = 4096, FF = 3584;
constexpr int WS_E = 0;
constexpr int WS_FLAG = 4096;
constexpr int WS_Q = 8192;
constexpr int WS_H = WS_Q + 4 * 32 * 8 * 512;
constexpr int WS_O = WS_H + 8 * 4 * FF * 4;
constexpr int WS_ML = WS_O + 32 * 4 * 16 * 512;
constexpr int WS_F = WS_ML + 32 * 4 * 16 * 8;
constexpr int WS_END = WS_F + 256 * 32 * 64;
constexpr int WS_STAMP = 8 << 20;
constexpr int OOB = 0x7f000000;
constexpr int AUX_NT = 2, AUX_SC1 = 16;
constexpr unsigned POISON = 0xFFFFFFFFu;
constexpr int SPIN_CAP = 1 << 14;
constexpr int L_XN = 0;
constexpr int L_Q = L_XN + 2048;
constexpr int L_M = L_Q + 2048;
constexpr int L_L = L_M + 256;
constexpr int L_RED = L_L + 256;
constexpr int RED_W = 8 * 18 * 16;
constexpr int L_ACC = L_RED + 8 * RED_W;
constexpr int L_O = L_ACC + 4 * 8192;
constexpr int O_W = 8 * 132 * 4;
constexpr int L_H = L_O + 4 * O_W;
constexpr int L_OA = L_H + 1024;
constexpr int L_END = L_OA + 1024;

struct Params {
  const void *x, *kc, *vc, *wq, *wo, *w1, *b1, *w2, *b2;
  void *out, *ws;
};

__device__ __forceinline__ rsrc_t mk(const void* p, int bytes) { return __builtin_amdgcn_make_buffer_rsrc((void*)p, 0, bytes, 0x00020000); }
template <int AUX> __device__ __forceinline__ v4u ld16(rsrc_t r, int voff, int soff) { return __builtin_amdgcn_raw_buffer_load_b128(r, voff, soff, AUX); }
template <int AUX> __device__ __forceinline__ v2u ld8(rsrc_t r, int voff, int soff) { return __builtin_amdgcn_raw_buffer_load_b64(r, voff, soff, AUX); }
__device__ __forceinline__ void st16(v4u v, rsrc_t r, int voff, int soff) { __builtin_amdgcn_raw_buffer_store_b128(v, r, voff, soff, AUX_SC1); }
__device__ __forceinline__ void st8(v2u v, rsrc_t r, int voff, int soff) { __builtin_amdgcn_raw_buffer_store_b64(v, r, voff, soff, AUX_SC1); }
__device__ __forceinline__ f32x4 mfma(v4u a, v4u b, f32x4 c) {
  return __builtin_amdgcn_mfma_f32_16x16x32_bf16(__builtin_bit_cast(bf16x8, a), __builtin_bit_cast(bf16x8, b), c, 0, 0, 0);
}
__device__ __forceinline__ float u2f(unsigned u) { return __builtin_bit_cast(float, u); }
__device__ __forceinline__ unsigned f2u(float f) { return __builtin_bit_cast(unsigned, f); }
__device__ __forceinline__ unsigned pk(float lo, float hi) {
  f32x2 v = {lo, hi};
  return __builtin_bit_cast(unsigned, __builtin_convertvector(v, bf16x2));
}
__device__ __forceinline__ float blo(unsigned u) { return u2f(u << 16); }
__device__ __forceinline__ float bhi(unsigned u) { return u2f(u & 0xffff0000u); }
__device__ __forceinline__ float rbf(float f) { return blo(pk(f, f)); }
template <int N> __device__ __forceinline__ float ror(float x) { return u2f(__builtin_amdgcn_mov_dpp(f2u(x), 0x120 + N, 0xf, 0xf, true)); }
template <int N> __device__ __forceinline__ unsigned roru(unsigned x) { return __builtin_amdgcn_mov_dpp(x, 0x120 + N, 0xf, 0xf, true); }
__device__ __forceinline__ float xrow_sum(float x) {
  auto s = __builtin_amdgcn_permlane16_swap(f2u(x), f2u(x), false, false);
  x = u2f(s[0]) + u2f(s[1]);
  auto t = __builtin_amdgcn_permlane32_swap(f2u(x), f2u(x), false, false);
  return u2f(t[0]) + u2f(t[1]);
}
__device__ __forceinline__ float xrow_max(float x) {
  auto s = __builtin_amdgcn_permlane16_swap(f2u(x), f2u(x), false, false);
  x = fmaxf(u2f(s[0]), u2f(s[1]));
  auto t = __builtin_amdgcn_permlane32_swap(f2u(x), f2u(x), false, false);
  return fmaxf(u2f(t[0]), u2f(t[1]));
}
__device__ __forceinline__ float wave_sum(float x) {
  x += ror<8>(x); x += ror<4>(x); x += ror<2>(x); x += ror<1>(x);
  return xrow_sum(x);
}
__device__ __forceinline__ unsigned umax4(v4u v) { return max(max(v.x, v.y), max(v.z, v.w)); }
__device__ __forceinline__ v4u nopoison(f32x4 v) {
  v4u u = __builtin_bit_cast(v4u, v);
  return v4u{min(u.x, POISON - 1), min(u.y, POISON - 1), min(u.z, POISON - 1), min(u.w, POISON - 1)};
}
#define PIN() __builtin_amdgcn_sched_barrier(0)
#define BAR() do { PIN(); asm volatile("s_waitcnt lgkmcnt(0)\n\ts_barrier" ::: "memory"); PIN(); } while (0)
#define REREAD() asm volatile("" ::: "memory")
__device__ __forceinline__ v4u tr8(const v4u* w, int j0) {
  const unsigned sel = (j0 & 1) ? 0x07060302u : 0x05040100u;
  const int c = j0 >> 1;
  return v4u{__builtin_amdgcn_perm(w[1][c], w[0][c], sel), __builtin_amdgcn_perm(w[3][c], w[2][c], sel),
             __builtin_amdgcn_perm(w[5][c], w[4][c], sel), __builtin_amdgcn_perm(w[7][c], w[6][c], sel)};
}
__device__ __forceinline__ void layernorm16(float (&v)[16]) {
  float s = 0.f;
#pragma unroll
  for (int i = 0; i < 16; ++i) s += v[i];
  const float mf = wave_sum(s) * (1.0f / DM);
  float ss = 0.f;
#pragma unroll
  for (int i = 0; i < 16; ++i) { const float d = v[i] - mf; ss = fmaf(d, d, ss); }
  const float var = rbf(wave_sum(ss) * (1.0f / DM));
  const float den = rbf(__builtin_amdgcn_sqrtf(rbf(var + 1e-5f)));
  const float m = rbf(mf), inv = __builtin_amdgcn_rcpf(den);
#pragma unroll
  for (int i = 0; i < 16; i += 2) {
    const unsigned d = pk(v[i] - m, v[i + 1] - m);
    const unsigned o = pk(blo(d) * inv, bhi(d) * inv);
    v[i] = blo(o); v[i + 1] = bhi(o);
  }
}

#define STAMP(i) do { PIN(); ts_[i] = (unsigned)__builtin_amdgcn_s_memrealtime(); PIN(); } while (0)
#define STAMPV(i, v) do { ts_[i] = (unsigned)(v); } while (0)
#define STAMP_FLUSH() do { if (lane == 0) { _Pragma("unroll") for (int i_ = 0; i_ < 20; ++i_) ((GAS unsigned long long*)((GAS char*)p.ws + WS_STAMP))[(blockIdx.x * 8 + w) * 20 + i_] = ts_[i_]; } } while (0)
#undef STAMP_FLUSH
#define STAMP_FLUSH() do { if (w == DIAGW && (DIAGB < 0 || (int)blockIdx.x == DIAGB)) { const unsigned lim_ = 6000u + 4u * (ts_[DIAG] - ts_[0]); \
    while ((unsigned)__builtin_amdgcn_s_memrealtime() - ts_[0] < lim_ && lim_ < 100000u) __builtin_amdgcn_s_sleep(1); } } while (0)

__global__ void __launch_bounds__(NTHR, 2) fused_decode(Params p) {
  __shared__ __attribute__((aligned(16))) unsigned char smem[L_END];
  LAS unsigned char* L = (LAS unsigned char*)smem;
  const int tid = threadIdx.x, lane = tid & 63;
  const int w = __builtin_amdgcn_readfirstlane(tid >> 6);
  const int wl = w & 3;
  const bool isA = w < 4;
  const int xs = blockIdx.x & 7, ci = blockIdx.x >> 3;
  const int n16 = lane & 15, g = lane >> 4;
  const rsrc_t rws = mk(p.ws, WS_END);
  unsigned ts_[20] = {0, 0, 0, 0, 0, 0, 0, 0, 0, 0, 0, 0, 0, 0, 0, 0, 0, 0, 0, 0};
  STAMP(0);
  const int gidx = 2 * xs + (ci >> 4), ab = gidx >> 2, kvh = gidx & 3, sp = ci & 15;
  const int hd = 4 * xs + (ci >> 3), cg = ci & 7;
  const int cu = xs * 32 + ci;

  asm volatile("" :: "s"(p.x), "s"(p.kc), "s"(p.vc), "s"(p.wq), "s"(p.wo), "s"(p.w1), "s"(p.b1), "s"(p.w2), "s"(p.b2), "s"(p.out), "s"(p.ws));
  if (isA) __builtin_amdgcn_s_setprio(1); else __builtin_amdgcn_s_setprio(2);
  int zoff = WS_E;
  asm volatile("" : "+v"(zoff));
  const v2u gen = ld8<AUX_SC1>(rws, zoff, 0);
  v4u xr[2];
  if (!isA) {
    const rsrc_t rx = mk(p.x, 4 * DM * 2);
    const int xo = wl * 2048 + lane * 32;
    xr[0] = ld16<0>(rx, xo, 0);
    xr[1] = ld16<0>(rx, xo + 16, 0);
  }
  PIN();
  unsigned cpf = 0;
  if (isA) {
    unsigned long long pc; int len;
    asm volatile("s_getpc_b64 %0\n.Lpf_base%=:\n\ts_mov_b32 %1, .Lfd_code_end-.Lpf_base%=" : "=s"(pc), "=s"(len));
    cpf = __builtin_amdgcn_raw_buffer_load_b32(mk((const void*)pc, len), tid * 128, 0, 0);
  }
  PIN();
  if (w == 7) {
    const v4u pz = {POISON, POISON, POISON, POISON};
    const int hf = lane >> 5, c4 = lane & 31;
#pragma unroll
    for (int i = 0; i < 2; ++i) st16(pz, rws, WS_Q + (((2 * i + hf) * 32 + ci) * 8 + xs) * 512 + c4 * 16, 0);
#pragma unroll
    for (int i = 0; i < 2; ++i) st16(pz, rws, ci < 28 ? WS_H + ((xs * 4 + 2 * i + hf) * FF + ci * 128) * 4 + c4 * 16 : OOB, 0);
#pragma unroll
    for (int i = 0; i < 4; ++i) st16(pz, rws, WS_O + (((8 * kvh + 2 * i + hf) * 4 + ab) * 16 + sp) * 512 + c4 * 16, 0);
    st8(v2u{POISON, POISON}, rws, lane < 8 ? WS_ML + (((8 * kvh + lane) * 4 + ab) * 16 + sp) * 8 : OOB, 0);
#pragma unroll
    for (int i = 0; i < 2; ++i) st16(pz, rws, WS_F + ((32 * cg + 16 * i + (lane >> 2)) * 32 + hd) * 64 + (lane & 3) * 16, 0);
  }
  PIN();
  v4u wv[8];
  {
    const int ldw = isA ? NQ * 2 : FF * 2;
    const rsrc_t rw = mk(isA ? p.wq : p.w1, DM * ldw);
    const int wo_ = (isA || ci < 28) ? (128 * xs + 32 * wl + 8 * g) * ldw + ci * 256 + n16 * 16 : OOB;
#pragma unroll
    for (int j = 0; j < 8; ++j) wv[j] = ld16<AUXW1>(rw, wo_, j * ldw);
  }
  PIN();
  STAMP(14);
  BAR();
#define MAKE_TAG() v2u gen_ = gen; asm volatile("" : "+v"(gen_)); \
  const unsigned long long e1 = (((unsigned long long)gen_.y << 32) | gen_.x) + 1ull, tag64 = e1 * 0x9E3779B97F4A7C15ull; \
  const unsigned tag0 = (unsigned)tag64, tag1 = (unsigned)(tag64 >> 32)

#define P1_MFMA() do { \
    const v4u act = *(const LAS v4u*)(L + L_XN + (isA ? 0 : 1024) + (n16 & 3) * 256 + (32 * wl + 8 * g) * 2); \
    f32x4 d_[8]; \
    _Pragma("unroll") for (int j0 = 0; j0 < 8; ++j0) d_[j0] = mfma(act, tr8(wv, j0), f32x4{0.f, 0.f, 0.f, 0.f}); \
    if (lane < 16) { _Pragma("unroll") for (int j0 = 0; j0 < 8; ++j0) *(LAS f32x4*)(L + L_RED + w * RED_W + (j0 * 17 + lane) * 16) = d_[j0]; } \
    } while (0)

  if (!isA) {
    float v[16];
#pragma unroll
    for (int i = 0; i < 8; ++i) { v[2 * i] = blo(xr[i >> 2][i & 3]); v[2 * i + 1] = bhi(xr[i >> 2][i & 3]); }
    if (w == 7) {
      MAKE_TAG();
      asm volatile("s_waitcnt vmcnt(8)" ::: "memory");
      st8(v2u{tag0, tag1}, rws, lane == 0 ? WS_FLAG + cu * 8 : OOB, 0);
    }
    asm volatile("" : "+v"(v[0]), "+v"(v[15]));
    STAMP(1);
#pragma unroll
    for (int pass = 0; pass < 2; ++pass) {
      layernorm16(v);
      if ((lane >> 3) == xs) {
        LAS v4u* d = (LAS v4u*)(L + L_XN + pass * 1024 + wl * 256 + (lane & 7) * 32);
        d[0] = v4u{pk(v[0], v[1]), pk(v[2], v[3]), pk(v[4], v[5]), pk(v[6], v[7])};
        d[1] = v4u{pk(v[8], v[9]), pk(v[10], v[11]), pk(v[12], v[13]), pk(v[14], v[15])};
      }
    }
    STAMP(2);
    BAR();
    STAMP(4);
    P1_MFMA();
    STAMP(5);
    BAR();
    const int mat = wl >> 1, b = lane & 3, jh = (lane >> 2) & 1, nn = (wl & 1) * 8 + (lane >> 3);
    f32x4 sum = {0.f, 0.f, 0.f, 0.f};
#pragma unroll
    for (int ww = 0; ww < 4; ++ww)
#pragma unroll
      for (int k = 0; k < 4; ++k) sum[k] += *(const LAS float*)(L + L_RED + (mat * 4 + ww) * RED_W + ((4 * jh + k) * 17 + nn) * 16 + b * 4);
    const int co = (8 * nn + 4 * jh) * 4;
    const int off = mat == 0 ? WS_Q + ((b * 32 + ci) * 8 + xs) * 512 + co : (ci < 28 ? WS_H + ((xs * 4 + b) * FF + ci * 128) * 4 + co : OOB);
    st16(nopoison(sum), rws, off, 0);
    PIN();
    __builtin_amdgcn_s_setprio(0);
    STAMP(3);
  }
  PIN();
  const size_t kvbase = ((size_t)gidx * 16 + sp) * 262144;
  const rsrc_t rk = mk((const char*)p.kc + kvbase, 262144), rv = mk((const char*)p.vc + kvbase, 262144);
  const int koff = (w >> 2) * 131072 + wl * 4096 + ((lane >> 2) & 3) * 256 + g * 64 + (lane & 3) * 16;
  const int voff = (w >> 2) * 131072 + wl * 4096 + lane * 16;
  v4u r[32];
#define KLD(i) do { r[i] = ld16<AUXK>(rk, koff, ((i) >> 2) * 16384 + ((i) & 3) * 1024); PIN(); } while (0)
#define VLD(i) do { r[i] = ld16<AUXV>(rv, voff, ((i) >> 2) * 16384 + ((i) & 3) * 1024); PIN(); } while (0)
#pragma unroll
  for (int i = 0; i < 16; ++i) KLD(i);
  PIN();
  if (isA) {
    STAMP(1);
    BAR();
    P1_MFMA();
    asm volatile("" :: "v"(cpf));
    BAR();
    STAMP(2);
  }
  PIN();
#pragma unroll
  for (int i = 16; i < FLPOS; ++i) KLD(i);
  PIN();
  v4u fl[2];
  if (isA) {
    fl[0] = ld16<AUX_SC1>(rws, WS_FLAG + lane * 16, 0);
    fl[1] = ld16<AUX_SC1>(rws, WS_FLAG + lane * 16, 1024);
  }
  PIN();
#pragma unroll
  for (int i = FLPOS; i < 32; ++i) KLD(i);
  PIN();
  if (isA) {
    STAMP(3);
    MAKE_TAG();
    int spins = 0;
#define FLAGS_BAD(a, b) __any((((a).x ^ tag0) | ((a).y ^ tag1) | ((a).z ^ tag0) | ((a).w ^ tag1) | ((b).x ^ tag0) | ((b).y ^ tag1) | ((b).z ^ tag0) | ((b).w ^ tag1)) != 0u)
    if (FLAGS_BAD(fl[0], fl[1])) {
      _Pragma("nounroll") while (true) {
        REREAD();
        __builtin_amdgcn_s_sleep(4);
        const v4u f0 = ld16<AUX_SC1>(rws, WS_FLAG + lane * 16, 0), f1 = ld16<AUX_SC1>(rws, WS_FLAG + lane * 16, 1024);
        if (++spins > SPIN_CAP || !FLAGS_BAD(f0, f1)) break;
      }
    }
    REREAD();
    STAMPV(18, spins);
    STAMP(4);
    if (blockIdx.x == 0 && w == 0) {
      st8(v2u{(unsigned)e1, (unsigned)(e1 >> 32)}, rws, lane == 0 ? WS_E : OOB, 0);
    }
    const int qo = WS_Q + ((ab * 32 + 8 * kvh + 2 * wl + (lane >> 5)) * 8) * 512 + (lane & 31) * 16;
    const int hc = lane & 7, hbt = (lane >> 3) & 3, hb = 112 * hd + 28 * wl + 4 * hc;
    const int ho = hc < 7 ? WS_H + ((16 * (lane >> 5) + hbt) * FF + hb) * 4 : OOB;
    const v2u bia = ld8<0>(mk(p.b1, FF * 2), hc < 7 ? hb * 2 : OOB, 0);
    v4u qp[8], hp[4];
    spins = 0;
    _Pragma("nounroll") while (true) {
      REREAD();
#pragma unroll
      for (int s = 0; s < 8; ++s) qp[s] = ld16<AUX_SC1>(rws, qo, s * 512);
#pragma unroll
      for (int s = 0; s < 4; ++s) hp[s] = ld16<AUX_SC1>(rws, ho, s * 4 * FF * 4);
      PIN();
      unsigned mx = 0;
#pragma unroll
      for (int s = 0; s < 8; ++s) mx = max(mx, umax4(qp[s]));
#pragma unroll
      for (int s = 0; s < 4; ++s) mx = max(mx, umax4(hp[s]));
      if (!__any(mx == POISON) || ++spins > SPIN_CAP) break;
      __builtin_amdgcn_s_sleep(8);
    }
    STAMPV(19, spins);
    STAMP(5);
    f32x4 qs = __builtin_bit_cast(f32x4, qp[0]);
#pragma unroll
    for (int s = 1; s < 8; ++s) qs += __builtin_bit_cast(f32x4, qp[s]);
    *(LAS v2u*)(L + L_Q + (2 * wl + (lane >> 5)) * 256 + (lane & 31) * 8) = v2u{pk(qs[0], qs[1]), pk(qs[2], qs[3])};
    float hv[4];
#pragma unroll
    for (int j = 0; j < 4; ++j) {
      const float t0 = (u2f(hp[0][j]) + u2f(hp[1][j])) + (u2f(hp[2][j]) + u2f(hp[3][j]));
      auto sw = __builtin_amdgcn_permlane32_swap(f2u(t0), f2u(t0), false, false);
      const float t = u2f(sw[0]) + u2f(sw[1]);
      const float u = rbf(rbf(t) + ((j & 1) ? bhi(bia[j >> 1]) : blo(bia[j >> 1])));
      const float z2 = u * fmaf(u * u, 0.044715f, 1.0f) * (-2.0f * 0.7978845608028654f * 1.4426950408889634f);
      hv[j] = u * __builtin_amdgcn_rcpf(1.0f + __builtin_amdgcn_exp2f(z2));
    }
    if (lane < 32) *(LAS v2u*)(L + L_H + wl * 256 + hbt * 64 + hc * 8) = v2u{pk(hv[0], hv[1]), pk(hv[2], hv[3])};
  }
  STAMP(6);
  BAR();
  STAMP(15);
  const int hl = n16 & 7, wb = n16 >> 3;
  const v4u qb0 = *(const LAS v4u*)(L + L_Q + hl * 256 + (4 * g + wb) * 16);
  const v4u qb1 = *(const LAS v4u*)(L + L_Q + hl * 256 + (4 * g + 2 + wb) * 16);
  float s[16];
#pragma unroll
  for (int u = 0; u < 8; ++u) {
    float t0[4], t1[4];
#pragma unroll
    for (int k = 0; k < 4; ++k) {
      const f32x4 d0 = mfma(r[4 * u + k], qb0, f32x4{0.f, 0.f, 0.f, 0.f});
      const f32x4 d1 = mfma(r[4 * u + k], qb1, f32x4{0.f, 0.f, 0.f, 0.f});
      t0[k] = d0[0] + d1[2];
      t1[k] = d0[1] + d1[3];
    }
#pragma unroll
    for (int k = 0; k < 4; k += 2) {
      const float xx = wb ? t1[k + 1] : t0[k], yy = wb ? t1[k] : t0[k + 1];
      s[2 * u + (k >> 1)] = xx + ror<8>(yy);
    }
    PIN();
#pragma unroll
    for (int k = 0; k < 4; ++k) VLD(4 * u + k);
    PIN();
  }
  STAMP(7);
  float m = -INFINITY;
#pragma unroll
  for (int i = 0; i < 16; i += 2) {
    const unsigned a = pk(s[i], s[i + 1]);
    const unsigned c = pk(blo(a) * 0.08838834764831845f, bhi(a) * 0.08838834764831845f);
    s[i] = blo(c); s[i + 1] = bhi(c);
    m = fmaxf(m, fmaxf(s[i], s[i + 1]));
  }
  m = fmaxf(m, ror<8>(m));
  m = xrow_max(m);
  if (lane < 8) *(LAS float*)(L + L_M + (w * 8 + lane) * 4) = m;
  f32x4 acc[8];
  float lsum = 0.f;
#pragma unroll
  for (int j = 0; j < 8; ++j) acc[j] = f32x4{0.f, 0.f, 0.f, 0.f};
  constexpr float LOG2E = 1.4426950408889634f;
  if (!isA) {
    __builtin_amdgcn_s_setprio(2);
    BAR();
    const float mA = *(const LAS float*)(L + L_M + (wl * 8 + hl) * 4);
#pragma unroll
    for (int ww = 0; ww < 8; ++ww) m = fmaxf(m, *(const LAS float*)(L + L_M + (ww * 8 + hl) * 4));
    const float f = __builtin_amdgcn_exp2f((mA - m) * LOG2E);
    lsum = f * *(const LAS float*)(L + L_L + (wl * 8 + hl) * 4);
#pragma unroll
    for (int j = 0; j < 8; ++j) acc[j] = f * *(const LAS f32x4*)(L + L_ACC + wl * 8192 + j * 1024 + lane * 16);
  }
  PIN();
  unsigned pb[16];
  {
    float l = 0.f;
    const unsigned msk = wb ? 0xffff0000u : 0x0000ffffu;
#pragma unroll
    for (int i = 0; i < 16; ++i) {
      const float e = __builtin_amdgcn_exp2f((s[i] - m) * LOG2E);
      l += e;
      const unsigned t = pk(e, e) & msk;
      pb[i] = t | roru<8>(t);
    }
    l += ror<8>(l);
    lsum += xrow_sum(l);
    if (lane < 8) *(LAS float*)(L + L_L + (w * 8 + lane) * 4) = lsum;
  }
  PIN();
#pragma unroll
  for (int T = 0; T < 4; ++T) {
    const v4u pop = {pb[4 * T], pb[4 * T + 1], pb[4 * T + 2], pb[4 * T + 3]};
#pragma unroll
    for (int j0 = 0; j0 < 8; ++j0) acc[j0] = mfma(tr8(&r[8 * T], j0), pop, acc[j0]);
    PIN();
  }
  STAMP(8);
  if (isA) {
#pragma unroll
    for (int j = 0; j < 8; ++j) *(LAS f32x4*)(L + L_ACC + wl * 8192 + j * 1024 + lane * 16) = acc[j];
    BAR();
    const rsrc_t r2 = mk(p.w2, FF * DM * 2), ro = mk(p.wo, NQ * DM * 2);
    const int o2 = (112 * hd + 28 * wl + 8 * g) * 2048 + cg * 256 + n16 * 16;
    const int o2b = g < 3 ? o2 : OOB;
    const int oo = (128 * hd + 32 * wl + 8 * g) * 2048 + cg * 256 + n16 * 16;
    v4u tw[16];
#define TLD(j) do { tw[j] = (j) < 8 ? ld16<AUXW2>(r2, (j) < 4 ? o2 : o2b, (j) * 2048) : ld16<AUXW2>(ro, oo, ((j) - 8) * 2048); PIN(); } while (0)
#pragma unroll
    for (int j = 0; j < NPRE; ++j) TLD(j);
    STAMP(9);
    BAR();
#pragma unroll
    for (int j = NPRE; j < 16; ++j) TLD(j);
    const v4u hact = *(const LAS v4u*)(L + L_H + wl * 256 + (n16 & 3) * 64 + g * 16);
    f32x4 d[8];
#pragma unroll
    for (int j0 = 0; j0 < 8; ++j0) d[j0] = mfma(hact, tr8(tw, j0), f32x4{0.f, 0.f, 0.f, 0.f});
    v4u wt[8];
#pragma unroll
    for (int j0 = 0; j0 < 8; ++j0) { wt[j0] = tr8(tw + 8, j0); asm volatile("" : "+v"(wt[j0])); }
    STAMP(10);
    BAR();
    STAMP(11);
    const v4u oact = *(const LAS v4u*)(L + L_OA + (n16 & 3) * 256 + (32 * wl + 8 * g) * 2);
#pragma unroll
    for (int j0 = 0; j0 < 8; ++j0) d[j0] = mfma(oact, wt[j0], d[j0]);
    if (lane < 16) {
#pragma unroll
      for (int j0 = 0; j0 < 8; ++j0) *(LAS f32x4*)(L + L_RED + wl * RED_W + (j0 * 18 + lane) * 16) = d[j0];
    }
    BAR();
    STAMP(12);
    asm volatile(".Lfd_code_end:");
  } else {
#pragma unroll
    for (int i = 0; i < 4; ++i) {
      const f32x4 t = {wb ? acc[4][i] : acc[0][i], wb ? acc[5][i] : acc[1][i], wb ? acc[6][i] : acc[2][i], wb ? acc[7][i] : acc[3][i]};
      *(LAS f32x4*)(L + L_O + wl * O_W + (hl * 132 + 8 * (4 * g + i) + 4 * wb) * 4) = t;
    }
    const int fb = (lane >> 2) & 3, fc = 4 * cu + (lane & 3);
    const unsigned xres = __builtin_amdgcn_raw_buffer_load_b16(mk(p.x, 4 * DM * 2), (fb * DM + fc) * 2, 0, 0);
    const unsigned b2v = __builtin_amdgcn_raw_buffer_load_b16(mk(p.b2, DM * 2), fc * 2, 0, 0);
    BAR();
    STAMP(9);
    {
      const int hh = 2 * wl + (lane >> 5), c4 = lane & 31;
      f32x4 t = *(const LAS f32x4*)(L + L_O + (hh * 132 + 4 * c4) * 4);
#pragma unroll
      for (int ww = 1; ww < 4; ++ww) t += *(const LAS f32x4*)(L + L_O + ww * O_W + (hh * 132 + 4 * c4) * 4);
      st16(nopoison(t), rws, WS_O + (((8 * kvh + hh) * 4 + ab) * 16 + sp) * 512 + c4 * 16, 0);
      if (w == 7) {
        float l = 0.f;
#pragma unroll
        for (int ww = 4; ww < 8; ++ww) l += *(const LAS float*)(L + L_L + (ww * 8 + hl) * 4);
        st8(v2u{min(f2u(m), POISON - 1), min(f2u(l), POISON - 1)}, rws, lane < 8 ? WS_ML + (((8 * kvh + lane) * 4 + ab) * 16 + sp) * 8 : OOB, 0);
      }
    }
    STAMP(10);
    {
      const int hf = lane >> 5;
      const int rec = (hd * 4 + wl) * 16;
      const int oo_ = WS_O + (rec + 8 * hf) * 512 + (lane & 31) * 16, mo = WS_ML + (rec + ((n16 + 8 * hf) & 15)) * 8;
      v4u op[8];
      v2u ml;
      int spins = 0;
      __builtin_amdgcn_s_setprio(0);
      _Pragma("nounroll") while (true) {
        REREAD();
        ml = ld8<AUX_SC1>(rws, mo, 0);
#pragma unroll
        for (int k = 0; k < 8; ++k) op[k] = ld16<AUX_SC1>(rws, oo_, k * 512);
        PIN();
        unsigned mx = max(ml.x, ml.y);
#pragma unroll
        for (int k = 0; k < 8; ++k) mx = max(mx, umax4(op[k]));
        if (!__any(mx == POISON) || ++spins > SPIN_CAP) break;
        __builtin_amdgcn_s_sleep(1);
      }
      STAMPV(18, spins);
      STAMP(13);
      float M = u2f(ml.x);
      M = fmaxf(M, ror<8>(M)); M = fmaxf(M, ror<4>(M)); M = fmaxf(M, ror<2>(M)); M = fmaxf(M, ror<1>(M));
      const float e = __builtin_amdgcn_exp2f((u2f(ml.x) - M) * LOG2E);
      float Ls = e * u2f(ml.y);
      Ls += ror<8>(Ls); Ls += ror<4>(Ls); Ls += ror<2>(Ls); Ls += ror<1>(Ls);
      const float f = e * __builtin_amdgcn_rcpf(Ls);
      f32x4 t = {0.f, 0.f, 0.f, 0.f};
#define OACC(k) t += u2f(__builtin_amdgcn_mov_dpp(f2u(f), 0x150 + (k), 0xf, 0xf, true)) * __builtin_bit_cast(f32x4, op[k])
      OACC(0); OACC(1); OACC(2); OACC(3); OACC(4); OACC(5); OACC(6); OACC(7);
#pragma unroll
      for (int k = 0; k < 4; ++k) { auto sw = __builtin_amdgcn_permlane32_swap(f2u(t[k]), f2u(t[k]), false, false); t[k] = u2f(sw[0]) + u2f(sw[1]); }
      if (lane < 32) *(LAS v2u*)(L + L_OA + wl * 256 + lane * 8) = v2u{pk(t[0], t[1]), pk(t[2], t[3])};
      if (wl < 2) __builtin_amdgcn_s_setprio(2);
    }
    BAR();
    BAR();
    if (wl < 2) {
      const int j0 = lane & 7, nn = wl * 8 + (lane >> 3);
      f32x4 t = *(const LAS f32x4*)(L + L_RED + (j0 * 18 + nn) * 16);
#pragma unroll
      for (int ww = 1; ww < 4; ++ww) t += *(const LAS f32x4*)(L + L_RED + ww * RED_W + (j0 * 18 + nn) * 16);
      st16(nopoison(t), rws, WS_F + ((32 * cg + 2 * nn + (j0 >> 2)) * 32 + hd) * 64 + (j0 & 3) * 16, 0);
      STAMP(11);
    } else if (wl == 2) {
      v4u f0, f1;
      int spins = 0;
      _Pragma("nounroll") while (true) {
        REREAD();
        f0 = ld16<AUX_SC1>(rws, WS_F + cu * 2048 + lane * 16, 0);
        f1 = ld16<AUX_SC1>(rws, WS_F + cu * 2048 + lane * 16, 1024);
        if (!__any(max(umax4(f0), umax4(f1)) == POISON) || ++spins > SPIN_CAP) break;
        __builtin_amdgcn_s_sleep(1);
      }
      STAMPV(17, spins);
      STAMP(11);
      f32x4 t = __builtin_bit_cast(f32x4, f0) + __builtin_bit_cast(f32x4, f1);
#pragma unroll
      for (int k = 0; k < 4; ++k) { float y = t[k]; y += ror<4>(y); y += ror<8>(y); t[k] = xrow_sum(y); }
      const float y = (fb == 0 ? t[0] : fb == 1 ? t[1] : fb == 2 ? t[2] : t[3]) + blo(xres) + blo(b2v);
      __builtin_amdgcn_raw_buffer_store_b16((unsigned short)pk(y, y), mk(p.out, 4 * DM * 2), lane < 16 ? (fb * DM + fc) * 2 : OOB, 0, 0);
      STAMP(12);
    }
  }
  STAMP_FLUSH();
}
}

extern "C" void kernel_launch(void* const* d_in, const int* in_sizes, int n_in, void* d_out, int out_size, void* d_ws, size_t ws_size,
                              hipStream_t stream) {
  Params p;
  p.x = d_in[0]; p.kc = d_in[1]; p.vc = d_in[2]; p.wq = d_in[3]; p.wo = d_in[4];
  p.w1 = d_in[6]; p.b1 = d_in[7]; p.w2 = d_in[8]; p.b2 = d_in[9];
  p.out = d_out; p.ws = d_ws;
  hipLaunchKernelGGL(fused_decode, dim3(NWG), dim3(NTHR), 0, stream, p);
}
```

```cpp
#define FL1 1
#include <hip/hip_runtime.h>
#include <hip/hip_bf16.h>
#include <stdint.h>

#define STAMPS 0
#define NPRE 6
#define CODEPF 1
#define TLBPF 0
#define WIN 5
#define WINW 6
#define FLPOS 28
#define RAMP 4
#define WINV 6
#define WINT 6
#define QKPIN 1
#define ABL 0
#define WINK WIN
#define OSLEEP 1
#define PZPOS 0
#define ODELAY 0
namespace {
typedef unsigned v4u __attribute__((ext_vector_type(4)));
typedef unsigned v2u __attribute__((ext_vector_type(2)));
typedef float f32x4 __attribute__((ext_vector_type(4)));
typedef short bf16x8 __attribute__((ext_vector_type(8)));
typedef __bf16 bf16x2 __attribute__((ext_vector_type(2)));
typedef float f32x2 __attribute__((ext_vector_type(2)));
typedef __amdgpu_buffer_rsrc_t rsrc_t;
#define GAS __attribute__((address_space(1)))
#define LAS __attribute__((address_space(3)))

constexpr int NWG = 256, NTHR = 512;
constexpr int DM = 1024, NQ = 4096, FF = 3584;
constexpr int WS_E = 0;
constexpr int WS_FLAG = 4096;
constexpr int WS_Q = 8192;
constexpr int WS_H = WS_Q + 4 * 32 * 4 * 512;
constexpr int WS_O = WS_H + 4 * 4 * FF * 4;
constexpr int WS_ML = WS_O + 32 * 4 * 16 * 256;
constexpr int WS_F = WS_ML + 32 * 4 * 16 * 8;
constexpr int WS_END = WS_F + 256 * 32 * 64;
constexpr int WS_STAMP = 8 << 20;
constexpr int OOB = 0x7f000000;
constexpr int AUX_NT = 2, AUX_SC1 = 16;
constexpr unsigned POISON = 0u;
constexpr int SPIN_CAP = 1 << 14;
constexpr int L_CTR = 0;
constexpr int L_XN = 64;
constexpr int L_Q = L_XN + 2048;
constexpr int L_M = L_Q + 2048;
constexpr int L_L = L_M + 256;
constexpr int L_RED = L_L + 256;
constexpr int RED_W = 8 * 18 * 16;
constexpr int L_O = L_RED + 4 * RED_W;
constexpr int O_W = 8 * 132 * 4;
constexpr int L_H = L_O + 5 * O_W;
constexpr int L_OA = L_H + 1024;
constexpr int L_END = L_OA + 1024;

struct Params {
  const void *x, *kc, *vc, *wq, *wo, *w1, *b1, *w2, *b2;
  void *out, *ws;
};

__device__ __forceinline__ rsrc_t mk(const void* p, int bytes) { return __builtin_amdgcn_make_buffer_rsrc((void*)p, 0, bytes, 0x00020000); }
template <int AUX> __device__ __forceinline__ v4u ld16(rsrc_t r, int voff, int soff) { return __builtin_amdgcn_raw_buffer_load_b128(r, voff, soff, AUX); }
template <int AUX> __device__ __forceinline__ v2u ld8(rsrc_t r, int voff, int soff) { return __builtin_amdgcn_raw_buffer_load_b64(r, voff, soff, AUX); }
__device__ __forceinline__ void st16(v4u v, rsrc_t r, int voff, int soff) { __builtin_amdgcn_raw_buffer_store_b128(v, r, voff, soff, AUX_SC1); }
__device__ __forceinline__ void st8(v2u v, rsrc_t r, int voff, int soff) { __builtin_amdgcn_raw_buffer_store_b64(v, r, voff, soff, AUX_SC1); }
__device__ __forceinline__ f32x4 mfma(v4u a, v4u b, f32x4 c) {
  return __builtin_amdgcn_mfma_f32_16x16x32_bf16(__builtin_bit_cast(bf16x8, a), __builtin_bit_cast(bf16x8, b), c, 0, 0, 0);
}
__device__ __forceinline__ float u2f(unsigned u) { return __builtin_bit_cast(float, u); }
__device__ __forceinline__ unsigned f2u(float f) { return __builtin_bit_cast(unsigned, f); }
__device__ __forceinline__ unsigned pk(float lo, float hi) {
  f32x2 v = {lo, hi};
  return __builtin_bit_cast(unsigned, __builtin_convertvector(v, bf16x2));
}
__device__ __forceinline__ float blo(unsigned u) { return u2f(u << 16); }
__device__ __forceinline__ float bhi(unsigned u) { return u2f(u & 0xffff0000u); }
__device__ __forceinline__ float rbf(float f) { return blo(pk(f, f)); }
template <int N> __device__ __forceinline__ float ror(float x) { return u2f(__builtin_amdgcn_mov_dpp(f2u(x), 0x120 + N, 0xf, 0xf, true)); }
template <int N> __device__ __forceinline__ unsigned roru(unsigned x) { return __builtin_amdgcn_mov_dpp(x, 0x120 + N, 0xf, 0xf, true); }
__device__ __forceinline__ float xrow_sum(float x) {
  auto s = __builtin_amdgcn_permlane16_swap(f2u(x), f2u(x), false, false);
  x = u2f(s[0]) + u2f(s[1]);
  auto t = __builtin_amdgcn_permlane32_swap(f2u(x), f2u(x), false, false);
  return u2f(t[0]) + u2f(t[1]);
}
__device__ __forceinline__ float xrow_max(float x) {
  auto s = __builtin_amdgcn_permlane16_swap(f2u(x), f2u(x), false, false);
  x = fmaxf(u2f(s[0]), u2f(s[1]));
  auto t = __builtin_amdgcn_permlane32_swap(f2u(x), f2u(x), false, false);
  return fmaxf(u2f(t[0]), u2f(t[1]));
}
__device__ __forceinline__ float wave_sum(float x) {
  x += ror<8>(x); x += ror<4>(x); x += ror<2>(x); x += ror<1>(x);
  return xrow_sum(x);
}
__device__ __forceinline__ unsigned umin4(v4u v) { return min(min(v.x, v.y), min(v.z, v.w)); }
__device__ __forceinline__ v4u nopoison(f32x4 v) {
  v4u u = __builtin_bit_cast(v4u, v);
  return v4u{max(u.x, 1u), max(u.y, 1u), max(u.z, 1u), max(u.w, 1u)};
}
#define PIN() __builtin_amdgcn_sched_barrier(0)
#define BAR() do { PIN(); asm volatile("s_waitcnt lgkmcnt(0)\n\ts_barrier" ::: "memory"); PIN(); } while (0)
#define REREAD() asm volatile("" ::: "memory")
__device__ __forceinline__ v4u tr8(const v4u* w, int j0) {
  const unsigned sel = (j0 & 1) ? 0x07060302u : 0x05040100u;
  const int c = j0 >> 1;
  return v4u{__builtin_amdgcn_perm(w[1][c], w[0][c], sel), __builtin_amdgcn_perm(w[3][c], w[2][c], sel),
             __builtin_amdgcn_perm(w[5][c], w[4][c], sel), __builtin_amdgcn_perm(w[7][c], w[6][c], sel)};
}
__device__ __forceinline__ void ln_stats(const float (&v)[16], float& m, float& inv) {
  float s = 0.f;
#pragma unroll
  for (int i = 0; i < 16; ++i) s += v[i];
  const float mf = wave_sum(s) * (1.0f / DM);
  float ss = 0.f;
#pragma unroll
  for (int i = 0; i < 16; ++i) { const float d = v[i] - mf; ss = fmaf(d, d, ss); }
  const float var = rbf(wave_sum(ss) * (1.0f / DM));
  m = rbf(mf);
  inv = __builtin_amdgcn_rcpf(rbf(__builtin_amdgcn_sqrtf(rbf(var + 1e-5f))));
}
__device__ __forceinline__ unsigned ln_pair(float a, float b, float m, float inv) {
  const unsigned d = pk(a - m, b - m);
  return pk(blo(d) * inv, bhi(d) * inv);
}
__device__ __forceinline__ unsigned lds_arrive(LAS unsigned* c, int lane) {
  unsigned old = 0;
  REREAD();
  if (lane == 0) old = __hip_atomic_fetch_add(c, 1u, __ATOMIC_RELAXED, __HIP_MEMORY_SCOPE_WORKGROUP);
  return __builtin_amdgcn_readfirstlane(old);
}
__device__ __forceinline__ void class_bar(LAS unsigned* c, int lane) {
  PIN();
  if (lds_arrive(c, lane) != 3u) {
    _Pragma("nounroll") while (__builtin_amdgcn_readfirstlane(__hip_atomic_load(c, __ATOMIC_RELAXED, __HIP_MEMORY_SCOPE_WORKGROUP)) < 4u)
      __builtin_amdgcn_s_sleep(1);
  }
  REREAD();
  PIN();
}
template <int N> __device__ __forceinline__ void pace() { asm volatile("s_waitcnt vmcnt(%0)" :: "n"(N) : "memory"); }

#define STAMP(i)
#define STAMPV(i, v)
#define STAMP_FLUSH()

__global__ void __launch_bounds__(NTHR, 2) fused_decode(Params p) {
  __shared__ __attribute__((aligned(16))) unsigned char smem[L_END];
  LAS unsigned char* L = (LAS unsigned char*)smem;
  LAS unsigned* ctr = (LAS unsigned*)(L + L_CTR);
  const int tid = threadIdx.x, lane = tid & 63;
  const int w = __builtin_amdgcn_readfirstlane(tid >> 6);
  const int wl = w & 3;
  const bool isA = w < 4;
  const int xs = blockIdx.x & 7, ci = blockIdx.x >> 3;
  const int n16 = lane & 15, g = lane >> 4;
  unsigned cpf = 0, tpf = 0;
  {
    unsigned long long pc; int len;
    asm volatile("s_getpc_b64 %0\n.Lpf_base%=:\n\ts_mov_b32 %1, .Lfd_code_end-.Lpf_base%=" : "=s"(pc), "=s"(len));
    if (tid < 5) cpf = __builtin_amdgcn_raw_buffer_load_b32(mk((const void*)pc, len), (tid * 32 + ci) * 128, 0, 0);
  }
  PIN();
  asm volatile("" :: "s"(p.x), "s"(p.kc), "s"(p.vc), "s"(p.wq), "s"(p.wo), "s"(p.w1), "s"(p.b1), "s"(p.w2), "s"(p.b2), "s"(p.out), "s"(p.ws));
  v2u xr[4];
  if (isA) {
    const rsrc_t rx = mk(p.x, 4 * DM * 2);
#pragma unroll
    for (int i = 0; i < 4; ++i) xr[i] = ld8<0>(rx, lane * 8, wl * 2048 + ((i + (xs >> 1)) & 3) * 512);
  }
  PIN();
  const rsrc_t rws = mk(p.ws, WS_END);
  const int gidx = 2 * xs + (ci >> 4), ab = gidx >> 2, kvh = gidx & 3, sp = ci & 15;
  const int kq = xs >> 1, isq = ci < 16, cb = isq ? 16 * (xs & 1) + ci : 14 * (xs & 1) + ci - 16;
  const int hd = 4 * xs + (ci >> 3), cg = ci & 7;
  const int cu = xs * 32 + ci;
  const size_t kvbase = ((size_t)gidx * 16 + sp) * 262144;
  unsigned long long gen = *(const __attribute__((address_space(4))) unsigned long long*)p.ws;
#define MAKE_TAG() const unsigned long long tag64 = (gen + 1ull) * 0x9E3779B97F4A7C15ull; \
  const unsigned tag0 = (unsigned)tag64, tag1 = (unsigned)(tag64 >> 32)

  const int hc = lane & 7, hbt = (lane >> 3) & 3, hb = 112 * hd + 28 * wl + 4 * hc;
  const int fb = (lane >> 2) & 3, fc = 4 * cu + (lane & 3);
  v2u sm;
  if (isA) {
    if (PZPOS == 1) {
      const v4u pz = {POISON, POISON, POISON, POISON};
      st16(pz, rws, wl < 2 ? WS_O + (((8 * kvh + 4 * (wl & 1) + g) * 4 + ab) * 16 + sp) * 256 + n16 * 16
                           : WS_F + ((32 * cg + 16 * (wl & 1) + (lane >> 2)) * 32 + hd) * 64 + (lane & 3) * 16, 0);
      if (wl == 0) st8(v2u{POISON, POISON}, rws, lane < 8 ? WS_ML + (((8 * kvh + lane) * 4 + ab) * 16 + sp) * 8 : OOB, 0);
      PIN();
    }
    if (tid < 8) ctr[tid] = 0u;
    PIN();
    float v[16];
#pragma unroll
    for (int i = 0; i < 8; ++i) { v[2 * i] = blo(xr[i >> 1][i & 1]); v[2 * i + 1] = bhi(xr[i >> 1][i & 1]); }
    STAMP(1);
    float m, inv;
    ln_stats(v, m, inv);
    if (!isq) {
#pragma unroll
      for (int i = 0; i < 16; i += 2) { const unsigned o = ln_pair(v[i], v[i + 1], m, inv); v[i] = blo(o); v[i + 1] = bhi(o); }
      ln_stats(v, m, inv);
    }
    *(LAS v2u*)(L + L_XN + wl * 512 + lane * 8) = v2u{ln_pair(v[0], v[1], m, inv), ln_pair(v[2], v[3], m, inv)};
    asm volatile("" : "+s"(gen));
    if (PZPOS == 2) {
      const v4u pz = {POISON, POISON, POISON, POISON};
      st16(pz, rws, wl < 2 ? WS_O + (((8 * kvh + 4 * (wl & 1) + g) * 4 + ab) * 16 + sp) * 256 + n16 * 16
                           : WS_F + ((32 * cg + 16 * (wl & 1) + (lane >> 2)) * 32 + hd) * 64 + (lane & 3) * 16, 0);
      if (wl == 0) st8(v2u{POISON, POISON}, rws, lane < 8 ? WS_ML + (((8 * kvh + lane) * 4 + ab) * 16 + sp) * 8 : OOB, 0);
      PIN();
    }
    STAMP(2);
    if (PZPOS) pace<0>();
    BAR();
    asm volatile("" :: "v"(cpf), "v"(tpf));
  } else {
    const int ldw = isq ? NQ * 2 : FF * 2;
    const rsrc_t rw = mk(isq ? p.wq : p.w1, DM * ldw);
    const int wo_ = ci < 30 ? (256 * kq + 64 * wl + 8 * g) * ldw + cb * 256 + n16 * 16 : OOB;
    v4u wv[16];
#pragma unroll
    for (int j = 0; j < 16; ++j) {
      if (j >= WINW) pace<WINW - 1>();
      wv[j] = ld16<AUX_NT>(rw, wo_, ((j >> 3) * 32 + (j & 7)) * ldw);
      PIN();
    }
    asm volatile("" : "+s"(gen));
    STAMP(1);
    BAR();
    STAMP(2);
    f32x4 d[8];
#pragma unroll
    for (int t = 0; t < 2; ++t) {
      const v4u act = *(const LAS v4u*)(L + L_XN + (n16 & 3) * 512 + (64 * wl + 32 * t + 8 * g) * 2);
#pragma unroll
      for (int j0 = 0; j0 < 8; ++j0) d[j0] = mfma(act, tr8(wv + 8 * t, j0), t ? d[j0] : f32x4{0.f, 0.f, 0.f, 0.f});
      PIN();
    }
    if (lane < 16) {
#pragma unroll
      for (int j0 = 0; j0 < 8; ++j0) *(LAS f32x4*)(L + L_RED + wl * RED_W + (j0 * 17 + lane) * 16) = d[j0];
    }
    STAMP(3);
    class_bar(ctr + 0, lane);
    if (wl < 2) {
      const int b = lane & 3, jh = (lane >> 2) & 1, nn = wl * 8 + (lane >> 3);
      f32x4 sum = {0.f, 0.f, 0.f, 0.f};
#pragma unroll
      for (int ww = 0; ww < 4; ++ww)
#pragma unroll
        for (int k = 0; k < 4; ++k) sum[k] += *(const LAS float*)(L + L_RED + ww * RED_W + ((4 * jh + k) * 17 + nn) * 16 + b * 4);
      const int co = (8 * nn + 4 * jh) * 4;
      const int off = isq ? WS_Q + ((b * 32 + cb) * 4 + kq) * 512 + co : (ci < 30 ? WS_H + ((kq * 4 + b) * FF + cb * 128) * 4 + co : OOB);
      st16(__builtin_bit_cast(v4u, sum), rws, (ABL & 4) ? OOB : off, 0);
    } else if (!(ABL & 1) && !PZPOS) {
      const v4u pz = {POISON, POISON, POISON, POISON};
      st16(pz, rws, WS_O + (((8 * kvh + 4 * (wl & 1) + g) * 4 + ab) * 16 + sp) * 256 + n16 * 16, 0);
      st16(pz, rws, WS_F + ((32 * cg + 16 * (wl & 1) + (lane >> 2)) * 32 + hd) * 64 + (lane & 3) * 16, 0);
      st8(v2u{POISON, POISON}, rws, lane < 4 ? WS_ML + (((8 * kvh + 4 * (wl & 1) + lane) * 4 + ab) * 16 + sp) * 8 : OOB, 0);
    }
    STAMP(4);
    sm.x = __builtin_amdgcn_raw_buffer_load_b16(mk(p.x, 4 * DM * 2), wl == 2 && !(ABL & 2) ? (fb * DM + fc) * 2 : OOB, 0, 0);
    sm.y = __builtin_amdgcn_raw_buffer_load_b16(mk(p.b2, DM * 2), wl == 2 && !(ABL & 2) ? fc * 2 : OOB, 0, 0);
    pace<0>();
    if (lds_arrive(ctr + 1, lane) == 3u) {
      MAKE_TAG();
      st8(v2u{tag0, tag1}, rws, lane == 0 && !(ABL & 4) ? WS_FLAG + cu * 8 : OOB, 0);
    }
    STAMP(5);
    BAR();
  }
  const rsrc_t rk = mk((const char*)p.kc + kvbase, 262144), rv = mk((const char*)p.vc + kvbase, 262144);
  const int koff = (w >> 2) * 131072 + wl * 4096 + ((lane >> 2) & 3) * 256 + g * 64 + (lane & 3) * 16;
  const int voff = (w >> 2) * 131072 + wl * 4096 + lane * 16;
  v4u r[32], fa_, fb_;
  __builtin_amdgcn_s_setprio(1);
#pragma unroll
  for (int i = 0; i < 32; ++i) {
    if (i == FLPOS) {
      fa_ = ld16<AUX_SC1>(rws, isA && (!FL1 || w == 0) ? WS_FLAG + lane * 16 : OOB, 0); fb_ = ld16<AUX_SC1>(rws, isA && (!FL1 || w == 0) ? WS_FLAG + lane * 16 : OOB, 1024);
      PIN();
    }
    if (i >= FLPOS && i < FLPOS + WINK) pace<WINK + 1>();
    else if (i >= WINK) pace<WINK - 1>();
    else if (i > 0 && RAMP) __builtin_amdgcn_s_sleep(RAMP);
    r[i] = ld16<AUX_NT>(rk, koff, (i >> 2) * 16384 + (i & 3) * 1024);
    PIN();
  }
  __builtin_amdgcn_s_setprio(0);
  STAMP(6);
  BAR();
  if (isA) {
    MAKE_TAG();
    int spins = 0;
#define FLAGS_BAD(a, b) __any((((a).x ^ tag0) | ((a).y ^ tag1) | ((a).z ^ tag0) | ((a).w ^ tag1) | ((b).x ^ tag0) | ((b).y ^ tag1) | ((b).z ^ tag0) | ((b).w ^ tag1)) != 0u)
    if (!(ABL & 4) && (!FL1 || w == 0) && FLAGS_BAD(fa_, fb_)) {
      _Pragma("nounroll") while (true) {
        REREAD();
        const v4u a = ld16<AUX_SC1>(rws, WS_FLAG + lane * 16, 0), b = ld16<AUX_SC1>(rws, WS_FLAG + lane * 16, 1024);
        if (++spins > SPIN_CAP || !FLAGS_BAD(a, b)) break;
        __builtin_amdgcn_s_sleep(4);
      }
    }
    REREAD();
    if (FL1) class_bar(ctr + 5, lane);
    STAMPV(18, spins);
    STAMP(3);
    if (blockIdx.x == 0 && w == 0) st8(v2u{(unsigned)(gen + 1ull), (unsigned)((gen + 1ull) >> 32)}, rws, lane == 0 ? WS_E : OOB, 0);
    const int qo = WS_Q + ((ab * 32 + 8 * kvh + 2 * wl + (lane >> 5)) * 4) * 512 + (lane & 31) * 16;
    v4u qp[4];
#pragma unroll
    for (int s = 0; s < 4; ++s) qp[s] = ld16<AUX_SC1>(rws, (ABL & 8) ? OOB : qo, s * 512);
    PIN();
    const f32x4 qs = (__builtin_bit_cast(f32x4, qp[0]) + __builtin_bit_cast(f32x4, qp[1])) + (__builtin_bit_cast(f32x4, qp[2]) + __builtin_bit_cast(f32x4, qp[3]));
    *(LAS v2u*)(L + L_Q + (2 * wl + (lane >> 5)) * 256 + (lane & 31) * 8) = v2u{pk(qs[0], qs[1]), pk(qs[2], qs[3])};
    class_bar(ctr + 2, lane);
    STAMP(4);
  }
  const int hl = n16 & 7, wb = n16 >> 3;
  const v4u qb0 = *(const LAS v4u*)(L + L_Q + hl * 256 + (4 * g + wb) * 16);
  const v4u qb1 = *(const LAS v4u*)(L + L_Q + hl * 256 + (4 * g + 2 + wb) * 16);
  float s[16];
#pragma unroll
  for (int u = 0; u < 8; ++u) {
    float t0[4], t1[4];
#pragma unroll
    for (int k = 0; k < 4; ++k) {
      const f32x4 d0 = mfma(r[4 * u + k], qb0, f32x4{0.f, 0.f, 0.f, 0.f});
      const f32x4 d1 = mfma(r[4 * u + k], qb1, f32x4{0.f, 0.f, 0.f, 0.f});
      t0[k] = d0[0] + d1[2];
      t1[k] = d0[1] + d1[3];
    }
#pragma unroll
    for (int k = 0; k < 4; k += 2) {
      const float xx = wb ? t1[k + 1] : t0[k], yy = wb ? t1[k] : t0[k + 1];
      s[2 * u + (k >> 1)] = xx + ror<8>(yy);
    }
    if (QKPIN) PIN();
  }
  float m = -INFINITY;
#pragma unroll
  for (int i = 0; i < 16; i += 2) {
    const unsigned a = pk(s[i], s[i + 1]);
    const unsigned c = pk(blo(a) * 0.08838834764831845f, bhi(a) * 0.08838834764831845f);
    s[i] = blo(c); s[i + 1] = bhi(c);
    m = fmaxf(m, fmaxf(s[i], s[i + 1]));
  }
  m = fmaxf(m, ror<8>(m));
  m = xrow_max(m);
  if (lane < 8) *(LAS float*)(L + L_M + (w * 8 + lane) * 4) = m;
  constexpr float LOG2E = 1.4426950408889634f;
  if (!isA) {
    class_bar(ctr + 3, lane);
#pragma unroll
    for (int ww = 0; ww < 8; ++ww) m = fmaxf(m, *(const LAS float*)(L + L_M + (ww * 8 + hl) * 4));
  }
  unsigned pb[16];
  float lsum = 0.f;
  {
    const unsigned msk = wb ? 0xffff0000u : 0x0000ffffu;
#pragma unroll
    for (int i = 0; i < 16; ++i) {
      const float e = __builtin_amdgcn_exp2f((s[i] - m) * LOG2E);
      lsum += e;
      const unsigned t = pk(e, e) & msk;
      pb[i] = t | roru<8>(t);
      asm volatile("" : "+v"(pb[i]));
    }
    lsum += ror<8>(lsum);
    lsum = xrow_sum(lsum);
    if (lane < 8) *(LAS float*)(L + L_L + (w * 8 + lane) * 4) = lsum;
  }
  f32x4 acc[8];
#pragma unroll
  for (int j = 0; j < 8; ++j) acc[j] = f32x4{0.f, 0.f, 0.f, 0.f};
  STAMP(7);
  BAR();
  STAMP(15);
#define PV(n) acc[(n) & 7] = mfma(tr8(&r[(n) & ~7], (n) & 7), v4u{pb[((n) >> 3) * 4], pb[((n) >> 3) * 4 + 1], pb[((n) >> 3) * 4 + 2], pb[((n) >> 3) * 4 + 3]}, acc[(n) & 7])
  __builtin_amdgcn_s_setprio(1);
#pragma unroll
  for (int i = 0; i < 32; ++i) {
    if (i >= WINV) pace<WINV - 1>();
    else if (i > 0 && RAMP) __builtin_amdgcn_s_sleep(RAMP);
    r[i] = ld16<AUX_NT>(rv, voff, (i >> 2) * 16384 + (i & 3) * 1024);
    PIN();
    if (i >= 7 + WINV) { PV(i - 7 - WINV); PIN(); }
  }
  const int mo = WS_ML + ((hd * 4 + wl) * 16 + ((n16 + 4 * g) & 15)) * 8;
  v2u ml = ld8<AUX_SC1>(rws, isA ? OOB : mo, 0);
  STAMP(16);
  BAR();
#pragma unroll
  for (int n = 32 - 7 - WINV; n < 32; ++n) { PV(n); if ((n & 7) == 7) PIN(); }
  STAMP(8);
#define PUT_ACC(f) _Pragma("unroll") for (int i = 0; i < 4; ++i) { \
      const f32x4 t = {wb ? acc[4][i] : acc[0][i], wb ? acc[5][i] : acc[1][i], wb ? acc[6][i] : acc[2][i], wb ? acc[7][i] : acc[3][i]}; \
      *(LAS f32x4*)(L + L_O + wl * O_W + (hl * 132 + 8 * (4 * g + i) + 4 * wb) * 4) = f t; }
  const int oro = ((2 * wl + (lane >> 5)) * 132 + 4 * (lane & 31)) * 4;
  if (isA) {
    __builtin_amdgcn_s_setprio(0);
    float M = m;
#pragma unroll
    for (int ww = 0; ww < 8; ++ww) M = fmaxf(M, *(const LAS float*)(L + L_M + (ww * 8 + hl) * 4));
    const float fa = __builtin_amdgcn_exp2f((m - M) * LOG2E);
    PUT_ACC(fa *);
    if (lane < 8) *(LAS float*)(L + L_L + (w * 8 + lane) * 4) = fa * lsum;
    class_bar(ctr + 4, lane);
    {
      f32x4 t = *(const LAS f32x4*)(L + L_O + oro);
#pragma unroll
      for (int ww = 1; ww < 4; ++ww) t += *(const LAS f32x4*)(L + L_O + ww * O_W + oro);
      *(LAS f32x4*)(L + L_O + 4 * O_W + oro) = t;
    }
    if (w == 0) {
      float l = 0.f;
#pragma unroll
      for (int ww = 0; ww < 8; ++ww) l += *(const LAS float*)(L + L_L + (ww * 8 + hl) * 4);
      st8(v2u{max(f2u(M), 1u), max(f2u(l), 1u)}, rws, lane < 8 ? WS_ML + (((8 * kvh + lane) * 4 + ab) * 16 + sp) * 8 : OOB, 0);
    }
    {
    const int ho = hc < 7 ? WS_H + ((8 * (lane >> 5) + hbt) * FF + hb) * 4 : OOB;
    v4u hp[2];
#pragma unroll
    for (int s = 0; s < 2; ++s) hp[s] = ld16<AUX_SC1>(rws, (ABL & 8) ? OOB : ho, s * 4 * FF * 4);
    sm = ld8<0>(mk(p.b1, FF * 2), hc < 7 && !(ABL & 2) ? hb * 2 : OOB, 0);
    PIN();
    float hv[4];
#pragma unroll
    for (int j = 0; j < 4; ++j) {
      const float t0 = u2f(hp[0][j]) + u2f(hp[1][j]);
      auto sw = __builtin_amdgcn_permlane32_swap(f2u(t0), f2u(t0), false, false);
      const float t = u2f(sw[0]) + u2f(sw[1]);
      const float u = rbf(rbf(t) + ((j & 1) ? bhi(sm[j >> 1]) : blo(sm[j >> 1])));
      const float z2 = u * fmaf(u * u, 0.044715f, 1.0f) * (-2.0f * 0.7978845608028654f * 1.4426950408889634f);
      hv[j] = u * __builtin_amdgcn_rcpf(1.0f + __builtin_amdgcn_exp2f(z2));
    }
    if (lane < 32) *(LAS v2u*)(L + L_H + wl * 256 + hbt * 64 + hc * 8) = v2u{pk(hv[0], hv[1]), pk(hv[2], hv[3])};
    }
    const rsrc_t r2 = mk(p.w2, FF * DM * 2), ro = mk(p.wo, NQ * DM * 2);
    const int o2 = (112 * hd + 28 * wl + 8 * g) * 2048 + cg * 256 + n16 * 16;
    const int o2b = g < 3 ? o2 : OOB;
    const int oo = (128 * hd + 32 * wl + 8 * g) * 2048 + cg * 256 + n16 * 16;
    const v4u hact = *(const LAS v4u*)(L + L_H + wl * 256 + (n16 & 3) * 64 + g * 16);
    v4u tw[16];
#define TLD(j) do { if ((j) >= WINT) pace<WINT - 1>(); else if ((j) > 0 && (j) < NPRE && RAMP) __builtin_amdgcn_s_sleep(RAMP); tw[j] = (j) < 8 ? ld16<AUX_NT>(r2, (j) < 4 ? o2 : o2b, (j) * 2048) : ld16<AUX_NT>(ro, oo, ((j) - 8) * 2048); PIN(); } while (0)
    STAMP(5);
    BAR();
#pragma unroll
    for (int j = 0; j < NPRE; ++j) TLD(j);
    STAMP(9);
    BAR();
#pragma unroll
    for (int j = NPRE; j < 16; ++j) TLD(j);
    f32x4 d[8];
#pragma unroll
    for (int j0 = 0; j0 < 8; ++j0) d[j0] = mfma(hact, tr8(tw, j0), f32x4{0.f, 0.f, 0.f, 0.f});
    v4u wt[8];
#pragma unroll
    for (int j0 = 0; j0 < 8; ++j0) { wt[j0] = tr8(tw + 8, j0); asm volatile("" : "+v"(wt[j0])); }
    STAMP(10);
    BAR();
    STAMP(11);
    const v4u oact = *(const LAS v4u*)(L + L_OA + (n16 & 3) * 256 + (32 * wl + 8 * g) * 2);
#pragma unroll
    for (int j0 = 0; j0 < 8; ++j0) d[j0] = mfma(oact, wt[j0], d[j0]);
    if (lane < 16) {
#pragma unroll
      for (int j0 = 0; j0 < 8; ++j0) *(LAS f32x4*)(L + L_RED + wl * RED_W + (j0 * 18 + lane) * 16) = d[j0];
    }
    BAR();
    STAMP(12);
    asm volatile(".Lfd_code_end:");
  } else {
    PUT_ACC();
    BAR();
    STAMP(9);
    {
      f32x4 t = *(const LAS f32x4*)(L + L_O + 4 * O_W + oro);
#pragma unroll
      for (int ww = 0; ww < 4; ++ww) t += *(const LAS f32x4*)(L + L_O + ww * O_W + oro);
      st8(v2u{max(pk(t[0], t[1]), 1u), max(pk(t[2], t[3]), 1u)}, rws, WS_O + (((8 * kvh + 2 * wl + (lane >> 5)) * 4 + ab) * 16 + sp) * 256 + (lane & 31) * 8, 0);
    }
    PIN();
    STAMP(10);
    __builtin_amdgcn_s_setprio(0);
    {
      int spins = 0;
      if (__any(min(ml.x, ml.y) == POISON)) {
        _Pragma("nounroll") while (true) {
          REREAD();
          ml = ld8<AUX_SC1>(rws, mo, 0);
          if (!__any(min(ml.x, ml.y) == POISON) || ++spins > SPIN_CAP) break;
          __builtin_amdgcn_s_sleep(2);
        }
      }
      STAMPV(19, spins);
      float M = u2f(ml.x);
      M = fmaxf(M, ror<8>(M)); M = fmaxf(M, ror<4>(M)); M = fmaxf(M, ror<2>(M)); M = fmaxf(M, ror<1>(M));
      const float e = __builtin_amdgcn_exp2f((u2f(ml.x) - M) * LOG2E);
      float Ls = e * u2f(ml.y);
      Ls += ror<8>(Ls); Ls += ror<4>(Ls); Ls += ror<2>(Ls); Ls += ror<1>(Ls);
      float f = e * __builtin_amdgcn_rcpf(Ls);
      asm volatile("" : "+v"(f));
      if (ODELAY) __builtin_amdgcn_s_sleep(ODELAY);
      const int oo_ = WS_O + ((hd * 4 + wl) * 16 + 4 * g) * 256 + n16 * 16;
      v4u op[4];
      int of[4];
#pragma unroll
      for (int k = 0; k < 4; ++k) { op[k] = v4u{0u, 0u, 0u, 0u}; of[k] = oo_; }
      spins = 0;
      _Pragma("nounroll") while (true) {
        REREAD();
        v4u nw[4];
#pragma unroll
        for (int k = 0; k < 4; ++k) nw[k] = ld16<AUX_SC1>(rws, of[k], k * 256);
        PIN();
        bool bad = false;
#pragma unroll
        for (int k = 0; k < 4; ++k) {
          op[k] |= nw[k];
          const bool b = umin4(op[k]) == POISON;
          of[k] = b ? oo_ : OOB;
          bad |= b;
        }
        if (!__any(bad) || ++spins > SPIN_CAP) break;
        __builtin_amdgcn_s_sleep(OSLEEP);
      }
      STAMPV(18, spins);
      STAMP(13);
      float t[8] = {0.f, 0.f, 0.f, 0.f, 0.f, 0.f, 0.f, 0.f};
#define OACC(k) do { const float fk = u2f(__builtin_amdgcn_mov_dpp(f2u(f), 0x150 + (k), 0xf, 0xf, true)); \
        _Pragma("unroll") for (int j = 0; j < 4; ++j) { t[2 * j] = fmaf(fk, blo(op[k][j]), t[2 * j]); t[2 * j + 1] = fmaf(fk, bhi(op[k][j]), t[2 * j + 1]); } } while (0)
      OACC(0); OACC(1); OACC(2); OACC(3);
      float u[4];
#pragma unroll
      for (int j = 0; j < 4; ++j) { auto sw = __builtin_amdgcn_permlane32_swap(f2u(t[j]), f2u(t[4 + j]), false, false); u[j] = u2f(sw[0]) + u2f(sw[1]); }
      auto s0 = __builtin_amdgcn_permlane16_swap(f2u(u[0]), f2u(u[2]), false, false);
      auto s1 = __builtin_amdgcn_permlane16_swap(f2u(u[1]), f2u(u[3]), false, false);
      *(LAS unsigned*)(L + L_OA + wl * 256 + n16 * 16 + g * 4) = pk(u2f(s0[0]) + u2f(s0[1]), u2f(s1[0]) + u2f(s1[1]));
      if (wl < 2) __builtin_amdgcn_s_setprio(2);
    }
    BAR();
    BAR();
    if (wl < 2) {
      const int j0 = lane & 7, nn = wl * 8 + (lane >> 3);
      f32x4 t = *(const LAS f32x4*)(L + L_RED + (j0 * 18 + nn) * 16);
#pragma unroll
      for (int ww = 1; ww < 4; ++ww) t += *(const LAS f32x4*)(L + L_RED + ww * RED_W + (j0 * 18 + nn) * 16);
      st16(nopoison(t), rws, WS_F + ((32 * cg + 2 * nn + (j0 >> 2)) * 32 + hd) * 64 + (j0 & 3) * 16, 0);
      STAMP(11);
    } else if (wl == 2) {
      v4u f0, f1;
      int spins = 0;
      _Pragma("nounroll") while (true) {
        REREAD();
        f0 = ld16<AUX_SC1>(rws, WS_F + cu * 2048 + lane * 16, 0);
        f1 = ld16<AUX_SC1>(rws, WS_F + cu * 2048 + lane * 16, 1024);
        if (!__any(min(umin4(f0), umin4(f1)) == POISON) || ++spins > SPIN_CAP) break;
        __builtin_amdgcn_s_sleep(1);
      }
      STAMPV(17, spins);
      STAMP(11);
      f32x4 t = __builtin_bit_cast(f32x4, f0) + __builtin_bit_cast(f32x4, f1);
#pragma unroll
      for (int k = 0; k < 4; ++k) { float y = t[k]; y += ror<4>(y); y += ror<8>(y); t[k] = xrow_sum(y); }
      const float y = (fb == 0 ? t[0] : fb == 1 ? t[1] : fb == 2 ? t[2] : t[3]) + blo(sm.x) + blo(sm.y);
      __builtin_amdgcn_raw_buffer_store_b16((unsigned short)pk(y, y), mk(p.out, 4 * DM * 2), lane < 16 ? (fb * DM + fc) * 2 : OOB, 0, 0);
      STAMP(12);
    }
  }
  STAMP_FLUSH();
}
}

extern "C" void kernel_launch(void* const* d_in, const int* in_sizes, int n_in, void* d_out, int out_size, void* d_ws, size_t ws_size,
                              hipStream_t stream) {
  Params p;
  p.x = d_in[0]; p.kc = d_in[1]; p.vc = d_in[2]; p.wq = d_in[3]; p.wo = d_in[4];
  p.w1 = d_in[6]; p.b1 = d_in[7]; p.w2 = d_in[8]; p.b2 = d_in[9];
  p.out = d_out; p.ws = d_ws;
  hipLaunchKernelGGL(fused_decode, dim3(NWG), dim3(NTHR), 0, stream, p);
}
```

```cpp
#define FL1 1
#define STAMPS 1
#define DIAG 0
#define DIAGW 6
#define DIAGB 0
#include <hip/hip_runtime.h>
#include <hip/hip_bf16.h>
#include <stdint.h>

#define NPRE 6
#define CODEPF 1
#define TLBPF 0
#define WIN 5
#define WINW 6
#define FLPOS 28
#define RAMP 4
#define WINV 6
#define WINT 6
#define QKPIN 1
#define ABL 0
#define WINK WIN
#define OSLEEP 1
#define PZPOS 0
#define ODELAY 0
namespace {
typedef unsigned v4u __attribute__((ext_vector_type(4)));
typedef unsigned v2u __attribute__((ext_vector_type(2)));
typedef float f32x4 __attribute__((ext_vector_type(4)));
typedef short bf16x8 __attribute__((ext_vector_type(8)));
typedef __bf16 bf16x2 __attribute__((ext_vector_type(2)));
typedef float f32x2 __attribute__((ext_vector_type(2)));
typedef __amdgpu_buffer_rsrc_t rsrc_t;
#define GAS __attribute__((address_space(1)))
#define LAS __attribute__((address_space(3)))

constexpr int NWG = 256, NTHR = 512;
constexpr int DM = 1024, NQ = 4096, FF = 3584;
constexpr int WS_E = 0;
constexpr int WS_FLAG = 4096;
constexpr int WS_Q = 8192;
constexpr int WS_H = WS_Q + 4 * 32 * 4 * 512;
constexpr int WS_O = WS_H + 4 * 4 * FF * 4;
constexpr int WS_ML = WS_O + 32 * 4 * 16 * 256;
constexpr int WS_F = WS_ML + 32 * 4 * 16 * 8;
constexpr int WS_END = WS_F + 256 * 32 * 64;
constexpr int WS_STAMP = 8 << 20;
constexpr int OOB = 0x7f000000;
constexpr int AUX_NT = 2, AUX_SC1 = 16;
constexpr unsigned POISON = 0u;
constexpr int SPIN_CAP = 1 << 14;
constexpr int L_CTR = 0;
constexpr int L_XN = 64;
constexpr int L_Q = L_XN + 2048;
constexpr int L_M = L_Q + 2048;
constexpr int L_L = L_M + 256;
constexpr int L_RED = L_L + 256;
constexpr int RED_W = 8 * 18 * 16;
constexpr int L_O = L_RED + 4 * RED_W;
constexpr int O_W = 8 * 132 * 4;
constexpr int L_H = L_O + 5 * O_W;
constexpr int L_OA = L_H + 1024;
constexpr int L_END = L_OA + 1024;

struct Params {
  const void *x, *kc, *vc, *wq, *wo, *w1, *b1, *w2, *b2;
  void *out, *ws;
};

__device__ __forceinline__ rsrc_t mk(const void* p, int bytes) { return __builtin_amdgcn_make_buffer_rsrc((void*)p, 0, bytes, 0x00020000); }
template <int AUX> __device__ __forceinline__ v4u ld16(rsrc_t r, int voff, int soff) { return __builtin_amdgcn_raw_buffer_load_b128(r, voff, soff, AUX); }
template <int AUX> __device__ __forceinline__ v2u ld8(rsrc_t r, int voff, int soff) { return __builtin_amdgcn_raw_buffer_load_b64(r, voff, soff, AUX); }
__device__ __forceinline__ void st16(v4u v, rsrc_t r, int voff, int soff) { __builtin_amdgcn_raw_buffer_store_b128(v, r, voff, soff, AUX_SC1); }
__device__ __forceinline__ void st8(v2u v, rsrc_t r, int voff, int soff) { __builtin_amdgcn_raw_buffer_store_b64(v, r, voff, soff, AUX_SC1); }
__device__ __forceinline__ f32x4 mfma(v4u a, v4u b, f32x4 c) {
  return __builtin_amdgcn_mfma_f32_16x16x32_bf16(__builtin_bit_cast(bf16x8, a), __builtin_bit_cast(bf16x8, b), c, 0, 0, 0);
}
__device__ __forceinline__ float u2f(unsigned u) { return __builtin_bit_cast(float, u); }
__device__ __forceinline__ unsigned f2u(float f) { return __builtin_bit_cast(unsigned, f); }
__device__ __forceinline__ unsigned pk(float lo, float hi) {
  f32x2 v = {lo, hi};
  return __builtin_bit_cast(unsigned, __builtin_convertvector(v, bf16x2));
}
__device__ __forceinline__ float blo(unsigned u) { return u2f(u << 16); }
__device__ __forceinline__ float bhi(unsigned u) { return u2f(u & 0xffff0000u); }
__device__ __forceinline__ float rbf(float f) { return blo(pk(f, f)); }
template <int N> __device__ __forceinline__ float ror(float x) { return u2f(__builtin_amdgcn_mov_dpp(f2u(x), 0x120 + N, 0xf, 0xf, true)); }
template <int N> __device__ __forceinline__ unsigned roru(unsigned x) { return __builtin_amdgcn_mov_dpp(x, 0x120 + N, 0xf, 0xf, true); }
__device__ __forceinline__ float xrow_sum(float x) {
  auto s = __builtin_amdgcn_permlane16_swap(f2u(x), f2u(x), false, false);
  x = u2f(s[0]) + u2f(s[1]);
  auto t = __builtin_amdgcn_permlane32_swap(f2u(x), f2u(x), false, false);
  return u2f(t[0]) + u2f(t[1]);
}
__device__ __forceinline__ float xrow_max(float x) {
  auto s = __builtin_amdgcn_permlane16_swap(f2u(x), f2u(x), false, false);
  x = fmaxf(u2f(s[0]), u2f(s[1]));
  auto t = __builtin_amdgcn_permlane32_swap(f2u(x), f2u(x), false, false);
  return fmaxf(u2f(t[0]), u2f(t[1]));
}
__device__ __forceinline__ float wave_sum(float x) {
  x += ror<8>(x); x += ror<4>(x); x += ror<2>(x); x += ror<1>(x);
  return xrow_sum(x);
}
__device__ __forceinline__ unsigned umin4(v4u v) { return min(min(v.x, v.y), min(v.z, v.w)); }
__device__ __forceinline__ v4u nopoison(f32x4 v) {
  v4u u = __builtin_bit_cast(v4u, v);
  return v4u{max(u.x, 1u), max(u.y, 1u), max(u.z, 1u), max(u.w, 1u)};
}
#define PIN() __builtin_amdgcn_sched_barrier(0)
#define BAR() do { PIN(); asm volatile("s_waitcnt lgkmcnt(0)\n\ts_barrier" ::: "memory"); PIN(); } while (0)
#define REREAD() asm volatile("" ::: "memory")
__device__ __forceinline__ v4u tr8(const v4u* w, int j0) {
  const unsigned sel = (j0 & 1) ? 0x07060302u : 0x05040100u;
  const int c = j0 >> 1;
  return v4u{__builtin_amdgcn_perm(w[1][c], w[0][c], sel), __builtin_amdgcn_perm(w[3][c], w[2][c], sel),
             __builtin_amdgcn_perm(w[5][c], w[4][c], sel), __builtin_amdgcn_perm(w[7][c], w[6][c], sel)};
}
__device__ __forceinline__ void ln_stats(const float (&v)[16], float& m, float& inv) {
  float s = 0.f;
#pragma unroll
  for (int i = 0; i < 16; ++i) s += v[i];
  const float mf = wave_sum(s) * (1.0f / DM);
  float ss = 0.f;
#pragma unroll
  for (int i = 0; i < 16; ++i) { const float d = v[i] - mf; ss = fmaf(d, d, ss); }
  const float var = rbf(wave_sum(ss) * (1.0f / DM));
  m = rbf(mf);
  inv = __builtin_amdgcn_rcpf(rbf(__builtin_amdgcn_sqrtf(rbf(var + 1e-5f))));
}
__device__ __forceinline__ unsigned ln_pair(float a, float b, float m, float inv) {
  const unsigned d = pk(a - m, b - m);
  return pk(blo(d) * inv, bhi(d) * inv);
}
__device__ __forceinline__ unsigned lds_arrive(LAS unsigned* c, int lane) {
  unsigned old = 0;
  REREAD();
  if (lane == 0) old = __hip_atomic_fetch_add(c, 1u, __ATOMIC_RELAXED, __HIP_MEMORY_SCOPE_WORKGROUP);
  return __builtin_amdgcn_readfirstlane(old);
}
__device__ __forceinline__ void class_bar(LAS unsigned* c, int lane) {
  PIN();
  if (lds_arrive(c, lane) != 3u) {
    _Pragma("nounroll") while (__builtin_amdgcn_readfirstlane(__hip_atomic_load(c, __ATOMIC_RELAXED, __HIP_MEMORY_SCOPE_WORKGROUP)) < 4u)
      __builtin_amdgcn_s_sleep(1);
  }
  REREAD();
  PIN();
}
template <int N> __device__ __forceinline__ void pace() { asm volatile("s_waitcnt vmcnt(%0)" :: "n"(N) : "memory"); }

#define STAMP(i) do { PIN(); ts_[i] = (unsigned)__builtin_amdgcn_s_memrealtime(); PIN(); } while (0)
#define STAMPV(i, v) do { ts_[i] = (unsigned)(v); } while (0)
#define STAMP_FLUSH() do { if (lane == 0) { _Pragma("unroll") for (int i_ = 0; i_ < 20; ++i_) ((GAS unsigned long long*)((GAS char*)p.ws + WS_STAMP))[(blockIdx.x * 8 + w) * 20 + i_] = ts_[i_]; } } while (0)
#undef STAMP_FLUSH
#define STAMP_FLUSH() do { if (w == DIAGW && (DIAGB < 0 || (int)blockIdx.x == DIAGB)) { const unsigned lim_ = 6000u + 4u * (ts_[DIAG] - ts_[0]); \
    while ((unsigned)__builtin_amdgcn_s_memrealtime() - ts_[0] < lim_ && lim_ < 100000u) __builtin_amdgcn_s_sleep(1); } } while (0)

__global__ void __launch_bounds__(NTHR, 2) fused_decode(Params p) {
  __shared__ __attribute__((aligned(16))) unsigned char smem[L_END];
  LAS unsigned char* L = (LAS unsigned char*)smem;
  LAS unsigned* ctr = (LAS unsigned*)(L + L_CTR);
  const int tid = threadIdx.x, lane = tid & 63;
  const int w = __builtin_amdgcn_readfirstlane(tid >> 6);
  const int wl = w & 3;
  const bool isA = w < 4;
  const int xs = blockIdx.x & 7, ci = blockIdx.x >> 3;
  const int n16 = lane & 15, g = lane >> 4;
  unsigned ts_[20] = {0, 0, 0, 0, 0, 0, 0, 0, 0, 0, 0, 0, 0, 0, 0, 0, 0, 0, 0, 0};
  STAMP(0);
  unsigned cpf = 0, tpf = 0;
  {
    unsigned long long pc; int len;
    asm volatile("s_getpc_b64 %0\n.Lpf_base%=:\n\ts_mov_b32 %1, .Lfd_code_end-.Lpf_base%=" : "=s"(pc), "=s"(len));
    if (tid < 5) cpf = __builtin_amdgcn_raw_buffer_load_b32(mk((const void*)pc, len), (tid * 32 + ci) * 128, 0, 0);
  }
  PIN();
  asm volatile("" :: "s"(p.x), "s"(p.kc), "s"(p.vc), "s"(p.wq), "s"(p.wo), "s"(p.w1), "s"(p.b1), "s"(p.w2), "s"(p.b2), "s"(p.out), "s"(p.ws));
  v2u xr[4];
  if (isA) {
    const rsrc_t rx = mk(p.x, 4 * DM * 2);
#pragma unroll
    for (int i = 0; i < 4; ++i) xr[i] = ld8<0>(rx, lane * 8, wl * 2048 + ((i + (xs >> 1)) & 3) * 512);
  }
  PIN();
  const rsrc_t rws = mk(p.ws, WS_END);
  const int gidx = 2 * xs + (ci >> 4), ab = gidx >> 2, kvh = gidx & 3, sp = ci & 15;
  const int kq = xs >> 1, isq = ci < 16, cb = isq ? 16 * (xs & 1) + ci : 14 * (xs & 1) + ci - 16;
  const int hd = 4 * xs + (ci >> 3), cg = ci & 7;
  const int cu = xs * 32 + ci;
  const size_t kvbase = ((size_t)gidx * 16 + sp) * 262144;
  unsigned long long gen = *(const __attribute__((address_space(4))) unsigned long long*)p.ws;
#define MAKE_TAG() const unsigned long long tag64 = (gen + 1ull) * 0x9E3779B97F4A7C15ull; \
  const unsigned tag0 = (unsigned)tag64, tag1 = (unsigned)(tag64 >> 32)

  const int hc = lane & 7, hbt = (lane >> 3) & 3, hb = 112 * hd + 28 * wl + 4 * hc;
  const int fb = (lane >> 2) & 3, fc = 4 * cu + (lane & 3);
  v2u sm;
  if (isA) {
    if (PZPOS == 1) {
      const v4u pz = {POISON, POISON, POISON, POISON};
      st16(pz, rws, wl < 2 ? WS_O + (((8 * kvh + 4 * (wl & 1) + g) * 4 + ab) * 16 + sp) * 256 + n16 * 16
                           : WS_F + ((32 * cg + 16 * (wl & 1) + (lane >> 2)) * 32 + hd) * 64 + (lane & 3) * 16, 0);
      if (wl == 0) st8(v2u{POISON, POISON}, rws, lane < 8 ? WS_ML + (((8 * kvh + lane) * 4 + ab) * 16 + sp) * 8 : OOB, 0);
      PIN();
    }
    if (tid < 8) ctr[tid] = 0u;
    PIN();
    float v[16];
#pragma unroll
    for (int i = 0; i < 8; ++i) { v[2 * i] = blo(xr[i >> 1][i & 1]); v[2 * i + 1] = bhi(xr[i >> 1][i & 1]); }
    asm volatile("" : "+v"(v[0]), "+v"(v[15]));
    STAMP(1);
    float m, inv;
    ln_stats(v, m, inv);
    if (!isq) {
#pragma unroll
      for (int i = 0; i < 16; i += 2) { const unsigned o = ln_pair(v[i], v[i + 1], m, inv); v[i] = blo(o); v[i + 1] = bhi(o); }
      ln_stats(v, m, inv);
    }
    *(LAS v2u*)(L + L_XN + wl * 512 + lane * 8) = v2u{ln_pair(v[0], v[1], m, inv), ln_pair(v[2], v[3], m, inv)};
    asm volatile("" : "+s"(gen));
    if (PZPOS == 2) {
      const v4u pz = {POISON, POISON, POISON, POISON};
      st16(pz, rws, wl < 2 ? WS_O + (((8 * kvh + 4 * (wl & 1) + g) * 4 + ab) * 16 + sp) * 256 + n16 * 16
                           : WS_F + ((32 * cg + 16 * (wl & 1) + (lane >> 2)) * 32 + hd) * 64 + (lane & 3) * 16, 0);
      if (wl == 0) st8(v2u{POISON, POISON}, rws, lane < 8 ? WS_ML + (((8 * kvh + lane) * 4 + ab) * 16 + sp) * 8 : OOB, 0);
      PIN();
    }
    STAMP(2);
    if (PZPOS) pace<0>();
    BAR();
    asm volatile("" :: "v"(cpf), "v"(tpf));
  } else {
    const int ldw = isq ? NQ * 2 : FF * 2;
    const rsrc_t rw = mk(isq ? p.wq : p.w1, DM * ldw);
    const int wo_ = ci < 30 ? (256 * kq + 64 * wl + 8 * g) * ldw + cb * 256 + n16 * 16 : OOB;
    v4u wv[16];
#pragma unroll
    for (int j = 0; j < 16; ++j) {
      if (j >= WINW) pace<WINW - 1>();
      wv[j] = ld16<AUX_NT>(rw, wo_, ((j >> 3) * 32 + (j & 7)) * ldw);
      PIN();
    }
    asm volatile("" : "+s"(gen));
    STAMP(1);
    BAR();
    STAMP(2);
    f32x4 d[8];
#pragma unroll
    for (int t = 0; t < 2; ++t) {
      const v4u act = *(const LAS v4u*)(L + L_XN + (n16 & 3) * 512 + (64 * wl + 32 * t + 8 * g) * 2);
#pragma unroll
      for (int j0 = 0; j0 < 8; ++j0) d[j0] = mfma(act, tr8(wv + 8 * t, j0), t ? d[j0] : f32x4{0.f, 0.f, 0.f, 0.f});
      PIN();
    }
    if (lane < 16) {
#pragma unroll
      for (int j0 = 0; j0 < 8; ++j0) *(LAS f32x4*)(L + L_RED + wl * RED_W + (j0 * 17 + lane) * 16) = d[j0];
    }
    STAMP(3);
    class_bar(ctr + 0, lane);
    if (wl < 2) {
      const int b = lane & 3, jh = (lane >> 2) & 1, nn = wl * 8 + (lane >> 3);
      f32x4 sum = {0.f, 0.f, 0.f, 0.f};
#pragma unroll
      for (int ww = 0; ww < 4; ++ww)
#pragma unroll
        for (int k = 0; k < 4; ++k) sum[k] += *(const LAS float*)(L + L_RED + ww * RED_W + ((4 * jh + k) * 17 + nn) * 16 + b * 4);
      const int co = (8 * nn + 4 * jh) * 4;
      const int off = isq ? WS_Q + ((b * 32 + cb) * 4 + kq) * 512 + co : (ci < 30 ? WS_H + ((kq * 4 + b) * FF + cb * 128) * 4 + co : OOB);
      st16(__builtin_bit_cast(v4u, sum), rws, (ABL & 4) ? OOB : off, 0);
    } else if (!(ABL & 1) && !PZPOS) {
      const v4u pz = {POISON, POISON, POISON, POISON};
      st16(pz, rws, WS_O + (((8 * kvh + 4 * (wl & 1) + g) * 4 + ab) * 16 + sp) * 256 + n16 * 16, 0);
      st16(pz, rws, WS_F + ((32 * cg + 16 * (wl & 1) + (lane >> 2)) * 32 + hd) * 64 + (lane & 3) * 16, 0);
      st8(v2u{POISON, POISON}, rws, lane < 4 ? WS_ML + (((8 * kvh + 4 * (wl & 1) + lane) * 4 + ab) * 16 + sp) * 8 : OOB, 0);
    }
    STAMP(4);
    sm.x = __builtin_amdgcn_raw_buffer_load_b16(mk(p.x, 4 * DM * 2), wl == 2 && !(ABL & 2) ? (fb * DM + fc) * 2 : OOB, 0, 0);
    sm.y = __builtin_amdgcn_raw_buffer_load_b16(mk(p.b2, DM * 2), wl == 2 && !(ABL & 2) ? fc * 2 : OOB, 0, 0);
    pace<0>();
    if (lds_arrive(ctr + 1, lane) == 3u) {
      MAKE_TAG();
      st8(v2u{tag0, tag1}, rws, lane == 0 && !(ABL & 4) ? WS_FLAG + cu * 8 : OOB, 0);
    }
    STAMP(5);
    BAR();
  }
  const rsrc_t rk = mk((const char*)p.kc + kvbase, 262144), rv = mk((const char*)p.vc + kvbase, 262144);
  const int koff = (w >> 2) * 131072 + wl * 4096 + ((lane >> 2) & 3) * 256 + g * 64 + (lane & 3) * 16;
  const int voff = (w >> 2) * 131072 + wl * 4096 + lane * 16;
  v4u r[32], fa_, fb_;
  __builtin_amdgcn_s_setprio(1);
#pragma unroll
  for (int i = 0; i < 32; ++i) {
    if (i == FLPOS) {
      fa_ = ld16<AUX_SC1>(rws, isA && (!FL1 || w == 0) ? WS_FLAG + lane * 16 : OOB, 0); fb_ = ld16<AUX_SC1>(rws, isA && (!FL1 || w == 0) ? WS_FLAG + lane * 16 : OOB, 1024);
      PIN();
    }
    if (i >= FLPOS && i < FLPOS + WINK) pace<WINK + 1>();
    else if (i >= WINK) pace<WINK - 1>();
    else if (i > 0 && RAMP) __builtin_amdgcn_s_sleep(RAMP);
    r[i] = ld16<AUX_NT>(rk, koff, (i >> 2) * 16384 + (i & 3) * 1024);
    PIN();
  }
  __builtin_amdgcn_s_setprio(0);
  STAMP(6);
  BAR();
  if (isA) {
    MAKE_TAG();
    int spins = 0;
#define FLAGS_BAD(a, b) __any((((a).x ^ tag0) | ((a).y ^ tag1) | ((a).z ^ tag0) | ((a).w ^ tag1) | ((b).x ^ tag0) | ((b).y ^ tag1) | ((b).z ^ tag0) | ((b).w ^ tag1)) != 0u)
    if (!(ABL & 4) && (!FL1 || w == 0) && FLAGS_BAD(fa_, fb_)) {
      _Pragma("nounroll") while (true) {
        REREAD();
        const v4u a = ld16<AUX_SC1>(rws, WS_FLAG + lane * 16, 0), b = ld16<AUX_SC1>(rws, WS_FLAG + lane * 16, 1024);
        if (++spins > SPIN_CAP || !FLAGS_BAD(a, b)) break;
        __builtin_amdgcn_s_sleep(4);
      }
    }
    REREAD();
    if (FL1) class_bar(ctr + 5, lane);
    STAMPV(18, spins);
    STAMP(3);
    if (blockIdx.x == 0 && w == 0) st8(v2u{(unsigned)(gen + 1ull), (unsigned)((gen + 1ull) >> 32)}, rws, lane == 0 ? WS_E : OOB, 0);
    const int qo = WS_Q + ((ab * 32 + 8 * kvh + 2 * wl + (lane >> 5)) * 4) * 512 + (lane & 31) * 16;
    v4u qp[4];
#pragma unroll
    for (int s = 0; s < 4; ++s) qp[s] = ld16<AUX_SC1>(rws, (ABL & 8) ? OOB : qo, s * 512);
    PIN();
    const f32x4 qs = (__builtin_bit_cast(f32x4, qp[0]) + __builtin_bit_cast(f32x4, qp[1])) + (__builtin_bit_cast(f32x4, qp[2]) + __builtin_bit_cast(f32x4, qp[3]));
    *(LAS v2u*)(L + L_Q + (2 * wl + (lane >> 5)) * 256 + (lane & 31) * 8) = v2u{pk(qs[0], qs[1]), pk(qs[2], qs[3])};
    class_bar(ctr + 2, lane);
    STAMP(4);
  }
  const int hl = n16 & 7, wb = n16 >> 3;
  const v4u qb0 = *(const LAS v4u*)(L + L_Q + hl * 256 + (4 * g + wb) * 16);
  const v4u qb1 = *(const LAS v4u*)(L + L_Q + hl * 256 + (4 * g + 2 + wb) * 16);
  float s[16];
#pragma unroll
  for (int u = 0; u < 8; ++u) {
    float t0[4], t1[4];
#pragma unroll
    for (int k = 0; k < 4; ++k) {
      const f32x4 d0 = mfma(r[4 * u + k], qb0, f32x4{0.f, 0.f, 0.f, 0.f});
      const f32x4 d1 = mfma(r[4 * u + k], qb1, f32x4{0.f, 0.f, 0.f, 0.f});
      t0[k] = d0[0] + d1[2];
      t1[k] = d0[1] + d1[3];
    }
#pragma unroll
    for (int k = 0; k < 4; k += 2) {
      const float xx = wb ? t1[k + 1] : t0[k], yy = wb ? t1[k] : t0[k + 1];
      s[2 * u + (k >> 1)] = xx + ror<8>(yy);
    }
    if (QKPIN) PIN();
  }
  float m = -INFINITY;
#pragma unroll
  for (int i = 0; i < 16; i += 2) {
    const unsigned a = pk(s[i], s[i + 1]);
    const unsigned c = pk(blo(a) * 0.08838834764831845f, bhi(a) * 0.08838834764831845f);
    s[i] = blo(c); s[i + 1] = bhi(c);
    m = fmaxf(m, fmaxf(s[i], s[i + 1]));
  }
  m = fmaxf(m, ror<8>(m));
  m = xrow_max(m);
  if (lane < 8) *(LAS float*)(L + L_M + (w * 8 + lane) * 4) = m;
  constexpr float LOG2E = 1.4426950408889634f;
  if (!isA) {
    class_bar(ctr + 3, lane);
#pragma unroll
    for (int ww = 0; ww < 8; ++ww) m = fmaxf(m, *(const LAS float*)(L + L_M + (ww * 8 + hl) * 4));
  }
  unsigned pb[16];
  float lsum = 0.f;
  {
    const unsigned msk = wb ? 0xffff0000u : 0x0000ffffu;
#pragma unroll
    for (int i = 0; i < 16; ++i) {
      const float e = __builtin_amdgcn_exp2f((s[i] - m) * LOG2E);
      lsum += e;
      const unsigned t = pk(e, e) & msk;
      pb[i] = t | roru<8>(t);
      asm volatile("" : "+v"(pb[i]));
    }
    lsum += ror<8>(lsum);
    lsum = xrow_sum(lsum);
    if (lane < 8) *(LAS float*)(L + L_L + (w * 8 + lane) * 4) = lsum;
  }
  f32x4 acc[8];
#pragma unroll
  for (int j = 0; j < 8; ++j) acc[j] = f32x4{0.f, 0.f, 0.f, 0.f};
  STAMP(7);
  BAR();
  STAMP(15);
#define PV(n) acc[(n) & 7] = mfma(tr8(&r[(n) & ~7], (n) & 7), v4u{pb[((n) >> 3) * 4], pb[((n) >> 3) * 4 + 1], pb[((n) >> 3) * 4 + 2], pb[((n) >> 3) * 4 + 3]}, acc[(n) & 7])
  __builtin_amdgcn_s_setprio(1);
#pragma unroll
  for (int i = 0; i < 32; ++i) {
    if (i >= WINV) pace<WINV - 1>();
    else if (i > 0 && RAMP) __builtin_amdgcn_s_sleep(RAMP);
    r[i] = ld16<AUX_NT>(rv, voff, (i >> 2) * 16384 + (i & 3) * 1024);
    PIN();
    if (i >= 7 + WINV) { PV(i - 7 - WINV); PIN(); }
  }
  const int mo = WS_ML + ((hd * 4 + wl) * 16 + ((n16 + 4 * g) & 15)) * 8;
  v2u ml = ld8<AUX_SC1>(rws, isA ? OOB : mo, 0);
  STAMP(16);
  BAR();
#pragma unroll
  for (int n = 32 - 7 - WINV; n < 32; ++n) { PV(n); if ((n & 7) == 7) PIN(); }
  STAMP(8);
#define PUT_ACC(f) _Pragma("unroll") for (int i = 0; i < 4; ++i) { \
      const f32x4 t = {wb ? acc[4][i] : acc[0][i], wb ? acc[5][i] : acc[1][i], wb ? acc[6][i] : acc[2][i], wb ? acc[7][i] : acc[3][i]}; \
      *(LAS f32x4*)(L + L_O + wl * O_W + (hl * 132 + 8 * (4 * g + i) + 4 * wb) * 4) = f t; }
  const int oro = ((2 * wl + (lane >> 5)) * 132 + 4 * (lane & 31)) * 4;
  if (isA) {
    __builtin_amdgcn_s_setprio(0);
    float M = m;
#pragma unroll
    for (int ww = 0; ww < 8; ++ww) M = fmaxf(M, *(const LAS float*)(L + L_M + (ww * 8 + hl) * 4));
    const float fa = __builtin_amdgcn_exp2f((m - M) * LOG2E);
    PUT_ACC(fa *);
    if (lane < 8) *(LAS float*)(L + L_L + (w * 8 + lane) * 4) = fa * lsum;
    class_bar(ctr + 4, lane);
    {
      f32x4 t = *(const LAS f32x4*)(L + L_O + oro);
#pragma unroll
      for (int ww = 1; ww < 4; ++ww) t += *(const LAS f32x4*)(L + L_O + ww * O_W + oro);
      *(LAS f32x4*)(L + L_O + 4 * O_W + oro) = t;
    }
    if (w == 0) {
      float l = 0.f;
#pragma unroll
      for (int ww = 0; ww < 8; ++ww) l += *(const LAS float*)(L + L_L + (ww * 8 + hl) * 4);
      st8(v2u{max(f2u(M), 1u), max(f2u(l), 1u)}, rws, lane < 8 ? WS_ML + (((8 * kvh + lane) * 4 + ab) * 16 + sp) * 8 : OOB, 0);
    }
    {
    const int ho = hc < 7 ? WS_H + ((8 * (lane >> 5) + hbt) * FF + hb) * 4 : OOB;
    v4u hp[2];
#pragma unroll
    for (int s = 0; s < 2; ++s) hp[s] = ld16<AUX_SC1>(rws, (ABL & 8) ? OOB : ho, s * 4 * FF * 4);
    sm = ld8<0>(mk(p.b1, FF * 2), hc < 7 && !(ABL & 2) ? hb * 2 : OOB, 0);
    PIN();
    float hv[4];
#pragma unroll
    for (int j = 0; j < 4; ++j) {
      const float t0 = u2f(hp[0][j]) + u2f(hp[1][j]);
      auto sw = __builtin_amdgcn_permlane32_swap(f2u(t0), f2u(t0), false, false);
      const float t = u2f(sw[0]) + u2f(sw[1]);
      const float u = rbf(rbf(t) + ((j & 1) ? bhi(sm[j >> 1]) : blo(sm[j >> 1])));
      const float z2 = u * fmaf(u * u, 0.044715f, 1.0f) * (-2.0f * 0.7978845608028654f * 1.4426950408889634f);
      hv[j] = u * __builtin_amdgcn_rcpf(1.0f + __builtin_amdgcn_exp2f(z2));
    }
    if (lane < 32) *(LAS v2u*)(L + L_H + wl * 256 + hbt * 64 + hc * 8) = v2u{pk(hv[0], hv[1]), pk(hv[2], hv[3])};
    }
    const rsrc_t r2 = mk(p.w2, FF * DM * 2), ro = mk(p.wo, NQ * DM * 2);
    const int o2 = (112 * hd + 28 * wl + 8 * g) * 2048 + cg * 256 + n16 * 16;
    const int o2b = g < 3 ? o2 : OOB;
    const int oo = (128 * hd + 32 * wl + 8 * g) * 2048 + cg * 256 + n16 * 16;
    const v4u hact = *(const LAS v4u*)(L + L_H + wl * 256 + (n16 & 3) * 64 + g * 16);
    v4u tw[16];
#define TLD(j) do { if ((j) >= WINT) pace<WINT - 1>(); else if ((j) > 0 && (j) < NPRE && RAMP) __builtin_amdgcn_s_sleep(RAMP); tw[j] = (j) < 8 ? ld16<AUX_NT>(r2, (j) < 4 ? o2 : o2b, (j) * 2048) : ld16<AUX_NT>(ro, oo, ((j) - 8) * 2048); PIN(); } while (0)
    STAMP(5);
    BAR();
#pragma unroll
    for (int j = 0; j < NPRE; ++j) TLD(j);
    STAMP(9);
    BAR();
#pragma unroll
    for (int j = NPRE; j < 16; ++j) TLD(j);
    f32x4 d[8];
#pragma unroll
    for (int j0 = 0; j0 < 8; ++j0) d[j0] = mfma(hact, tr8(tw, j0), f32x4{0.f, 0.f, 0.f, 0.f});
    v4u wt[8];
#pragma unroll
    for (int j0 = 0; j0 < 8; ++j0) { wt[j0] = tr8(tw + 8, j0); asm volatile("" : "+v"(wt[j0])); }
    STAMP(10);
    BAR();
    STAMP(11);
    const v4u oact = *(const LAS v4u*)(L + L_OA + (n16 & 3) * 256 + (32 * wl + 8 * g) * 2);
#pragma unroll
    for (int j0 = 0; j0 < 8; ++j0) d[j0] = mfma(oact, wt[j0], d[j0]);
    if (lane < 16) {
#pragma unroll
      for (int j0 = 0; j0 < 8; ++j0) *(LAS f32x4*)(L + L_RED + wl * RED_W + (j0 * 18 + lane) * 16) = d[j0];
    }
    BAR();
    STAMP(12);
    asm volatile(".Lfd_code_end:");
  } else {
    PUT_ACC();
    BAR();
    STAMP(9);
    {
      f32x4 t = *(const LAS f32x4*)(L + L_O + 4 * O_W + oro);
#pragma unroll
      for (int ww = 0; ww < 4; ++ww) t += *(const LAS f32x4*)(L + L_O + ww * O_W + oro);
      st8(v2u{max(pk(t[0], t[1]), 1u), max(pk(t[2], t[3]), 1u)}, rws, WS_O + (((8 * kvh + 2 * wl + (lane >> 5)) * 4 + ab) * 16 + sp) * 256 + (lane & 31) * 8, 0);
    }
    PIN();
    STAMP(10);
    __builtin_amdgcn_s_setprio(0);
    {
      int spins = 0;
      if (__any(min(ml.x, ml.y) == POISON)) {
        _Pragma("nounroll") while (true) {
          REREAD();
          ml = ld8<AUX_SC1>(rws, mo, 0);
          if (!__any(min(ml.x, ml.y) == POISON) || ++spins > SPIN_CAP) break;
          __builtin_amdgcn_s_sleep(2);
        }
      }
      STAMPV(19, spins);
      float M = u2f(ml.x);
      M = fmaxf(M, ror<8>(M)); M = fmaxf(M, ror<4>(M)); M = fmaxf(M, ror<2>(M)); M = fmaxf(M, ror<1>(M));
      const float e = __builtin_amdgcn_exp2f((u2f(ml.x) - M) * LOG2E);
      float Ls = e * u2f(ml.y);
      Ls += ror<8>(Ls); Ls += ror<4>(Ls); Ls += ror<2>(Ls); Ls += ror<1>(Ls);
      float f = e * __builtin_amdgcn_rcpf(Ls);
      asm volatile("" : "+v"(f));
      if (ODELAY) __builtin_amdgcn_s_sleep(ODELAY);
      const int oo_ = WS_O + ((hd * 4 + wl) * 16 + 4 * g) * 256 + n16 * 16;
      v4u op[4];
      int of[4];
#pragma unroll
      for (int k = 0; k < 4; ++k) { op[k] = v4u{0u, 0u, 0u, 0u}; of[k] = oo_; }
      spins = 0;
      _Pragma("nounroll") while (true) {
        REREAD();
        v4u nw[4];
#pragma unroll
        for (int k = 0; k < 4; ++k) nw[k] = ld16<AUX_SC1>(rws, of[k], k * 256);
        PIN();
        bool bad = false;
#pragma unroll
        for (int k = 0; k < 4; ++k) {
          op[k] |= nw[k];
          const bool b = umin4(op[k]) == POISON;
          of[k] = b ? oo_ : OOB;
          bad |= b;
        }
        if (!__any(bad) || ++spins > SPIN_CAP) break;
        __builtin_amdgcn_s_sleep(OSLEEP);
      }
      STAMPV(18, spins);
      STAMP(13);
      float t[8] = {0.f, 0.f, 0.f, 0.f, 0.f, 0.f, 0.f, 0.f};
#define OACC(k) do { const float fk = u2f(__builtin_amdgcn_mov_dpp(f2u(f), 0x150 + (k), 0xf, 0xf, true)); \
        _Pragma("unroll") for (int j = 0; j < 4; ++j) { t[2 * j] = fmaf(fk, blo(op[k][j]), t[2 * j]); t[2 * j + 1] = fmaf(fk, bhi(op[k][j]), t[2 * j + 1]); } } while (0)
      OACC(0); OACC(1); OACC(2); OACC(3);
      float u[4];
#pragma unroll
      for (int j = 0; j < 4; ++j) { auto sw = __builtin_amdgcn_permlane32_swap(f2u(t[j]), f2u(t[4 + j]), false, false); u[j] = u2f(sw[0]) + u2f(sw[1]); }
      auto s0 = __builtin_amdgcn_permlane16_swap(f2u(u[0]), f2u(u[2]), false, false);
      auto s1 = __builtin_amdgcn_permlane16_swap(f2u(u[1]), f2u(u[3]), false, false);
      *(LAS unsigned*)(L + L_OA + wl * 256 + n16 * 16 + g * 4) = pk(u2f(s0[0]) + u2f(s0[1]), u2f(s1[0]) + u2f(s1[1]));
      if (wl < 2) __builtin_amdgcn_s_setprio(2);
    }
    BAR();
    BAR();
    if (wl < 2) {
      const int j0 = lane & 7, nn = wl * 8 + (lane >> 3);
      f32x4 t = *(const LAS f32x4*)(L + L_RED + (j0 * 18 + nn) * 16);
#pragma unroll
      for (int ww = 1; ww < 4; ++ww) t += *(const LAS f32x4*)(L + L_RED + ww * RED_W + (j0 * 18 + nn) * 16);
      st16(nopoison(t), rws, WS_F + ((32 * cg + 2 * nn + (j0 >> 2)) * 32 + hd) * 64 + (j0 & 3) * 16, 0);
      STAMP(11);
    } else if (wl == 2) {
      v4u f0, f1;
      int spins = 0;
      _Pragma("nounroll") while (true) {
        REREAD();
        f0 = ld16<AUX_SC1>(rws, WS_F + cu * 2048 + lane * 16, 0);
        f1 = ld16<AUX_SC1>(rws, WS_F + cu * 2048 + lane * 16, 1024);
        if (!__any(min(umin4(f0), umin4(f1)) == POISON) || ++spins > SPIN_CAP) break;
        __builtin_amdgcn_s_sleep(1);
      }
      STAMPV(17, spins);
      STAMP(11);
      f32x4 t = __builtin_bit_cast(f32x4, f0) + __builtin_bit_cast(f32x4, f1);
#pragma unroll
      for (int k = 0; k < 4; ++k) { float y = t[k]; y += ror<4>(y); y += ror<8>(y); t[k] = xrow_sum(y); }
      const float y = (fb == 0 ? t[0] : fb == 1 ? t[1] : fb == 2 ? t[2] : t[3]) + blo(sm.x) + blo(sm.y);
      __builtin_amdgcn_raw_buffer_store_b16((unsigned short)pk(y, y), mk(p.out, 4 * DM * 2), lane < 16 ? (fb * DM + fc) * 2 : OOB, 0, 0);
      STAMP(12);
    }
  }
  STAMP_FLUSH();
}
}

extern "C" void kernel_launch(void* const* d_in, const int* in_sizes, int n_in, void* d_out, int out_size, void* d_ws, size_t ws_size,
                              hipStream_t stream) {
  Params p;
  p.x = d_in[0]; p.kc = d_in[1]; p.vc = d_in[2]; p.wq = d_in[3]; p.wo = d_in[4];
  p.w1 = d_in[6]; p.b1 = d_in[7]; p.w2 = d_in[8]; p.b2 = d_in[9];
  p.out = d_out; p.ws = d_ws;
  hipLaunchKernelGGL(fused_decode, dim3(NWG), dim3(NTHR), 0, stream, p);
}
```

```cpp
#define LSTEP 4
#define SB 25
#include <hip/hip_runtime.h>
#include <hip/hip_bf16.h>
#include <stdint.h>

#define STAMPS 0
#define NPRE 6
#define CODEPF 1
#define TLBPF 0
#define WIN 5
#define WINW 6
#define FLPOS 28
#define RAMP 4
#define WINV 6
#define WINT 6
#define QKPIN 1
#define ABL 0
#define WINK WIN
#define OSLEEP 1
#define PZPOS 0
#define FL1 1
#define CB4 1
#define OUTAUX 16
#define FMODE 0
#define DYN 1
#define W0 4
#define WS1 1
#define WS2 3
#define SB2 10
#define TH1 -30
#define TH2 20
#define S2 235
#define S3 700
#define T3 460
#define T4 430
#define T5 445
#define NSMP 64
#define PRPOS 1
#define PRSL 20
#define ODELAY 25
namespace {
typedef unsigned v4u __attribute__((ext_vector_type(4)));
typedef unsigned v2u __attribute__((ext_vector_type(2)));
typedef float f32x4 __attribute__((ext_vector_type(4)));
typedef short bf16x8 __attribute__((ext_vector_type(8)));
typedef __bf16 bf16x2 __attribute__((ext_vector_type(2)));
typedef float f32x2 __attribute__((ext_vector_type(2)));
typedef __amdgpu_buffer_rsrc_t rsrc_t;
#define GAS __attribute__((address_space(1)))
#define LAS __attribute__((address_space(3)))

constexpr int NWG = 256, NTHR = 512;
constexpr int DM = 1024, NQ = 4096, FF = 3584;
constexpr int WS_E = 0;
constexpr int WS_FLAG = 4096;
constexpr int WS_LINE = 8192;
constexpr int WS_Q = WS_LINE + 3 * 32768;
constexpr int WS_H = WS_Q + 4 * 32 * 4 * 512;
constexpr int WS_O = WS_H + 4 * 4 * FF * 4;
constexpr int WS_ML = WS_O + 32 * 4 * 16 * 256;
constexpr int WS_F = WS_ML + 32 * 4 * 16 * 8;
constexpr int WS_END = WS_F + 256 * 32 * 128;
constexpr int WS_STAMP = 8 << 20;
constexpr int OOB = 0x7f000000;
constexpr int AUX_NT = 2, AUX_SC1 = 16;
constexpr unsigned POISON = 0u;
constexpr int SPIN_CAP = 1 << 14;
constexpr int L_CTR = 0;
constexpr int L_XN = 64;
constexpr int L_Q = L_XN + 2048;
constexpr int L_M = L_Q + 2048;
constexpr int L_L = L_M + 256;
constexpr int L_RED = L_L + 256;
constexpr int RED_W = 8 * 18 * 16;
constexpr int L_O = L_RED + 4 * RED_W;
constexpr int O_W = 8 * 132 * 4;
constexpr int L_H = L_O + 5 * O_W;
constexpr int L_OA = L_H + 1024;
constexpr int L_END = L_OA + 1024;

struct Params {
  const void *x, *kc, *vc, *wq, *wo, *w1, *b1, *w2, *b2;
  void *out, *ws;
};

__device__ __forceinline__ rsrc_t mk(const void* p, int bytes) { return __builtin_amdgcn_make_buffer_rsrc((void*)p, 0, bytes, 0x00020000); }
template <int AUX> __device__ __forceinline__ v4u ld16(rsrc_t r, int voff, int soff) { return __builtin_amdgcn_raw_buffer_load_b128(r, voff, soff, AUX); }
template <int AUX> __device__ __forceinline__ v2u ld8(rsrc_t r, int voff, int soff) { return __builtin_amdgcn_raw_buffer_load_b64(r, voff, soff, AUX); }
__device__ __forceinline__ void st16(v4u v, rsrc_t r, int voff, int soff) { __builtin_amdgcn_raw_buffer_store_b128(v, r, voff, soff, AUX_SC1); }
__device__ __forceinline__ void st8(v2u v, rsrc_t r, int voff, int soff) { __builtin_amdgcn_raw_buffer_store_b64(v, r, voff, soff, AUX_SC1); }
__device__ __forceinline__ f32x4 mfma(v4u a, v4u b, f32x4 c) {
  return __builtin_amdgcn_mfma_f32_16x16x32_bf16(__builtin_bit_cast(bf16x8, a), __builtin_bit_cast(bf16x8, b), c, 0, 0, 0);
}
__device__ __forceinline__ float u2f(unsigned u) { return __builtin_bit_cast(float, u); }
__device__ __forceinline__ unsigned f2u(float f) { return __builtin_bit_cast(unsigned, f); }
__device__ __forceinline__ unsigned pk(float lo, float hi) {
  f32x2 v = {lo, hi};
  return __builtin_bit_cast(unsigned, __builtin_convertvector(v, bf16x2));
}
__device__ __forceinline__ float blo(unsigned u) { return u2f(u << 16); }
__device__ __forceinline__ float bhi(unsigned u) { return u2f(u & 0xffff0000u); }
__device__ __forceinline__ float rbf(float f) { return blo(pk(f, f)); }
template <int N> __device__ __forceinline__ float ror(float x) { return u2f(__builtin_amdgcn_mov_dpp(f2u(x), 0x120 + N, 0xf, 0xf, true)); }
template <int N> __device__ __forceinline__ unsigned roru(unsigned x) { return __builtin_amdgcn_mov_dpp(x, 0x120 + N, 0xf, 0xf, true); }
__device__ __forceinline__ float xrow_sum(float x) {
  auto s = __builtin_amdgcn_permlane16_swap(f2u(x), f2u(x), false, false);
  x = u2f(s[0]) + u2f(s[1]);
  auto t = __builtin_amdgcn_permlane32_swap(f2u(x), f2u(x), false, false);
  return u2f(t[0]) + u2f(t[1]);
}
__device__ __forceinline__ float xrow_max(float x) {
  auto s = __builtin_amdgcn_permlane16_swap(f2u(x), f2u(x), false, false);
  x = fmaxf(u2f(s[0]), u2f(s[1]));
  auto t = __builtin_amdgcn_permlane32_swap(f2u(x), f2u(x), false, false);
  return fmaxf(u2f(t[0]), u2f(t[1]));
}
__device__ __forceinline__ float wave_sum(float x) {
  x += ror<8>(x); x += ror<4>(x); x += ror<2>(x); x += ror<1>(x);
  return xrow_sum(x);
}
__device__ __forceinline__ unsigned umin4(v4u v) { return min(min(v.x, v.y), min(v.z, v.w)); }
__device__ __forceinline__ v4u nopoison(f32x4 v) {
  v4u u = __builtin_bit_cast(v4u, v);
  return v4u{max(u.x, 1u), max(u.y, 1u), max(u.z, 1u), max(u.w, 1u)};
}
#define PIN() __builtin_amdgcn_sched_barrier(0)
#define BAR() do { PIN(); asm volatile("s_waitcnt lgkmcnt(0)\n\ts_barrier" ::: "memory"); PIN(); } while (0)
#define REREAD() asm volatile("" ::: "memory")
__device__ __forceinline__ v4u tr8(const v4u* w, int j0) {
  const unsigned sel = (j0 & 1) ? 0x07060302u : 0x05040100u;
  const int c = j0 >> 1;
  return v4u{__builtin_amdgcn_perm(w[1][c], w[0][c], sel), __builtin_amdgcn_perm(w[3][c], w[2][c], sel),
             __builtin_amdgcn_perm(w[5][c], w[4][c], sel), __builtin_amdgcn_perm(w[7][c], w[6][c], sel)};
}
__device__ __forceinline__ void ln_stats(const float (&v)[16], float& m, float& inv) {
  float s = 0.f;
#pragma unroll
  for (int i = 0; i < 16; ++i) s += v[i];
  const float mf = wave_sum(s) * (1.0f / DM);
  float ss = 0.f;
#pragma unroll
  for (int i = 0; i < 16; ++i) { const float d = v[i] - mf; ss = fmaf(d, d, ss); }
  const float var = rbf(wave_sum(ss) * (1.0f / DM));
  m = rbf(mf);
  inv = __builtin_amdgcn_rcpf(rbf(__builtin_amdgcn_sqrtf(rbf(var + 1e-5f))));
}
__device__ __forceinline__ unsigned ln_pair(float a, float b, float m, float inv) {
  const unsigned d = pk(a - m, b - m);
  return pk(blo(d) * inv, bhi(d) * inv);
}
__device__ __forceinline__ unsigned lds_arrive(LAS unsigned* c, int lane) {
  unsigned old = 0;
  REREAD();
  if (lane == 0) old = __hip_atomic_fetch_add(c, 1u, __ATOMIC_RELAXED, __HIP_MEMORY_SCOPE_WORKGROUP);
  return __builtin_amdgcn_readfirstlane(old);
}
template <int SL = 1> __device__ __forceinline__ void class_bar(LAS unsigned* c, int lane) {
  PIN();
  if (lds_arrive(c, lane) != 3u) {
    _Pragma("nounroll") while (__builtin_amdgcn_readfirstlane(__hip_atomic_load(c, __ATOMIC_RELAXED, __HIP_MEMORY_SCOPE_WORKGROUP)) < 4u)
      __builtin_amdgcn_s_sleep(SL);
  }
  REREAD();
  PIN();
}
template <int X> __device__ __forceinline__ void dyn_pace(int lag) {
  asm volatile("s_cmp_gt_i32 %0, %1\n\ts_cbranch_scc1 1f\n\ts_waitcnt vmcnt(%3)\n1:\n\ts_cmp_gt_i32 %0, %2\n\ts_cbranch_scc1 2f\n\ts_waitcnt vmcnt(%4)\n2:\n\ts_waitcnt vmcnt(%5)"
               :: "s"(lag), "n"(TH1), "n"(TH2), "n"(W0 - 1 + X), "n"(W0 + WS1 - 1 + X), "n"(W0 + WS2 - 1 + X) : "memory", "scc");
}
__device__ __forceinline__ int now32() { return (int)(unsigned)__builtin_amdgcn_s_memrealtime(); }
template <int N> __device__ __forceinline__ void pace() { asm volatile("s_waitcnt vmcnt(%0)" :: "n"(N) : "memory"); }

#define STAMP(i)
#define STAMPV(i, v)
#define STAMP_FLUSH()

__global__ void __launch_bounds__(NTHR, 2) fused_decode(Params p) {
  __shared__ __attribute__((aligned(16))) unsigned char smem[L_END];
  LAS unsigned char* L = (LAS unsigned char*)smem;
  LAS unsigned* ctr = (LAS unsigned*)(L + L_CTR);
  const int tid = threadIdx.x, lane = tid & 63;
  const int w = __builtin_amdgcn_readfirstlane(tid >> 6);
  const int wl = w & 3;
  const bool isA = w < 4;
  const int xs = blockIdx.x & 7, ci = blockIdx.x >> 3;
  const int n16 = lane & 15, g = lane >> 4;
  const int tbase = DYN ? now32() + SB : 0;
  int lag = 0;
  unsigned cpf = 0, tpf = 0;
  {
    unsigned long long pc; int len;
    asm volatile("s_getpc_b64 %0\n.Lpf_base%=:\n\ts_mov_b32 %1, .Lfd_code_end-.Lpf_base%=" : "=s"(pc), "=s"(len));
    if (tid < 5) cpf = __builtin_amdgcn_raw_buffer_load_b32(mk((const void*)pc, len), (tid * 32 + ci) * 128, 0, 0);
  }
  PIN();
  asm volatile("" :: "s"(p.x), "s"(p.kc), "s"(p.vc), "s"(p.wq), "s"(p.wo), "s"(p.w1), "s"(p.b1), "s"(p.w2), "s"(p.b2), "s"(p.out), "s"(p.ws));
  v2u xr[4];
  if (isA) {
    const rsrc_t rx = mk(p.x, 4 * DM * 2);
#pragma unroll
    for (int i = 0; i < 4; ++i) xr[i] = ld8<0>(rx, lane * 8, wl * 2048 + ((i + (xs >> 1)) & 3) * 512);
  }
  PIN();
  const rsrc_t rws = mk(p.ws, WS_END);
  const int gidx = 2 * xs + (ci >> 4), ab = gidx >> 2, kvh = gidx & 3, sp = ci & 15;
  const int kq = xs >> 1, isq = ci < 16, cb = isq ? 16 * (xs & 1) + ci : 14 * (xs & 1) + ci - 16;
  const int hd = 4 * xs + (ci >> 3), cg = ci & 7;
  const int cu = xs * 32 + ci;
  const size_t kvbase = ((size_t)gidx * 16 + sp) * 262144;
  unsigned long long gen = *(const __attribute__((address_space(4))) unsigned long long*)p.ws;
#define MAKE_TAG() const unsigned long long tag64 = (gen + 1ull) * 0x9E3779B97F4A7C15ull; \
  const unsigned tag0 = (unsigned)tag64, tag1 = (unsigned)(tag64 >> 32)

  const int hc = lane & 7, hbt = (lane >> 3) & 3, hb = 112 * hd + 28 * wl + 4 * hc;
  const int fb = FMODE == 2 ? 2 * (lane & 1) + ((lane >> 3) & 1) : (lane >> 2) & 3, fc = 4 * cu + (FMODE == 2 ? (lane >> 1) & 3 : lane & 3);
  v2u sm;
  if (isA) {
    if (tid < 8) ctr[tid] = 0u;
    PIN();
    float v[16];
#pragma unroll
    for (int i = 0; i < 8; ++i) { v[2 * i] = blo(xr[i >> 1][i & 1]); v[2 * i + 1] = bhi(xr[i >> 1][i & 1]); }
    STAMP(1);
    float m, inv;
    ln_stats(v, m, inv);
    if (!isq) {
#pragma unroll
      for (int i = 0; i < 16; i += 2) { const unsigned o = ln_pair(v[i], v[i + 1], m, inv); v[i] = blo(o); v[i + 1] = bhi(o); }
      ln_stats(v, m, inv);
    }
    *(LAS v2u*)(L + L_XN + wl * 512 + lane * 8) = v2u{ln_pair(v[0], v[1], m, inv), ln_pair(v[2], v[3], m, inv)};
    asm volatile("" : "+s"(gen));
    STAMP(2);
    BAR();
    asm volatile("" :: "v"(cpf), "v"(tpf));
  } else {
    const int ldw = isq ? NQ * 2 : FF * 2;
    const rsrc_t rw = mk(isq ? p.wq : p.w1, DM * ldw);
    const int wo_ = ci < 30 ? (256 * kq + 64 * wl + 8 * g) * ldw + cb * 256 + n16 * 16 : OOB;
    v4u wv[16];
#pragma unroll
    for (int j = 0; j < 16; ++j) {
      if (PZPOS && j == PZPOS && !(ABL & 1)) {
        const v4u pz = {POISON, POISON, POISON, POISON};
        st16(pz, rws, wl < 2 ? WS_O + (((8 * kvh + 4 * wl + g) * 4 + ab) * 16 + sp) * 256 + n16 * 16
                             : wl == 2 && lane < 4 ? WS_ML + (gidx * 16 + sp) * 64 + lane * 16 : OOB, 0);
        if (FMODE == 0) st16(pz, rws, wl >= 2 ? WS_F + ((32 * cg + 16 * (wl & 1) + (lane >> 2)) * 32 + hd) * 64 + (lane & 3) * 16 : OOB, 0);
        PIN();
      }
      if (PZPOS && j >= WINW && j < WINW + PZPOS) pace<WINW + (FMODE == 0)>();
      else if (j >= WINW) pace<WINW - 1>();
      wv[j] = ld16<AUX_NT>(rw, wo_, ((j >> 3) * 32 + (j & 7)) * ldw);
      PIN();
    }
    asm volatile("" : "+s"(gen));
    STAMP(1);
    BAR();
    STAMP(2);
    f32x4 d[8];
#pragma unroll
    for (int t = 0; t < 2; ++t) {
      const v4u act = *(const LAS v4u*)(L + L_XN + (n16 & 3) * 512 + (64 * wl + 32 * t + 8 * g) * 2);
#pragma unroll
      for (int j0 = 0; j0 < 8; ++j0) d[j0] = mfma(act, tr8(wv + 8 * t, j0), t ? d[j0] : f32x4{0.f, 0.f, 0.f, 0.f});
      PIN();
    }
    if (lane < 16) {
#pragma unroll
      for (int j0 = 0; j0 < 8; ++j0) *(LAS f32x4*)(L + L_RED + wl * RED_W + (j0 * 17 + lane) * 16) = d[j0];
    }
    STAMP(3);
    class_bar(ctr + 0, lane);
    if (wl < 2) {
      const int b = g, jh = lane & 1, nn = wl * 8 + (n16 >> 1);
      f32x4 sum = {0.f, 0.f, 0.f, 0.f};
#pragma unroll
      for (int ww = 0; ww < 4; ++ww)
#pragma unroll
        for (int k = 0; k < 4; ++k) sum[k] += *(const LAS float*)(L + L_RED + ww * RED_W + ((4 * jh + k) * 17 + nn) * 16 + b * 4);
      const int co = (8 * nn + 4 * jh) * 4;
      const int off = isq ? WS_Q + ((b * 32 + cb) * 4 + kq) * 512 + co : (ci < 30 ? WS_H + ((kq * 4 + b) * FF + cb * 128) * 4 + co : OOB);
      st16(__builtin_bit_cast(v4u, sum), rws, (ABL & 4) ? OOB : off, 0);
    }
    if (!(ABL & 1) && !PZPOS) {
        const v4u pz = {POISON, POISON, POISON, POISON};
        st16(pz, rws, wl < 2 ? WS_O + (((8 * kvh + 4 * wl + g) * 4 + ab) * 16 + sp) * 256 + n16 * 16
                             : wl == 2 && lane < 4 ? WS_ML + (gidx * 16 + sp) * 64 + lane * 16 : OOB, 0);
        if (FMODE == 0) st16(pz, rws, wl >= 2 ? WS_F + ((32 * cg + 16 * (wl & 1) + (lane >> 2)) * 32 + hd) * 64 + (lane & 3) * 16 : OOB, 0);
        PIN();
      }
    STAMP(4);
    sm.x = __builtin_amdgcn_raw_buffer_load_b16(mk(p.x, 4 * DM * 2), wl == 2 && !(ABL & 2) ? (fb * DM + fc) * 2 : OOB, 0, 0);
    sm.y = __builtin_amdgcn_raw_buffer_load_b16(mk(p.b2, DM * 2), wl == 2 && !(ABL & 2) ? fc * 2 : OOB, 0, 0);
    pace<0>();
    if (lds_arrive(ctr + 1, lane) == 3u) {
      MAKE_TAG();
      st8(v2u{tag0, tag1}, rws, lane == 0 ? WS_FLAG + cu * 8 : lane == 1 ? WS_LINE + cu * 128 : OOB, 0);
    }
    STAMP(5);
    BAR();
  }
  const rsrc_t rk = mk((const char*)p.kc + kvbase, 262144), rv = mk((const char*)p.vc + kvbase, 262144);
  const int koff = (w >> 2) * 131072 + wl * 4096 + ((lane >> 2) & 3) * 256 + g * 64 + (lane & 3) * 16;
  const int voff = (w >> 2) * 131072 + wl * 4096 + lane * 16;
  v4u r[32];
  v2u fq;
  const int fhd = 8 * kvh + 2 * wl + ((lane >> 2) & 1);
  const int fqo = WS_LINE + ((2 * (lane & 3) + (fhd >> 4)) * 32 + (fhd & 15)) * 128;
  __builtin_amdgcn_s_setprio(1);
#pragma unroll
  for (int i = 0; i < 32; ++i) {
    if (i == FLPOS) { fq = ld8<AUX_SC1>(rws, isA && lane < 8 ? fqo : OOB, 0); PIN(); }
    if (DYN && i >= W0) { if (i >= FLPOS) dyn_pace<1>(lag); else dyn_pace<0>(lag); }
    else if (i >= FLPOS && i < FLPOS + WINK) pace<WINK>();
    else if (i >= WINK) pace<WINK - 1>();
    else if (i > 0 && RAMP) __builtin_amdgcn_s_sleep(RAMP);
    r[i] = ld16<AUX_NT>(rk, koff, (i >> 2) * 16384 + (i & 3) * 1024);
    PIN();
    if (DYN && (i % LSTEP) == 0) { lag = now32() - tbase - (isA ? S2 + (S3 - S2) * i / 32 : S3 + T3 * i / 32); asm volatile("" : "+s"(lag)); PIN(); }
  }
  __builtin_amdgcn_s_setprio(0);
  STAMP(6);
  BAR();
  MAKE_TAG();
  if (DYN == 2 && wl == 0) __builtin_amdgcn_raw_buffer_store_b32((unsigned)now32() ^ tag0, rws, lane == 0 ? WS_LINE + (1 + (w >> 2)) * 32768 + cu * 128 : OOB, 0, AUX_SC1);
  if (isA) {
    int spins = 0;
    if (__any(lane < 8 && ((fq.x ^ tag0) | (fq.y ^ tag1)) != 0u)) {
      _Pragma("nounroll") while (true) {
        REREAD();
        const v2u a = ld8<AUX_SC1>(rws, lane < 8 ? fqo : OOB, 0);
        if (++spins > SPIN_CAP || !__any(lane < 8 && ((a.x ^ tag0) | (a.y ^ tag1)) != 0u)) break;
        __builtin_amdgcn_s_sleep(2);
      }
    }
    REREAD();
    STAMPV(18, spins);
    STAMP(3);
    const int qo = WS_Q + ((ab * 32 + 8 * kvh + 2 * wl + (lane >> 5)) * 4) * 512 + (lane & 31) * 16;
    v4u qp[4];
#pragma unroll
    for (int s = 0; s < 4; ++s) qp[s] = ld16<AUX_SC1>(rws, (ABL & 8) ? OOB : qo, s * 512);
    PIN();
    const f32x4 qs = (__builtin_bit_cast(f32x4, qp[0]) + __builtin_bit_cast(f32x4, qp[1])) + (__builtin_bit_cast(f32x4, qp[2]) + __builtin_bit_cast(f32x4, qp[3]));
    *(LAS v2u*)(L + L_Q + (2 * wl + (lane >> 5)) * 256 + (lane & 31) * 8) = v2u{pk(qs[0], qs[1]), pk(qs[2], qs[3])};
    class_bar(ctr + 2, lane);
    STAMP(4);
  }
  const int hl = n16 & 7, wb = n16 >> 3;
  const v4u qb0 = *(const LAS v4u*)(L + L_Q + hl * 256 + (4 * g + wb) * 16);
  const v4u qb1 = *(const LAS v4u*)(L + L_Q + hl * 256 + (4 * g + 2 + wb) * 16);
#define PRLOAD() pr = __builtin_amdgcn_raw_buffer_load_b32(rws, wl == 1 && lane < NSMP ? WS_LINE + (lane * (256 / NSMP) + (ci & (256 / NSMP - 1))) * 128 : OOB, (1 + (w >> 2)) * 32768, AUX_SC1)
  float s[16];
#pragma unroll
  for (int u = 0; u < 8; ++u) {
    float t0[4], t1[4];
#pragma unroll
    for (int k = 0; k < 4; ++k) {
      const f32x4 d0 = mfma(r[4 * u + k], qb0, f32x4{0.f, 0.f, 0.f, 0.f});
      const f32x4 d1 = mfma(r[4 * u + k], qb1, f32x4{0.f, 0.f, 0.f, 0.f});
      t0[k] = d0[0] + d1[2];
      t1[k] = d0[1] + d1[3];
    }
#pragma unroll
    for (int k = 0; k < 4; k += 2) {
      const float xx = wb ? t1[k + 1] : t0[k], yy = wb ? t1[k] : t0[k + 1];
      s[2 * u + (k >> 1)] = xx + ror<8>(yy);
    }
    if (QKPIN) PIN();
  }
  unsigned pr = 0u;
  if (DYN == 2 && PRPOS == 0) { PIN(); PRLOAD(); PIN(); }
  float m = -INFINITY;
#pragma unroll
  for (int i = 0; i < 16; i += 2) {
    const unsigned a = pk(s[i], s[i + 1]);
    const unsigned c = pk(blo(a) * 0.08838834764831845f, bhi(a) * 0.08838834764831845f);
    s[i] = blo(c); s[i + 1] = bhi(c);
    m = fmaxf(m, fmaxf(s[i], s[i + 1]));
  }
  m = fmaxf(m, ror<8>(m));
  m = xrow_max(m);
  if (lane < 8) *(LAS float*)(L + L_M + (w * 8 + lane) * 4) = m;
  constexpr float LOG2E = 1.4426950408889634f;
  if (!isA) {
    if (wl == 0) {
      int spins = 0;
      _Pragma("nounroll") while (true) {
        REREAD();
        const v4u a = ld16<AUX_SC1>(rws, WS_FLAG + lane * 16, 0), b = ld16<AUX_SC1>(rws, WS_FLAG + lane * 16, 1024);
        const unsigned bad = (a.x ^ tag0) | (a.y ^ tag1) | (a.z ^ tag0) | (a.w ^ tag1) | (b.x ^ tag0) | (b.y ^ tag1) | (b.z ^ tag0) | (b.w ^ tag1);
        if (!__any(bad != 0u) || ++spins > SPIN_CAP) break;
        __builtin_amdgcn_s_sleep(4);
      }
      REREAD();
      if (blockIdx.x == 0) st8(v2u{(unsigned)(gen + 1ull), (unsigned)((gen + 1ull) >> 32)}, rws, lane == 0 ? WS_E : OOB, 0);
    }
    class_bar(ctr + 3, lane);
#pragma unroll
    for (int ww = 0; ww < 8; ++ww) m = fmaxf(m, *(const LAS float*)(L + L_M + (ww * 8 + hl) * 4));
  }
  unsigned pb[16];
  float lsum = 0.f;
  {
    const unsigned msk = wb ? 0xffff0000u : 0x0000ffffu;
#pragma unroll
    for (int i = 0; i < 16; ++i) {
      const float e = __builtin_amdgcn_exp2f((s[i] - m) * LOG2E);
      lsum += e;
      const unsigned t = pk(e, e) & msk;
      pb[i] = t | roru<8>(t);
      asm volatile("" : "+v"(pb[i]));
    }
    lsum += ror<8>(lsum);
    lsum = xrow_sum(lsum);
    if (lane < 8) *(LAS float*)(L + L_L + (w * 8 + lane) * 4) = lsum;
  }
  f32x4 acc[8];
#pragma unroll
  for (int j = 0; j < 8; ++j) acc[j] = f32x4{0.f, 0.f, 0.f, 0.f};
  int tref = tbase + S3 + T3 + (isA ? 0 : T4);
  if (DYN == 2 && wl == 1) {
    if (PRPOS == 1) { if (!isA) __builtin_amdgcn_s_sleep(PRSL); PRLOAD(); }
    const unsigned tn = (unsigned)now32();
    const unsigned age = tn - (pr ^ tag0);
    const bool ok = age < 800u && lane < NSMP;
    const float mean = wave_sum(ok ? (float)age : 0.f) * __builtin_amdgcn_rcpf(fmaxf(wave_sum(ok ? 1.f : 0.f), 1.f));
    if (lane == 0) ctr[8 + (w >> 2)] = tn - (int)mean + (isA ? T3 : T4) + SB2;
  }
  STAMP(7);
  BAR();
  STAMP(15);
constexpr int PVLAG = DYN ? W0 + WS2 : WINV;
#define PV(n) acc[(n) & 7] = mfma(tr8(&r[(n) & ~7], (n) & 7), v4u{pb[((n) >> 3) * 4], pb[((n) >> 3) * 4 + 1], pb[((n) >> 3) * 4 + 2], pb[((n) >> 3) * 4 + 3]}, acc[(n) & 7])
  __builtin_amdgcn_s_setprio(1);
#pragma unroll
  for (int i = 0; i < 32; ++i) {
    if (DYN && i >= W0) dyn_pace<0>(lag);
    else if (i >= WINV) pace<WINV - 1>();
    else if (i > 0 && RAMP) __builtin_amdgcn_s_sleep(RAMP);
    r[i] = ld16<AUX_NT>(rv, voff, (i >> 2) * 16384 + (i & 3) * 1024);
    PIN();
    if (DYN && (i % LSTEP) == 0) { if (i == 0 && DYN == 2) tref = __builtin_amdgcn_readfirstlane(ctr[8 + (w >> 2)]); lag = now32() - tref - (isA ? T4 * i / 32 : T5 * i / 32); asm volatile("" : "+s"(lag)); PIN(); }
    if (i >= 7 + PVLAG) { PV(i - 7 - PVLAG); PIN(); }
  }
  const int mo = WS_ML + ((wl * 4 + (hd >> 3)) * 16 + ((n16 + 4 * g) & 15)) * 64 + (hd & 7) * 8;
  v2u ml = ld8<AUX_SC1>(rws, isA ? OOB : mo, 0);
  STAMP(16);
  BAR();
#pragma unroll
  for (int n = 32 - 7 - PVLAG; n < 32; ++n) { PV(n); if ((n & 7) == 7) PIN(); }
  STAMP(8);
#define PUT_ACC(f) _Pragma("unroll") for (int i = 0; i < 4; ++i) { \
      const f32x4 t = {wb ? acc[4][i] : acc[0][i], wb ? acc[5][i] : acc[1][i], wb ? acc[6][i] : acc[2][i], wb ? acc[7][i] : acc[3][i]}; \
      *(LAS f32x4*)(L + L_O + wl * O_W + (hl * 132 + 8 * (4 * g + i) + 4 * wb) * 4) = f t; }
  const int oro = ((2 * wl + (g & 1)) * 132 + 8 * n16 + 4 * (g >> 1)) * 4;
  if (isA) {
    __builtin_amdgcn_s_setprio(0);
    float M = m;
#pragma unroll
    for (int ww = 0; ww < 8; ++ww) M = fmaxf(M, *(const LAS float*)(L + L_M + (ww * 8 + hl) * 4));
    const float fa = __builtin_amdgcn_exp2f((m - M) * LOG2E);
    PUT_ACC(fa *);
    if (lane < 8) *(LAS float*)(L + L_L + (w * 8 + lane) * 4) = fa * lsum;
    class_bar(ctr + 4, lane);
    {
      f32x4 t = *(const LAS f32x4*)(L + L_O + oro);
#pragma unroll
      for (int ww = 1; ww < 4; ++ww) t += *(const LAS f32x4*)(L + L_O + ww * O_W + oro);
      *(LAS f32x4*)(L + L_O + 4 * O_W + oro) = t;
    }
    if (w == 0) {
      float l = 0.f;
#pragma unroll
      for (int ww = 0; ww < 8; ++ww) l += *(const LAS float*)(L + L_L + (ww * 8 + hl) * 4);
      st8(v2u{max(f2u(M), 1u), max(f2u(l), 1u)}, rws, lane < 8 ? WS_ML + (gidx * 16 + sp) * 64 + lane * 8 : OOB, 0);
    }
    {
    const int ho = hc < 7 ? WS_H + ((8 * (lane >> 5) + hbt) * FF + hb) * 4 : OOB;
    v4u hp[2];
#pragma unroll
    for (int s = 0; s < 2; ++s) hp[s] = ld16<AUX_SC1>(rws, (ABL & 8) ? OOB : ho, s * 4 * FF * 4);
    sm = ld8<0>(mk(p.b1, FF * 2), hc < 7 && !(ABL & 2) ? hb * 2 : OOB, 0);
    PIN();
    float hv[4];
#pragma unroll
    for (int j = 0; j < 4; ++j) {
      const float t0 = u2f(hp[0][j]) + u2f(hp[1][j]);
      auto sw = __builtin_amdgcn_permlane32_swap(f2u(t0), f2u(t0), false, false);
      const float t = u2f(sw[0]) + u2f(sw[1]);
      const float u = rbf(rbf(t) + ((j & 1) ? bhi(sm[j >> 1]) : blo(sm[j >> 1])));
      const float z2 = u * fmaf(u * u, 0.044715f, 1.0f) * (-2.0f * 0.7978845608028654f * 1.4426950408889634f);
      hv[j] = u * __builtin_amdgcn_rcpf(1.0f + __builtin_amdgcn_exp2f(z2));
    }
    if (lane < 32) *(LAS v2u*)(L + L_H + wl * 256 + hbt * 64 + hc * 8) = v2u{pk(hv[0], hv[1]), pk(hv[2], hv[3])};
    }
    const rsrc_t r2 = mk(p.w2, FF * DM * 2), ro = mk(p.wo, NQ * DM * 2);
    const int o2 = (112 * hd + 28 * wl + 8 * g) * 2048 + cg * 256 + n16 * 16;
    const int o2b = g < 3 ? o2 : OOB;
    const int oo = (128 * hd + 32 * wl + 8 * g) * 2048 + cg * 256 + n16 * 16;
    const v4u hact = *(const LAS v4u*)(L + L_H + wl * 256 + (n16 & 3) * 64 + g * 16);
    v4u tw[16];
#define TLD(j) do { if ((j) >= WINT) pace<WINT - 1>(); else if ((j) > 0 && (j) < NPRE && RAMP) __builtin_amdgcn_s_sleep(RAMP); tw[j] = (j) < 8 ? ld16<AUX_NT>(r2, (j) < 4 ? o2 : o2b, (j) * 2048) : ld16<AUX_NT>(ro, oo, ((j) - 8) * 2048); PIN(); } while (0)
    STAMP(5);
    BAR();
#pragma unroll
    for (int j = 0; j < NPRE; ++j) TLD(j);
    STAMP(9);
    if (!CB4) BAR();
#pragma unroll
    for (int j = NPRE; j < 16; ++j) TLD(j);
    f32x4 d[8];
#pragma unroll
    for (int j0 = 0; j0 < 8; ++j0) d[j0] = mfma(hact, tr8(tw, j0), f32x4{0.f, 0.f, 0.f, 0.f});
    v4u wt[8];
#pragma unroll
    for (int j0 = 0; j0 < 8; ++j0) { wt[j0] = tr8(tw + 8, j0); asm volatile("" : "+v"(wt[j0])); }
    STAMP(10);
    BAR();
    STAMP(11);
    const v4u oact = *(const LAS v4u*)(L + L_OA + (n16 & 3) * 256 + (32 * wl + 8 * g) * 2);
#pragma unroll
    for (int j0 = 0; j0 < 8; ++j0) d[j0] = mfma(oact, wt[j0], d[j0]);
    if (lane < 16) {
#pragma unroll
      for (int j0 = 0; j0 < 8; ++j0) *(LAS f32x4*)(L + L_RED + wl * RED_W + (j0 * 18 + lane) * 16) = d[j0];
    }
    BAR();
    STAMP(12);
    asm volatile(".Lfd_code_end:");
  } else {
    PUT_ACC();
    if (CB4) class_bar<0>(ctr + 6, lane); else BAR();
    STAMP(9);
    {
      f32x4 t = *(const LAS f32x4*)(L + L_O + 4 * O_W + oro);
#pragma unroll
      for (int ww = 0; ww < 4; ++ww) t += *(const LAS f32x4*)(L + L_O + ww * O_W + oro);
      const unsigned a0 = max(pk(t[0], t[1]), 1u), a1 = max(pk(t[2], t[3]), 1u);
      auto s0 = __builtin_amdgcn_permlane32_swap(a0, a0, false, false);
      auto s1 = __builtin_amdgcn_permlane32_swap(a1, a1, false, false);
      st16(v4u{s0[0], s1[0], s0[1], s1[1]}, rws, lane < 32 ? WS_O + (((8 * kvh + 2 * wl + g) * 4 + ab) * 16 + sp) * 256 + n16 * 16 : OOB, 0);
    }
    PIN();
    STAMP(10);
    __builtin_amdgcn_s_setprio(0);
    {
      int spins = 0;
      if (__any(min(ml.x, ml.y) == POISON)) {
        _Pragma("nounroll") while (true) {
          REREAD();
          ml = ld8<AUX_SC1>(rws, mo, 0);
          if (!__any(min(ml.x, ml.y) == POISON) || ++spins > SPIN_CAP) break;
          __builtin_amdgcn_s_sleep(2);
        }
      }
      STAMPV(19, spins);
      float M = u2f(ml.x);
      M = fmaxf(M, ror<8>(M)); M = fmaxf(M, ror<4>(M)); M = fmaxf(M, ror<2>(M)); M = fmaxf(M, ror<1>(M));
      const float e = __builtin_amdgcn_exp2f((u2f(ml.x) - M) * LOG2E);
      float Ls = e * u2f(ml.y);
      Ls += ror<8>(Ls); Ls += ror<4>(Ls); Ls += ror<2>(Ls); Ls += ror<1>(Ls);
      float f = e * __builtin_amdgcn_rcpf(Ls);
      asm volatile("" : "+v"(f));
      if (ODELAY) __builtin_amdgcn_s_sleep(ODELAY);
      const int oo_ = WS_O + ((hd * 4 + wl) * 16 + 4 * g) * 256 + n16 * 16;
      v4u op[4];
      int of[4];
#pragma unroll
      for (int k = 0; k < 4; ++k) { op[k] = v4u{0u, 0u, 0u, 0u}; of[k] = oo_; }
      spins = 0;
      _Pragma("nounroll") while (true) {
        REREAD();
        v4u nw[4];
#pragma unroll
        for (int k = 0; k < 4; ++k) nw[k] = ld16<AUX_SC1>(rws, of[k], k * 256);
        PIN();
        bool bad = false;
#pragma unroll
        for (int k = 0; k < 4; ++k) {
          op[k] |= nw[k];
          const bool b = umin4(op[k]) == POISON;
          of[k] = b ? oo_ : OOB;
          bad |= b;
        }
        if (!__any(bad) || ++spins > SPIN_CAP) break;
        __builtin_amdgcn_s_sleep(OSLEEP);
      }
      STAMPV(18, spins);
      STAMP(13);
      float t[8] = {0.f, 0.f, 0.f, 0.f, 0.f, 0.f, 0.f, 0.f};
#define OACC(k) do { const float fk = u2f(__builtin_amdgcn_mov_dpp(f2u(f), 0x150 + (k), 0xf, 0xf, true)); \
        _Pragma("unroll") for (int j = 0; j < 4; ++j) { t[2 * j] = fmaf(fk, blo(op[k][j]), t[2 * j]); t[2 * j + 1] = fmaf(fk, bhi(op[k][j]), t[2 * j + 1]); } } while (0)
      OACC(0); OACC(1); OACC(2); OACC(3);
      float u[4];
#pragma unroll
      for (int j = 0; j < 4; ++j) { auto sw = __builtin_amdgcn_permlane32_swap(f2u(t[j]), f2u(t[4 + j]), false, false); u[j] = u2f(sw[0]) + u2f(sw[1]); }
      auto s0 = __builtin_amdgcn_permlane16_swap(f2u(u[0]), f2u(u[2]), false, false);
      auto s1 = __builtin_amdgcn_permlane16_swap(f2u(u[1]), f2u(u[3]), false, false);
      *(LAS unsigned*)(L + L_OA + wl * 256 + n16 * 16 + g * 4) = pk(u2f(s0[0]) + u2f(s0[1]), u2f(s1[0]) + u2f(s1[1]));
      if (wl != 2) __builtin_amdgcn_s_setprio(2);
    }
    BAR();
    BAR();
    if (FMODE == 2) {
      const int bp = lane & 1, j0 = (lane >> 1) & 7, nn = wl * 4 + g;
      f32x2 t = *(const LAS f32x2*)(L + L_RED + (j0 * 18 + nn) * 16 + bp * 8);
#pragma unroll
      for (int ww = 1; ww < 4; ++ww) t += *(const LAS f32x2*)(L + L_RED + ww * RED_W + (j0 * 18 + nn) * 16 + bp * 8);
      st16(v4u{f2u(t[0]), tag0, f2u(t[1]), tag1}, rws, WS_F + ((32 * cg + 2 * nn + (j0 >> 2)) * 32 + hd) * 128 + (lane & 7) * 16, 0);
    }
    if (FMODE != 2 && wl < 2) {
      const int j0 = lane & 7, nn = wl * 8 + (lane >> 3);
      f32x4 t = *(const LAS f32x4*)(L + L_RED + (j0 * 18 + nn) * 16);
#pragma unroll
      for (int ww = 1; ww < 4; ++ww) t += *(const LAS f32x4*)(L + L_RED + ww * RED_W + (j0 * 18 + nn) * 16);
      st16(FMODE == 1 ? v4u{pk(t[0], t[1]), tag0, pk(t[2], t[3]), tag1} : nopoison(t), rws, WS_F + ((32 * cg + 2 * nn + (j0 >> 2)) * 32 + hd) * 64 + (j0 & 3) * 16, 0);
      STAMP(11);
    } else if (wl == 2) {
      v4u f0, f1, f2, f3;
      int spins = 0;
      _Pragma("nounroll") while (true) {
        REREAD();
        f0 = ld16<AUX_SC1>(rws, WS_F + cu * (FMODE == 2 ? 4096 : 2048) + lane * 16, 0);
        f1 = ld16<AUX_SC1>(rws, WS_F + cu * (FMODE == 2 ? 4096 : 2048) + lane * 16, 1024);
        if (FMODE == 2) {
          f2 = ld16<AUX_SC1>(rws, WS_F + cu * 4096 + lane * 16, 2048);
          f3 = ld16<AUX_SC1>(rws, WS_F + cu * 4096 + lane * 16, 3072);
        }
        bool bad;
        if (FMODE == 0) bad = min(umin4(f0), umin4(f1)) == POISON;
        else bad = ((f0.y ^ tag0) | (f0.w ^ tag1) | (f1.y ^ tag0) | (f1.w ^ tag1)) != 0u;
        if (FMODE == 2) bad |= ((f2.y ^ tag0) | (f2.w ^ tag1) | (f3.y ^ tag0) | (f3.w ^ tag1)) != 0u;
        if (!__any(bad) || ++spins > SPIN_CAP) break;
        __builtin_amdgcn_s_sleep(1);
      }
      STAMPV(17, spins);
      STAMP(11);
      float y;
      if (FMODE == 2) {
        const float y0 = (u2f(f0.x) + u2f(f1.x)) + (u2f(f2.x) + u2f(f3.x)), y1 = (u2f(f0.z) + u2f(f1.z)) + (u2f(f2.z) + u2f(f3.z));
        y = xrow_sum((wb ? y1 : y0) + ror<8>(wb ? y0 : y1));
      } else {
        f32x4 t = FMODE == 1 ? f32x4{blo(f0.x) + blo(f1.x), bhi(f0.x) + bhi(f1.x), blo(f0.z) + blo(f1.z), bhi(f0.z) + bhi(f1.z)}
                             : __builtin_bit_cast(f32x4, f0) + __builtin_bit_cast(f32x4, f1);
        const bool h8 = lane & 8, h4 = lane & 4;
        const float a0 = (h8 ? t[2] : t[0]) + ror<8>(h8 ? t[0] : t[2]), a1 = (h8 ? t[3] : t[1]) + ror<8>(h8 ? t[1] : t[3]);
        const float k = h4 ? a1 : a0, o = h4 ? a0 : a1;
        const float o4 = u2f(__builtin_amdgcn_mov_dpp(f2u(o), 0x141, 0xf, 0xf, true));
        y = xrow_sum(k + u2f(__builtin_amdgcn_mov_dpp(f2u(o4), 0x1B, 0xf, 0xf, true)));
      }
      y += blo(sm.x) + blo(sm.y);
      __builtin_amdgcn_raw_buffer_store_b16((unsigned short)pk(y, y), mk(p.out, 4 * DM * 2), lane < 16 ? (fb * DM + fc) * 2 : OOB, 0, OUTAUX);
      STAMP(12);
    }
  }
  STAMP_FLUSH();
}
}

extern "C" void kernel_launch(void* const* d_in, const int* in_sizes, int n_in, void* d_out, int out_size, void* d_ws, size_t ws_size,
                              hipStream_t stream) {
  Params p;
  p.x = d_in[0]; p.kc = d_in[1]; p.vc = d_in[2]; p.wq = d_in[3]; p.wo = d_in[4];
  p.w1 = d_in[6]; p.b1 = d_in[7]; p.w2 = d_in[8]; p.b2 = d_in[9];
  p.out = d_out; p.ws = d_ws;
  hipLaunchKernelGGL(fused_decode, dim3(NWG), dim3(NTHR), 0, stream, p);
}
```

```cpp
#define STAMPS 1
#define DYN 0
#define DIAG 6
#define DIAGW 0
#define DIAGB -1
#define DIAGS 1
#include <hip/hip_runtime.h>
#include <hip/hip_bf16.h>
#include <stdint.h>

#define NPRE 6
#define CODEPF 1
#define TLBPF 0
#define WIN 5
#define WINW 6
#define FLPOS 28
#define RAMP 4
#define WINV 6
#define WINT 6
#define QKPIN 1
#define ABL 0
#define WINK WIN
#define OSLEEP 1
#define PZPOS 0
#define FL1 1
#define CB4 1
#define OUTAUX 16
#define FMODE 0
#define W0 4
#define WS1 1
#define WS2 3
#define SB 0
#define SB2 10
#define TH1 -30
#define TH2 20
#define S2 235
#define S3 700
#define T3 460
#define T4 430
#define T5 445
#define NSMP 64
#define PRPOS 1
#define PRSL 20
#define LSTEP 8
#define ODELAY 25
namespace {
typedef unsigned v4u __attribute__((ext_vector_type(4)));
typedef unsigned v2u __attribute__((ext_vector_type(2)));
typedef float f32x4 __attribute__((ext_vector_type(4)));
typedef short bf16x8 __attribute__((ext_vector_type(8)));
typedef __bf16 bf16x2 __attribute__((ext_vector_type(2)));
typedef float f32x2 __attribute__((ext_vector_type(2)));
typedef __amdgpu_buffer_rsrc_t rsrc_t;
#define GAS __attribute__((address_space(1)))
#define LAS __attribute__((address_space(3)))

constexpr int NWG = 256, NTHR = 512;
constexpr int DM = 1024, NQ = 4096, FF = 3584;
constexpr int WS_E = 0;
constexpr int WS_FLAG = 4096;
constexpr int WS_LINE = 8192;
constexpr int WS_Q = WS_LINE + 3 * 32768;
constexpr int WS_H = WS_Q + 4 * 32 * 4 * 512;
constexpr int WS_O = WS_H + 4 * 4 * FF * 4;
constexpr int WS_ML = WS_O + 32 * 4 * 16 * 256;
constexpr int WS_F = WS_ML + 32 * 4 * 16 * 8;
constexpr int WS_END = WS_F + 256 * 32 * 128;
constexpr int WS_STAMP = 8 << 20;
constexpr int OOB = 0x7f000000;
constexpr int AUX_NT = 2, AUX_SC1 = 16;
constexpr unsigned POISON = 0u;
constexpr int SPIN_CAP = 1 << 14;
constexpr int L_CTR = 0;
constexpr int L_XN = 64;
constexpr int L_Q = L_XN + 2048;
constexpr int L_M = L_Q + 2048;
constexpr int L_L = L_M + 256;
constexpr int L_RED = L_L + 256;
constexpr int RED_W = 8 * 18 * 16;
constexpr int L_O = L_RED + 4 * RED_W;
constexpr int O_W = 8 * 132 * 4;
constexpr int L_H = L_O + 5 * O_W;
constexpr int L_OA = L_H + 1024;
constexpr int L_END = L_OA + 1024;

struct Params {
  const void *x, *kc, *vc, *wq, *wo, *w1, *b1, *w2, *b2;
  void *out, *ws;
};

__device__ __forceinline__ rsrc_t mk(const void* p, int bytes) { return __builtin_amdgcn_make_buffer_rsrc((void*)p, 0, bytes, 0x00020000); }
template <int AUX> __device__ __forceinline__ v4u ld16(rsrc_t r, int voff, int soff) { return __builtin_amdgcn_raw_buffer_load_b128(r, voff, soff, AUX); }
template <int AUX> __device__ __forceinline__ v2u ld8(rsrc_t r, int voff, int soff) { return __builtin_amdgcn_raw_buffer_load_b64(r, voff, soff, AUX); }
__device__ __forceinline__ void st16(v4u v, rsrc_t r, int voff, int soff) { __builtin_amdgcn_raw_buffer_store_b128(v, r, voff, soff, AUX_SC1); }
__device__ __forceinline__ void st8(v2u v, rsrc_t r, int voff, int soff) { __builtin_amdgcn_raw_buffer_store_b64(v, r, voff, soff, AUX_SC1); }
__device__ __forceinline__ f32x4 mfma(v4u a, v4u b, f32x4 c) {
  return __builtin_amdgcn_mfma_f32_16x16x32_bf16(__builtin_bit_cast(bf16x8, a), __builtin_bit_cast(bf16x8, b), c, 0, 0, 0);
}
__device__ __forceinline__ float u2f(unsigned u) { return __builtin_bit_cast(float, u); }
__device__ __forceinline__ unsigned f2u(float f) { return __builtin_bit_cast(unsigned, f); }
__device__ __forceinline__ unsigned pk(float lo, float hi) {
  f32x2 v = {lo, hi};
  return __builtin_bit_cast(unsigned, __builtin_convertvector(v, bf16x2));
}
__device__ __forceinline__ float blo(unsigned u) { return u2f(u << 16); }
__device__ __forceinline__ float bhi(unsigned u) { return u2f(u & 0xffff0000u); }
__device__ __forceinline__ float rbf(float f) { return blo(pk(f, f)); }
template <int N> __device__ __forceinline__ float ror(float x) { return u2f(__builtin_amdgcn_mov_dpp(f2u(x), 0x120 + N, 0xf, 0xf, true)); }
template <int N> __device__ __forceinline__ unsigned roru(unsigned x) { return __builtin_amdgcn_mov_dpp(x, 0x120 + N, 0xf, 0xf, true); }
__device__ __forceinline__ float xrow_sum(float x) {
  auto s = __builtin_amdgcn_permlane16_swap(f2u(x), f2u(x), false, false);
  x = u2f(s[0]) + u2f(s[1]);
  auto t = __builtin_amdgcn_permlane32_swap(f2u(x), f2u(x), false, false);
  return u2f(t[0]) + u2f(t[1]);
}
__device__ __forceinline__ float xrow_max(float x) {
  auto s = __builtin_amdgcn_permlane16_swap(f2u(x), f2u(x), false, false);
  x = fmaxf(u2f(s[0]), u2f(s[1]));
  auto t = __builtin_amdgcn_permlane32_swap(f2u(x), f2u(x), false, false);
  return fmaxf(u2f(t[0]), u2f(t[1]));
}
__device__ __forceinline__ float wave_sum(float x) {
  x += ror<8>(x); x += ror<4>(x); x += ror<2>(x); x += ror<1>(x);
  return xrow_sum(x);
}
__device__ __forceinline__ unsigned umin4(v4u v) { return min(min(v.x, v.y), min(v.z, v.w)); }
__device__ __forceinline__ v4u nopoison(f32x4 v) {
  v4u u = __builtin_bit_cast(v4u, v);
  return v4u{max(u.x, 1u), max(u.y, 1u), max(u.z, 1u), max(u.w, 1u)};
}
#define PIN() __builtin_amdgcn_sched_barrier(0)
#define BAR() do { PIN(); asm volatile("s_waitcnt lgkmcnt(0)\n\ts_barrier" ::: "memory"); PIN(); } while (0)
#define REREAD() asm volatile("" ::: "memory")
__device__ __forceinline__ v4u tr8(const v4u* w, int j0) {
  const unsigned sel = (j0 & 1) ? 0x07060302u : 0x05040100u;
  const int c = j0 >> 1;
  return v4u{__builtin_amdgcn_perm(w[1][c], w[0][c], sel), __builtin_amdgcn_perm(w[3][c], w[2][c], sel),
             __builtin_amdgcn_perm(w[5][c], w[4][c], sel), __builtin_amdgcn_perm(w[7][c], w[6][c], sel)};
}
__device__ __forceinline__ void ln_stats(const float (&v)[16], float& m, float& inv) {
  float s = 0.f;
#pragma unroll
  for (int i = 0; i < 16; ++i) s += v[i];
  const float mf = wave_sum(s) * (1.0f / DM);
  float ss = 0.f;
#pragma unroll
  for (int i = 0; i < 16; ++i) { const float d = v[i] - mf; ss = fmaf(d, d, ss); }
  const float var = rbf(wave_sum(ss) * (1.0f / DM));
  m = rbf(mf);
  inv = __builtin_amdgcn_rcpf(rbf(__builtin_amdgcn_sqrtf(rbf(var + 1e-5f))));
}
__device__ __forceinline__ unsigned ln_pair(float a, float b, float m, float inv) {
  const unsigned d = pk(a - m, b - m);
  return pk(blo(d) * inv, bhi(d) * inv);
}
__device__ __forceinline__ unsigned lds_arrive(LAS unsigned* c, int lane) {
  unsigned old = 0;
  REREAD();
  if (lane == 0) old = __hip_atomic_fetch_add(c, 1u, __ATOMIC_RELAXED, __HIP_MEMORY_SCOPE_WORKGROUP);
  return __builtin_amdgcn_readfirstlane(old);
}
template <int SL = 1> __device__ __forceinline__ void class_bar(LAS unsigned* c, int lane) {
  PIN();
  if (lds_arrive(c, lane) != 3u) {
    _Pragma("nounroll") while (__builtin_amdgcn_readfirstlane(__hip_atomic_load(c, __ATOMIC_RELAXED, __HIP_MEMORY_SCOPE_WORKGROUP)) < 4u)
      __builtin_amdgcn_s_sleep(SL);
  }
  REREAD();
  PIN();
}
template <int X> __device__ __forceinline__ void dyn_pace(int lag) {
  asm volatile("s_cmp_gt_i32 %0, %1\n\ts_cbranch_scc1 1f\n\ts_waitcnt vmcnt(%3)\n1:\n\ts_cmp_gt_i32 %0, %2\n\ts_cbranch_scc1 2f\n\ts_waitcnt vmcnt(%4)\n2:\n\ts_waitcnt vmcnt(%5)"
               :: "s"(lag), "n"(TH1), "n"(TH2), "n"(W0 - 1 + X), "n"(W0 + WS1 - 1 + X), "n"(W0 + WS2 - 1 + X) : "memory", "scc");
}
__device__ __forceinline__ int now32() { return (int)(unsigned)__builtin_amdgcn_s_memrealtime(); }
template <int N> __device__ __forceinline__ void pace() { asm volatile("s_waitcnt vmcnt(%0)" :: "n"(N) : "memory"); }

#define STAMP(i) do { PIN(); ts_[i] = (unsigned)__builtin_amdgcn_s_memrealtime(); PIN(); } while (0)
#define STAMPV(i, v) do { ts_[i] = (unsigned)(v); } while (0)
#define STAMP_FLUSH() do { if (lane == 0) { _Pragma("unroll") for (int i_ = 0; i_ < 20; ++i_) ((GAS unsigned long long*)((GAS char*)p.ws + WS_STAMP))[(blockIdx.x * 8 + w) * 20 + i_] = ts_[i_]; } } while (0)
#undef STAMP_FLUSH
#define STAMP_FLUSH() do { if (w == DIAGW && (DIAGB < 0 ? ci < 30 : (int)blockIdx.x == DIAGB)) { const unsigned lim_ = DIAGS > 0 ? 6000u + 8u * (ts_[DIAG] - ts_[0]) : 30000u - 8u * (ts_[DIAG] - ts_[0]); \
    while ((unsigned)__builtin_amdgcn_s_memrealtime() - ts_[0] < lim_ && lim_ < 100000u) __builtin_amdgcn_s_sleep(1); } } while (0)

__global__ void __launch_bounds__(NTHR, 2) fused_decode(Params p) {
  __shared__ __attribute__((aligned(16))) unsigned char smem[L_END];
  LAS unsigned char* L = (LAS unsigned char*)smem;
  LAS unsigned* ctr = (LAS unsigned*)(L + L_CTR);
  const int tid = threadIdx.x, lane = tid & 63;
  const int w = __builtin_amdgcn_readfirstlane(tid >> 6);
  const int wl = w & 3;
  const bool isA = w < 4;
  const int xs = blockIdx.x & 7, ci = blockIdx.x >> 3;
  const int n16 = lane & 15, g = lane >> 4;
  unsigned ts_[20] = {0, 0, 0, 0, 0, 0, 0, 0, 0, 0, 0, 0, 0, 0, 0, 0, 0, 0, 0, 0};
  STAMP(0);
  const int tbase = DYN ? now32() + SB : 0;
  int lag = 0;
  unsigned cpf = 0, tpf = 0;
  {
    unsigned long long pc; int len;
    asm volatile("s_getpc_b64 %0\n.Lpf_base%=:\n\ts_mov_b32 %1, .Lfd_code_end-.Lpf_base%=" : "=s"(pc), "=s"(len));
    if (tid < 5) cpf = __builtin_amdgcn_raw_buffer_load_b32(mk((const void*)pc, len), (tid * 32 + ci) * 128, 0, 0);
  }
  PIN();
  asm volatile("" :: "s"(p.x), "s"(p.kc), "s"(p.vc), "s"(p.wq), "s"(p.wo), "s"(p.w1), "s"(p.b1), "s"(p.w2), "s"(p.b2), "s"(p.out), "s"(p.ws));
  v2u xr[4];
  if (isA) {
    const rsrc_t rx = mk(p.x, 4 * DM * 2);
#pragma unroll
    for (int i = 0; i < 4; ++i) xr[i] = ld8<0>(rx, lane * 8, wl * 2048 + ((i + (xs >> 1)) & 3) * 512);
  }
  PIN();
  const rsrc_t rws = mk(p.ws, WS_END);
  const int gidx = 2 * xs + (ci >> 4), ab = gidx >> 2, kvh = gidx & 3, sp = ci & 15;
  const int kq = xs >> 1, isq = ci < 16, cb = isq ? 16 * (xs & 1) + ci : 14 * (xs & 1) + ci - 16;
  const int hd = 4 * xs + (ci >> 3), cg = ci & 7;
  const int cu = xs * 32 + ci;
  const size_t kvbase = ((size_t)gidx * 16 + sp) * 262144;
  unsigned long long gen = *(const __attribute__((address_space(4))) unsigned long long*)p.ws;
#define MAKE_TAG() const unsigned long long tag64 = (gen + 1ull) * 0x9E3779B97F4A7C15ull; \
  const unsigned tag0 = (unsigned)tag64, tag1 = (unsigned)(tag64 >> 32)

  const int hc = lane & 7, hbt = (lane >> 3) & 3, hb = 112 * hd + 28 * wl + 4 * hc;
  const int fb = FMODE == 2 ? 2 * (lane & 1) + ((lane >> 3) & 1) : (lane >> 2) & 3, fc = 4 * cu + (FMODE == 2 ? (lane >> 1) & 3 : lane & 3);
  v2u sm;
  if (isA) {
    if (tid < 8) ctr[tid] = 0u;
    PIN();
    float v[16];
#pragma unroll
    for (int i = 0; i < 8; ++i) { v[2 * i] = blo(xr[i >> 1][i & 1]); v[2 * i + 1] = bhi(xr[i >> 1][i & 1]); }
    asm volatile("" : "+v"(v[0]), "+v"(v[15]));
    STAMP(1);
    float m, inv;
    ln_stats(v, m, inv);
    if (!isq) {
#pragma unroll
      for (int i = 0; i < 16; i += 2) { const unsigned o = ln_pair(v[i], v[i + 1], m, inv); v[i] = blo(o); v[i + 1] = bhi(o); }
      ln_stats(v, m, inv);
    }
    *(LAS v2u*)(L + L_XN + wl * 512 + lane * 8) = v2u{ln_pair(v[0], v[1], m, inv), ln_pair(v[2], v[3], m, inv)};
    asm volatile("" : "+s"(gen));
    STAMP(2);
    BAR();
    asm volatile("" :: "v"(cpf), "v"(tpf));
  } else {
    const int ldw = isq ? NQ * 2 : FF * 2;
    const rsrc_t rw = mk(isq ? p.wq : p.w1, DM * ldw);
    const int wo_ = ci < 30 ? (256 * kq + 64 * wl + 8 * g) * ldw + cb * 256 + n16 * 16 : OOB;
    v4u wv[16];
#pragma unroll
    for (int j = 0; j < 16; ++j) {
      if (PZPOS && j == PZPOS && !(ABL & 1)) {
        const v4u pz = {POISON, POISON, POISON, POISON};
        st16(pz, rws, wl < 2 ? WS_O + (((8 * kvh + 4 * wl + g) * 4 + ab) * 16 + sp) * 256 + n16 * 16
                             : wl == 2 && lane < 4 ? WS_ML + (gidx * 16 + sp) * 64 + lane * 16 : OOB, 0);
        if (FMODE == 0) st16(pz, rws, wl >= 2 ? WS_F + ((32 * cg + 16 * (wl & 1) + (lane >> 2)) * 32 + hd) * 64 + (lane & 3) * 16 : OOB, 0);
        PIN();
      }
      if (PZPOS && j >= WINW && j < WINW + PZPOS) pace<WINW + (FMODE == 0)>();
      else if (j >= WINW) pace<WINW - 1>();
      wv[j] = ld16<AUX_NT>(rw, wo_, ((j >> 3) * 32 + (j & 7)) * ldw);
      PIN();
    }
    asm volatile("" : "+s"(gen));
    STAMP(1);
    BAR();
    STAMP(2);
    f32x4 d[8];
#pragma unroll
    for (int t = 0; t < 2; ++t) {
      const v4u act = *(const LAS v4u*)(L + L_XN + (n16 & 3) * 512 + (64 * wl + 32 * t + 8 * g) * 2);
#pragma unroll
      for (int j0 = 0; j0 < 8; ++j0) d[j0] = mfma(act, tr8(wv + 8 * t, j0), t ? d[j0] : f32x4{0.f, 0.f, 0.f, 0.f});
      PIN();
    }
    if (lane < 16) {
#pragma unroll
      for (int j0 = 0; j0 < 8; ++j0) *(LAS f32x4*)(L + L_RED + wl * RED_W + (j0 * 17 + lane) * 16) = d[j0];
    }
    STAMP(3);
    class_bar(ctr + 0, lane);
    if (wl < 2) {
      const int b = g, jh = lane & 1, nn = wl * 8 + (n16 >> 1);
      f32x4 sum = {0.f, 0.f, 0.f, 0.f};
#pragma unroll
      for (int ww = 0; ww < 4; ++ww)
#pragma unroll
        for (int k = 0; k < 4; ++k) sum[k] += *(const LAS float*)(L + L_RED + ww * RED_W + ((4 * jh + k) * 17 + nn) * 16 + b * 4);
      const int co = (8 * nn + 4 * jh) * 4;
      const int off = isq ? WS_Q + ((b * 32 + cb) * 4 + kq) * 512 + co : (ci < 30 ? WS_H + ((kq * 4 + b) * FF + cb * 128) * 4 + co : OOB);
      st16(__builtin_bit_cast(v4u, sum), rws, (ABL & 4) ? OOB : off, 0);
    }
    if (!(ABL & 1) && !PZPOS) {
        const v4u pz = {POISON, POISON, POISON, POISON};
        st16(pz, rws, wl < 2 ? WS_O + (((8 * kvh + 4 * wl + g) * 4 + ab) * 16 + sp) * 256 + n16 * 16
                             : wl == 2 && lane < 4 ? WS_ML + (gidx * 16 + sp) * 64 + lane * 16 : OOB, 0);
        if (FMODE == 0) st16(pz, rws, wl >= 2 ? WS_F + ((32 * cg + 16 * (wl & 1) + (lane >> 2)) * 32 + hd) * 64 + (lane & 3) * 16 : OOB, 0);
        PIN();
      }
    STAMP(4);
    sm.x = __builtin_amdgcn_raw_buffer_load_b16(mk(p.x, 4 * DM * 2), wl == 2 && !(ABL & 2) ? (fb * DM + fc) * 2 : OOB, 0, 0);
    sm.y = __builtin_amdgcn_raw_buffer_load_b16(mk(p.b2, DM * 2), wl == 2 && !(ABL & 2) ? fc * 2 : OOB, 0, 0);
    pace<0>();
    if (lds_arrive(ctr + 1, lane) == 3u) {
      MAKE_TAG();
      st8(v2u{tag0, tag1}, rws, lane == 0 ? WS_FLAG + cu * 8 : lane == 1 ? WS_LINE + cu * 128 : OOB, 0);
    }
    STAMP(5);
    BAR();
  }
  const rsrc_t rk = mk((const char*)p.kc + kvbase, 262144), rv = mk((const char*)p.vc + kvbase, 262144);
  const int koff = (w >> 2) * 131072 + wl * 4096 + ((lane >> 2) & 3) * 256 + g * 64 + (lane & 3) * 16;
  const int voff = (w >> 2) * 131072 + wl * 4096 + lane * 16;
  v4u r[32];
  v2u fq;
  const int fhd = 8 * kvh + 2 * wl + ((lane >> 2) & 1);
  const int fqo = WS_LINE + ((2 * (lane & 3) + (fhd >> 4)) * 32 + (fhd & 15)) * 128;
  __builtin_amdgcn_s_setprio(1);
#pragma unroll
  for (int i = 0; i < 32; ++i) {
    if (i == FLPOS) { fq = ld8<AUX_SC1>(rws, isA && lane < 8 ? fqo : OOB, 0); PIN(); }
    if (DYN && i >= W0) { if (i >= FLPOS) dyn_pace<1>(lag); else dyn_pace<0>(lag); }
    else if (i >= FLPOS && i < FLPOS + WINK) pace<WINK>();
    else if (i >= WINK) pace<WINK - 1>();
    else if (i > 0 && RAMP) __builtin_amdgcn_s_sleep(RAMP);
    r[i] = ld16<AUX_NT>(rk, koff, (i >> 2) * 16384 + (i & 3) * 1024);
    PIN();
    if (DYN && (i % LSTEP) == 0) { lag = now32() - tbase - (isA ? S2 + (S3 - S2) * i / 32 : S3 + T3 * i / 32); asm volatile("" : "+s"(lag)); PIN(); }
  }
  __builtin_amdgcn_s_setprio(0);
  STAMP(6);
  BAR();
  MAKE_TAG();
  if (DYN == 2 && wl == 0) __builtin_amdgcn_raw_buffer_store_b32((unsigned)now32() ^ tag0, rws, lane == 0 ? WS_LINE + (1 + (w >> 2)) * 32768 + cu * 128 : OOB, 0, AUX_SC1);
  if (isA) {
    int spins = 0;
    if (__any(lane < 8 && ((fq.x ^ tag0) | (fq.y ^ tag1)) != 0u)) {
      _Pragma("nounroll") while (true) {
        REREAD();
        const v2u a = ld8<AUX_SC1>(rws, lane < 8 ? fqo : OOB, 0);
        if (++spins > SPIN_CAP || !__any(lane < 8 && ((a.x ^ tag0) | (a.y ^ tag1)) != 0u)) break;
        __builtin_amdgcn_s_sleep(2);
      }
    }
    REREAD();
    STAMPV(18, spins);
    STAMP(3);
    const int qo = WS_Q + ((ab * 32 + 8 * kvh + 2 * wl + (lane >> 5)) * 4) * 512 + (lane & 31) * 16;
    v4u qp[4];
#pragma unroll
    for (int s = 0; s < 4; ++s) qp[s] = ld16<AUX_SC1>(rws, (ABL & 8) ? OOB : qo, s * 512);
    PIN();
    const f32x4 qs = (__builtin_bit_cast(f32x4, qp[0]) + __builtin_bit_cast(f32x4, qp[1])) + (__builtin_bit_cast(f32x4, qp[2]) + __builtin_bit_cast(f32x4, qp[3]));
    *(LAS v2u*)(L + L_Q + (2 * wl + (lane >> 5)) * 256 + (lane & 31) * 8) = v2u{pk(qs[0], qs[1]), pk(qs[2], qs[3])};
    class_bar(ctr + 2, lane);
    STAMP(4);
  }
  const int hl = n16 & 7, wb = n16 >> 3;
  const v4u qb0 = *(const LAS v4u*)(L + L_Q + hl * 256 + (4 * g + wb) * 16);
  const v4u qb1 = *(const LAS v4u*)(L + L_Q + hl * 256 + (4 * g + 2 + wb) * 16);
#define PRLOAD() pr = __builtin_amdgcn_raw_buffer_load_b32(rws, wl == 1 && lane < NSMP ? WS_LINE + (lane * (256 / NSMP) + (ci & (256 / NSMP - 1))) * 128 : OOB, (1 + (w >> 2)) * 32768, AUX_SC1)
  float s[16];
#pragma unroll
  for (int u = 0; u < 8; ++u) {
    float t0[4], t1[4];
#pragma unroll
    for (int k = 0; k < 4; ++k) {
      const f32x4 d0 = mfma(r[4 * u + k], qb0, f32x4{0.f, 0.f, 0.f, 0.f});
      const f32x4 d1 = mfma(r[4 * u + k], qb1, f32x4{0.f, 0.f, 0.f, 0.f});
      t0[k] = d0[0] + d1[2];
      t1[k] = d0[1] + d1[3];
    }
#pragma unroll
    for (int k = 0; k < 4; k += 2) {
      const float xx = wb ? t1[k + 1] : t0[k], yy = wb ? t1[k] : t0[k + 1];
      s[2 * u + (k >> 1)] = xx + ror<8>(yy);
    }
    if (QKPIN) PIN();
  }
  unsigned pr = 0u;
  if (DYN == 2 && PRPOS == 0) { PIN(); PRLOAD(); PIN(); }
  float m = -INFINITY;
#pragma unroll
  for (int i = 0; i < 16; i += 2) {
    const unsigned a = pk(s[i], s[i + 1]);
    const unsigned c = pk(blo(a) * 0.08838834764831845f, bhi(a) * 0.08838834764831845f);
    s[i] = blo(c); s[i + 1] = bhi(c);
    m = fmaxf(m, fmaxf(s[i], s[i + 1]));
  }
  m = fmaxf(m, ror<8>(m));
  m = xrow_max(m);
  if (lane < 8) *(LAS float*)(L + L_M + (w * 8 + lane) * 4) = m;
  constexpr float LOG2E = 1.4426950408889634f;
  if (!isA) {
    if (wl == 0) {
      int spins = 0;
      _Pragma("nounroll") while (true) {
        REREAD();
        const v4u a = ld16<AUX_SC1>(rws, WS_FLAG + lane * 16, 0), b = ld16<AUX_SC1>(rws, WS_FLAG + lane * 16, 1024);
        const unsigned bad = (a.x ^ tag0) | (a.y ^ tag1) | (a.z ^ tag0) | (a.w ^ tag1) | (b.x ^ tag0) | (b.y ^ tag1) | (b.z ^ tag0) | (b.w ^ tag1);
        if (!__any(bad != 0u) || ++spins > SPIN_CAP) break;
        __builtin_amdgcn_s_sleep(4);
      }
      REREAD();
      if (blockIdx.x == 0) st8(v2u{(unsigned)(gen + 1ull), (unsigned)((gen + 1ull) >> 32)}, rws, lane == 0 ? WS_E : OOB, 0);
    }
    class_bar(ctr + 3, lane);
#pragma unroll
    for (int ww = 0; ww < 8; ++ww) m = fmaxf(m, *(const LAS float*)(L + L_M + (ww * 8 + hl) * 4));
  }
  unsigned pb[16];
  float lsum = 0.f;
  {
    const unsigned msk = wb ? 0xffff0000u : 0x0000ffffu;
#pragma unroll
    for (int i = 0; i < 16; ++i) {
      const float e = __builtin_amdgcn_exp2f((s[i] - m) * LOG2E);
      lsum += e;
      const unsigned t = pk(e, e) & msk;
      pb[i] = t | roru<8>(t);
      asm volatile("" : "+v"(pb[i]));
    }
    lsum += ror<8>(lsum);
    lsum = xrow_sum(lsum);
    if (lane < 8) *(LAS float*)(L + L_L + (w * 8 + lane) * 4) = lsum;
  }
  f32x4 acc[8];
#pragma unroll
  for (int j = 0; j < 8; ++j) acc[j] = f32x4{0.f, 0.f, 0.f, 0.f};
  int tref = tbase + S3 + T3 + (isA ? 0 : T4);
  if (DYN == 2 && wl == 1) {
    if (PRPOS == 1) { if (!isA) __builtin_amdgcn_s_sleep(PRSL); PRLOAD(); }
    const unsigned tn = (unsigned)now32();
    const unsigned age = tn - (pr ^ tag0);
    const bool ok = age < 800u && lane < NSMP;
    const float mean = wave_sum(ok ? (float)age : 0.f) * __builtin_amdgcn_rcpf(fmaxf(wave_sum(ok ? 1.f : 0.f), 1.f));
    if (lane == 0) ctr[8 + (w >> 2)] = tn - (int)mean + (isA ? T3 : T4) + SB2;
  }
  STAMP(7);
  BAR();
  STAMP(15);
constexpr int PVLAG = DYN ? W0 + WS2 : WINV;
#define PV(n) acc[(n) & 7] = mfma(tr8(&r[(n) & ~7], (n) & 7), v4u{pb[((n) >> 3) * 4], pb[((n) >> 3) * 4 + 1], pb[((n) >> 3) * 4 + 2], pb[((n) >> 3) * 4 + 3]}, acc[(n) & 7])
  __builtin_amdgcn_s_setprio(1);
#pragma unroll
  for (int i = 0; i < 32; ++i) {
    if (DYN && i >= W0) dyn_pace<0>(lag);
    else if (i >= WINV) pace<WINV - 1>();
    else if (i > 0 && RAMP) __builtin_amdgcn_s_sleep(RAMP);
    r[i] = ld16<AUX_NT>(rv, voff, (i >> 2) * 16384 + (i & 3) * 1024);
    PIN();
    if (DYN && (i % LSTEP) == 0) { if (i == 0 && DYN == 2) tref = __builtin_amdgcn_readfirstlane(ctr[8 + (w >> 2)]); lag = now32() - tref - (isA ? T4 * i / 32 : T5 * i / 32); asm volatile("" : "+s"(lag)); PIN(); }
    if (i >= 7 + PVLAG) { PV(i - 7 - PVLAG); PIN(); }
  }
  const int mo = WS_ML + ((wl * 4 + (hd >> 3)) * 16 + ((n16 + 4 * g) & 15)) * 64 + (hd & 7) * 8;
  v2u ml = ld8<AUX_SC1>(rws, isA ? OOB : mo, 0);
  STAMP(16);
  BAR();
#pragma unroll
  for (int n = 32 - 7 - PVLAG; n < 32; ++n) { PV(n); if ((n & 7) == 7) PIN(); }
  STAMP(8);
#define PUT_ACC(f) _Pragma("unroll") for (int i = 0; i < 4; ++i) { \
      const f32x4 t = {wb ? acc[4][i] : acc[0][i], wb ? acc[5][i] : acc[1][i], wb ? acc[6][i] : acc[2][i], wb ? acc[7][i] : acc[3][i]}; \
      *(LAS f32x4*)(L + L_O + wl * O_W + (hl * 132 + 8 * (4 * g + i) + 4 * wb) * 4) = f t; }
  const int oro = ((2 * wl + (g & 1)) * 132 + 8 * n16 + 4 * (g >> 1)) * 4;
  if (isA) {
    __builtin_amdgcn_s_setprio(0);
    float M = m;
#pragma unroll
    for (int ww = 0; ww < 8; ++ww) M = fmaxf(M, *(const LAS float*)(L + L_M + (ww * 8 + hl) * 4));
    const float fa = __builtin_amdgcn_exp2f((m - M) * LOG2E);
    PUT_ACC(fa *);
    if (lane < 8) *(LAS float*)(L + L_L + (w * 8 + lane) * 4) = fa * lsum;
    class_bar(ctr + 4, lane);
    {
      f32x4 t = *(const LAS f32x4*)(L + L_O + oro);
#pragma unroll
      for (int ww = 1; ww < 4; ++ww) t += *(const LAS f32x4*)(L + L_O + ww * O_W + oro);
      *(LAS f32x4*)(L + L_O + 4 * O_W + oro) = t;
    }
    if (w == 0) {
      float l = 0.f;
#pragma unroll
      for (int ww = 0; ww < 8; ++ww) l += *(const LAS float*)(L + L_L + (ww * 8 + hl) * 4);
      st8(v2u{max(f2u(M), 1u), max(f2u(l), 1u)}, rws, lane < 8 ? WS_ML + (gidx * 16 + sp) * 64 + lane * 8 : OOB, 0);
    }
    {
    const int ho = hc < 7 ? WS_H + ((8 * (lane >> 5) + hbt) * FF + hb) * 4 : OOB;
    v4u hp[2];
#pragma unroll
    for (int s = 0; s < 2; ++s) hp[s] = ld16<AUX_SC1>(rws, (ABL & 8) ? OOB : ho, s * 4 * FF * 4);
    sm = ld8<0>(mk(p.b1, FF * 2), hc < 7 && !(ABL & 2) ? hb * 2 : OOB, 0);
    PIN();
    float hv[4];
#pragma unroll
    for (int j = 0; j < 4; ++j) {
      const float t0 = u2f(hp[0][j]) + u2f(hp[1][j]);
      auto sw = __builtin_amdgcn_permlane32_swap(f2u(t0), f2u(t0), false, false);
      const float t = u2f(sw[0]) + u2f(sw[1]);
      const float u = rbf(rbf(t) + ((j & 1) ? bhi(sm[j >> 1]) : blo(sm[j >> 1])));
      const float z2 = u * fmaf(u * u, 0.044715f, 1.0f) * (-2.0f * 0.7978845608028654f * 1.4426950408889634f);
      hv[j] = u * __builtin_amdgcn_rcpf(1.0f + __builtin_amdgcn_exp2f(z2));
    }
    if (lane < 32) *(LAS v2u*)(L + L_H + wl * 256 + hbt * 64 + hc * 8) = v2u{pk(hv[0], hv[1]), pk(hv[2], hv[3])};
    }
    const rsrc_t r2 = mk(p.w2, FF * DM * 2), ro = mk(p.wo, NQ * DM * 2);
    const int o2 = (112 * hd + 28 * wl + 8 * g) * 2048 + cg * 256 + n16 * 16;
    const int o2b = g < 3 ? o2 : OOB;
    const int oo = (128 * hd + 32 * wl + 8 * g) * 2048 + cg * 256 + n16 * 16;
    const v4u hact = *(const LAS v4u*)(L + L_H + wl * 256 + (n16 & 3) * 64 + g * 16);
    v4u tw[16];
#define TLD(j) do { if ((j) >= WINT) pace<WINT - 1>(); else if ((j) > 0 && (j) < NPRE && RAMP) __builtin_amdgcn_s_sleep(RAMP); tw[j] = (j) < 8 ? ld16<AUX_NT>(r2, (j) < 4 ? o2 : o2b, (j) * 2048) : ld16<AUX_NT>(ro, oo, ((j) - 8) * 2048); PIN(); } while (0)
    STAMP(5);
    BAR();
#pragma unroll
    for (int j = 0; j < NPRE; ++j) TLD(j);
    STAMP(9);
    if (!CB4) BAR();
#pragma unroll
    for (int j = NPRE; j < 16; ++j) TLD(j);
    f32x4 d[8];
#pragma unroll
    for (int j0 = 0; j0 < 8; ++j0) d[j0] = mfma(hact, tr8(tw, j0), f32x4{0.f, 0.f, 0.f, 0.f});
    v4u wt[8];
#pragma unroll
    for (int j0 = 0; j0 < 8; ++j0) { wt[j0] = tr8(tw + 8, j0); asm volatile("" : "+v"(wt[j0])); }
    STAMP(10);
    BAR();
    STAMP(11);
    const v4u oact = *(const LAS v4u*)(L + L_OA + (n16 & 3) * 256 + (32 * wl + 8 * g) * 2);
#pragma unroll
    for (int j0 = 0; j0 < 8; ++j0) d[j0] = mfma(oact, wt[j0], d[j0]);
    if (lane < 16) {
#pragma unroll
      for (int j0 = 0; j0 < 8; ++j0) *(LAS f32x4*)(L + L_RED + wl * RED_W + (j0 * 18 + lane) * 16) = d[j0];
    }
    BAR();
    STAMP(12);
    asm volatile(".Lfd_code_end:");
  } else {
    PUT_ACC();
    if (CB4) class_bar<0>(ctr + 6, lane); else BAR();
    STAMP(9);
    {
      f32x4 t = *(const LAS f32x4*)(L + L_O + 4 * O_W + oro);
#pragma unroll
      for (int ww = 0; ww < 4; ++ww) t += *(const LAS f32x4*)(L + L_O + ww * O_W + oro);
      const unsigned a0 = max(pk(t[0], t[1]), 1u), a1 = max(pk(t[2], t[3]), 1u);
      auto s0 = __builtin_amdgcn_permlane32_swap(a0, a0, false, false);
      auto s1 = __builtin_amdgcn_permlane32_swap(a1, a1, false, false);
      st16(v4u{s0[0], s1[0], s0[1], s1[1]}, rws, lane < 32 ? WS_O + (((8 * kvh + 2 * wl + g) * 4 + ab) * 16 + sp) * 256 + n16 * 16 : OOB, 0);
    }
    PIN();
    STAMP(10);
    __builtin_amdgcn_s_setprio(0);
    {
      int spins = 0;
      if (__any(min(ml.x, ml.y) == POISON)) {
        _Pragma("nounroll") while (true) {
          REREAD();
          ml = ld8<AUX_SC1>(rws, mo, 0);
          if (!__any(min(ml.x, ml.y) == POISON) || ++spins > SPIN_CAP) break;
          __builtin_amdgcn_s_sleep(2);
        }
      }
      STAMPV(19, spins);
      float M = u2f(ml.x);
      M = fmaxf(M, ror<8>(M)); M = fmaxf(M, ror<4>(M)); M = fmaxf(M, ror<2>(M)); M = fmaxf(M, ror<1>(M));
      const float e = __builtin_amdgcn_exp2f((u2f(ml.x) - M) * LOG2E);
      float Ls = e * u2f(ml.y);
      Ls += ror<8>(Ls); Ls += ror<4>(Ls); Ls += ror<2>(Ls); Ls += ror<1>(Ls);
      float f = e * __builtin_amdgcn_rcpf(Ls);
      asm volatile("" : "+v"(f));
      if (ODELAY) __builtin_amdgcn_s_sleep(ODELAY);
      const int oo_ = WS_O + ((hd * 4 + wl) * 16 + 4 * g) * 256 + n16 * 16;
      v4u op[4];
      int of[4];
#pragma unroll
      for (int k = 0; k < 4; ++k) { op[k] = v4u{0u, 0u, 0u, 0u}; of[k] = oo_; }
      spins = 0;
      _Pragma("nounroll") while (true) {
        REREAD();
        v4u nw[4];
#pragma unroll
        for (int k = 0; k < 4; ++k) nw[k] = ld16<AUX_SC1>(rws, of[k], k * 256);
        PIN();
        bool bad = false;
#pragma unroll
        for (int k = 0; k < 4; ++k) {
          op[k] |= nw[k];
          const bool b = umin4(op[k]) == POISON;
          of[k] = b ? oo_ : OOB;
          bad |= b;
        }
        if (!__any(bad) || ++spins > SPIN_CAP) break;
        __builtin_amdgcn_s_sleep(OSLEEP);
      }
      STAMPV(18, spins);
      STAMP(13);
      float t[8] = {0.f, 0.f, 0.f, 0.f, 0.f, 0.f, 0.f, 0.f};
#define OACC(k) do { const float fk = u2f(__builtin_amdgcn_mov_dpp(f2u(f), 0x150 + (k), 0xf, 0xf, true)); \
        _Pragma("unroll") for (int j = 0; j < 4; ++j) { t[2 * j] = fmaf(fk, blo(op[k][j]), t[2 * j]); t[2 * j + 1] = fmaf(fk, bhi(op[k][j]), t[2 * j + 1]); } } while (0)
      OACC(0); OACC(1); OACC(2); OACC(3);
      float u[4];
#pragma unroll
      for (int j = 0; j < 4; ++j) { auto sw = __builtin_amdgcn_permlane32_swap(f2u(t[j]), f2u(t[4 + j]), false, false); u[j] = u2f(sw[0]) + u2f(sw[1]); }
      auto s0 = __builtin_amdgcn_permlane16_swap(f2u(u[0]), f2u(u[2]), false, false);
      auto s1 = __builtin_amdgcn_permlane16_swap(f2u(u[1]), f2u(u[3]), false, false);
      *(LAS unsigned*)(L + L_OA + wl * 256 + n16 * 16 + g * 4) = pk(u2f(s0[0]) + u2f(s0[1]), u2f(s1[0]) + u2f(s1[1]));
      if (wl != 2) __builtin_amdgcn_s_setprio(2);
    }
    BAR();
    BAR();
    if (FMODE == 2) {
      const int bp = lane & 1, j0 = (lane >> 1) & 7, nn = wl * 4 + g;
      f32x2 t = *(const LAS f32x2*)(L + L_RED + (j0 * 18 + nn) * 16 + bp * 8);
#pragma unroll
      for (int ww = 1; ww < 4; ++ww) t += *(const LAS f32x2*)(L + L_RED + ww * RED_W + (j0 * 18 + nn) * 16 + bp * 8);
      st16(v4u{f2u(t[0]), tag0, f2u(t[1]), tag1}, rws, WS_F + ((32 * cg + 2 * nn + (j0 >> 2)) * 32 + hd) * 128 + (lane & 7) * 16, 0);
    }
    if (FMODE != 2 && wl < 2) {
      const int j0 = lane & 7, nn = wl * 8 + (lane >> 3);
      f32x4 t = *(const LAS f32x4*)(L + L_RED + (j0 * 18 + nn) * 16);
#pragma unroll
      for (int ww = 1; ww < 4; ++ww) t += *(const LAS f32x4*)(L + L_RED + ww * RED_W + (j0 * 18 + nn) * 16);
      st16(FMODE == 1 ? v4u{pk(t[0], t[1]), tag0, pk(t[2], t[3]), tag1} : nopoison(t), rws, WS_F + ((32 * cg + 2 * nn + (j0 >> 2)) * 32 + hd) * 64 + (j0 & 3) * 16, 0);
      STAMP(11);
    } else if (wl == 2) {
      v4u f0, f1, f2, f3;
      int spins = 0;
      _Pragma("nounroll") while (true) {
        REREAD();
        f0 = ld16<AUX_SC1>(rws, WS_F + cu * (FMODE == 2 ? 4096 : 2048) + lane * 16, 0);
        f1 = ld16<AUX_SC1>(rws, WS_F + cu * (FMODE == 2 ? 4096 : 2048) + lane * 16, 1024);
        if (FMODE == 2) {
          f2 = ld16<AUX_SC1>(rws, WS_F + cu * 4096 + lane * 16, 2048);
          f3 = ld16<AUX_SC1>(rws, WS_F + cu * 4096 + lane * 16, 3072);
        }
        bool bad;
        if (FMODE == 0) bad = min(umin4(f0), umin4(f1)) == POISON;
        else bad = ((f0.y ^ tag0) | (f0.w ^ tag1) | (f1.y ^ tag0) | (f1.w ^ tag1)) != 0u;
        if (FMODE == 2) bad |= ((f2.y ^ tag0) | (f2.w ^ tag1) | (f3.y ^ tag0) | (f3.w ^ tag1)) != 0u;
        if (!__any(bad) || ++spins > SPIN_CAP) break;
        __builtin_amdgcn_s_sleep(1);
      }
      STAMPV(17, spins);
      STAMP(11);
      float y;
      if (FMODE == 2) {
        const float y0 = (u2f(f0.x) + u2f(f1.x)) + (u2f(f2.x) + u2f(f3.x)), y1 = (u2f(f0.z) + u2f(f1.z)) + (u2f(f2.z) + u2f(f3.z));
        y = xrow_sum((wb ? y1 : y0) + ror<8>(wb ? y0 : y1));
      } else {
        f32x4 t = FMODE == 1 ? f32x4{blo(f0.x) + blo(f1.x), bhi(f0.x) + bhi(f1.x), blo(f0.z) + blo(f1.z), bhi(f0.z) + bhi(f1.z)}
                             : __builtin_bit_cast(f32x4, f0) + __builtin_bit_cast(f32x4, f1);
        const bool h8 = lane & 8, h4 = lane & 4;
        const float a0 = (h8 ? t[2] : t[0]) + ror<8>(h8 ? t[0] : t[2]), a1 = (h8 ? t[3] : t[1]) + ror<8>(h8 ? t[1] : t[3]);
        const float k = h4 ? a1 : a0, o = h4 ? a0 : a1;
        const float o4 = u2f(__builtin_amdgcn_mov_dpp(f2u(o), 0x141, 0xf, 0xf, true));
        y = xrow_sum(k + u2f(__builtin_amdgcn_mov_dpp(f2u(o4), 0x1B, 0xf, 0xf, true)));
      }
      y += blo(sm.x) + blo(sm.y);
      __builtin_amdgcn_raw_buffer_store_b16((unsigned short)pk(y, y), mk(p.out, 4 * DM * 2), lane < 16 ? (fb * DM + fc) * 2 : OOB, 0, OUTAUX);
      STAMP(12);
    }
  }
  STAMP_FLUSH();
}
}

extern "C" void kernel_launch(void* const* d_in, const int* in_sizes, int n_in, void* d_out, int out_size, void* d_ws, size_t ws_size,
                              hipStream_t stream) {
  Params p;
  p.x = d_in[0]; p.kc = d_in[1]; p.vc = d_in[2]; p.wq = d_in[3]; p.wo = d_in[4];
  p.w1 = d_in[6]; p.b1 = d_in[7]; p.w2 = d_in[8]; p.b2 = d_in[9];
  p.out = d_out; p.ws = d_ws;
  hipLaunchKernelGGL(fused_decode, dim3(NWG), dim3(NTHR), 0, stream, p);
}
```

```cpp
#define SB 0
#include <hip/hip_runtime.h>
#include <hip/hip_bf16.h>
#include <stdint.h>

#define STAMPS 0
#define NPRE 6
#define CODEPF 1
#define TLBPF 0
#define WIN 5
#define WINW 6
#define FLPOS 28
#define RAMP 4
#define WINV 6
#define WINT 6
#define QKPIN 1
#define ABL 0
#define WINK WIN
#define OSLEEP 1
#define PZPOS 0
#define FL1 1
#define CB4 1
#define OUTAUX 16
#define FMODE 0
#define DYN 1
#define W0 4
#define WS1 1
#define WS2 3
#define SB2 10
#define TH1 -30
#define TH2 20
#define S2 235
#define S3 700
#define T3 460
#define T4 430
#define T5 445
#define NSMP 64
#define PRPOS 1
#define PRSL 20
#define LSTEP 4
#define SPREAD 0
#define BLKSZ 262144
#define KVORD 0
#define QST 18
#define QLD 16
#define PZST 16
#define WSOFF 0
#define FLST 16
#define MLST 16
#define PZF 16
#define OST 16
#define FST 16
#define OLD 16
#define F128 0
#define FDELAY 20
#define FSLEEP 1
#define ODELAY 25
namespace {
typedef unsigned v4u __attribute__((ext_vector_type(4)));
typedef unsigned v2u __attribute__((ext_vector_type(2)));
typedef float f32x4 __attribute__((ext_vector_type(4)));
typedef short bf16x8 __attribute__((ext_vector_type(8)));
typedef __bf16 bf16x2 __attribute__((ext_vector_type(2)));
typedef float f32x2 __attribute__((ext_vector_type(2)));
typedef __amdgpu_buffer_rsrc_t rsrc_t;
#define GAS __attribute__((address_space(1)))
#define LAS __attribute__((address_space(3)))

constexpr int NWG = 256, NTHR = 512;
constexpr int DM = 1024, NQ = 4096, FF = 3584;
constexpr int WS_E = 0;
constexpr int WS_FLAG = 4096;
constexpr int WS_LINE = 8192;
constexpr int WS_ML = WS_LINE + 3 * 4096;
constexpr int WS_Q = WS_ML + 2048;
constexpr int WS_H = WS_Q + 32768;
constexpr int WS_O = WS_H + 2 * FF * 4;
constexpr int WS_F = WS_O + 65536;
constexpr int BLK = BLKSZ;
static_assert(WS_F + 65536 <= BLK || !SPREAD, "block overflow");
constexpr int WS_END = 8 * 1048576;
__device__ __forceinline__ int a_line(int k, int cu) { return 8 * WS_LINE + k * 32768 + cu * 128; }
__device__ __forceinline__ int a_ml(int grp, int s) { return 8 * WS_ML + (grp * 16 + s) * 64; }
__device__ __forceinline__ int a_q(int b, int head, int kq) { return 8 * WS_Q + ((b * 32 + head) * 4 + kq) * 512; }
__device__ __forceinline__ int a_h(int kq, int b) { return 8 * WS_H + (((kq >> 1) * 4 + b) * 2 + (kq & 1)) * FF * 4; }
__device__ __forceinline__ int a_o(int head, int b, int s) { return 8 * WS_O + ((head * 4 + b) * 16 + s) * 256; }
__device__ __forceinline__ int a_f(int f, int hd) { return F128 ? 8 * WS_F + ((f >> 1) * 32 + hd) * 128 + (f & 1) * 64 : 8 * WS_F + (f * 32 + hd) * 64; }
constexpr long WS_STAMP = (8l << 20) - (long)WSOFF * 1048576;
constexpr int OOB = 0x7f000000;
constexpr int AUX_NT = 2, AUX_SC1 = 16;
constexpr unsigned POISON = 0u;
constexpr int SPIN_CAP = 1 << 14;
constexpr int L_CTR = 0;
constexpr int L_XN = 64;
constexpr int L_Q = L_XN + 2048;
constexpr int L_M = L_Q + 2048;
constexpr int L_L = L_M + 256;
constexpr int L_RED = L_L + 256;
constexpr int RED_W = 8 * 18 * 16;
constexpr int L_O = L_RED + 4 * RED_W;
constexpr int O_W = 8 * 132 * 4;
constexpr int L_H = L_O + 5 * O_W;
constexpr int L_OA = L_H + 1024;
constexpr int L_END = L_OA + 1024;

struct Params {
  const void *x, *kc, *vc, *wq, *wo, *w1, *b1, *w2, *b2;
  void *out, *ws;
};

__device__ __forceinline__ rsrc_t mk(const void* p, int bytes) { return __builtin_amdgcn_make_buffer_rsrc((void*)p, 0, bytes, 0x00020000); }
template <int AUX> __device__ __forceinline__ v4u ld16(rsrc_t r, int voff, int soff) { return __builtin_amdgcn_raw_buffer_load_b128(r, voff, soff, AUX); }
template <int AUX> __device__ __forceinline__ v2u ld8(rsrc_t r, int voff, int soff) { return __builtin_amdgcn_raw_buffer_load_b64(r, voff, soff, AUX); }
template <int AUX = AUX_SC1> __device__ __forceinline__ void st16(v4u v, rsrc_t r, int voff, int soff) { __builtin_amdgcn_raw_buffer_store_b128(v, r, voff, soff, AUX); }
__device__ __forceinline__ void stz(v4u v, rsrc_t r, int voff, int soff) { __builtin_amdgcn_raw_buffer_store_b128(v, r, voff, soff, PZST); }
template <int AUX = AUX_SC1> __device__ __forceinline__ void st8(v2u v, rsrc_t r, int voff, int soff) { __builtin_amdgcn_raw_buffer_store_b64(v, r, voff, soff, AUX); }
__device__ __forceinline__ f32x4 mfma(v4u a, v4u b, f32x4 c) {
  return __builtin_amdgcn_mfma_f32_16x16x32_bf16(__builtin_bit_cast(bf16x8, a), __builtin_bit_cast(bf16x8, b), c, 0, 0, 0);
}
__device__ __forceinline__ float u2f(unsigned u) { return __builtin_bit_cast(float, u); }
__device__ __forceinline__ unsigned f2u(float f) { return __builtin_bit_cast(unsigned, f); }
__device__ __forceinline__ unsigned pk(float lo, float hi) {
  f32x2 v = {lo, hi};
  return __builtin_bit_cast(unsigned, __builtin_convertvector(v, bf16x2));
}
__device__ __forceinline__ float blo(unsigned u) { return u2f(u << 16); }
__device__ __forceinline__ float bhi(unsigned u) { return u2f(u & 0xffff0000u); }
__device__ __forceinline__ float rbf(float f) { return blo(pk(f, f)); }
template <int N> __device__ __forceinline__ float ror(float x) { return u2f(__builtin_amdgcn_mov_dpp(f2u(x), 0x120 + N, 0xf, 0xf, true)); }
template <int N> __device__ __forceinline__ unsigned roru(unsigned x) { return __builtin_amdgcn_mov_dpp(x, 0x120 + N, 0xf, 0xf, true); }
__device__ __forceinline__ float xrow_sum(float x) {
  auto s = __builtin_amdgcn_permlane16_swap(f2u(x), f2u(x), false, false);
  x = u2f(s[0]) + u2f(s[1]);
  auto t = __builtin_amdgcn_permlane32_swap(f2u(x), f2u(x), false, false);
  return u2f(t[0]) + u2f(t[1]);
}
__device__ __forceinline__ float xrow_max(float x) {
  auto s = __builtin_amdgcn_permlane16_swap(f2u(x), f2u(x), false, false);
  x = fmaxf(u2f(s[0]), u2f(s[1]));
  auto t = __builtin_amdgcn_permlane32_swap(f2u(x), f2u(x), false, false);
  return fmaxf(u2f(t[0]), u2f(t[1]));
}
__device__ __forceinline__ float wave_sum(float x) {
  x += ror<8>(x); x += ror<4>(x); x += ror<2>(x); x += ror<1>(x);
  return xrow_sum(x);
}
__device__ __forceinline__ unsigned umin4(v4u v) { return min(min(v.x, v.y), min(v.z, v.w)); }
__device__ __forceinline__ v4u nopoison(f32x4 v) {
  v4u u = __builtin_bit_cast(v4u, v);
  return v4u{max(u.x, 1u), max(u.y, 1u), max(u.z, 1u), max(u.w, 1u)};
}
#define PIN() __builtin_amdgcn_sched_barrier(0)
#define BAR() do { PIN(); asm volatile("s_waitcnt lgkmcnt(0)\n\ts_barrier" ::: "memory"); PIN(); } while (0)
#define REREAD() asm volatile("" ::: "memory")
__device__ __forceinline__ v4u tr8(const v4u* w, int j0) {
  const unsigned sel = (j0 & 1) ? 0x07060302u : 0x05040100u;
  const int c = j0 >> 1;
  return v4u{__builtin_amdgcn_perm(w[1][c], w[0][c], sel), __builtin_amdgcn_perm(w[3][c], w[2][c], sel),
             __builtin_amdgcn_perm(w[5][c], w[4][c], sel), __builtin_amdgcn_perm(w[7][c], w[6][c], sel)};
}
__device__ __forceinline__ void ln_stats(const float (&v)[16], float& m, float& inv) {
  float s = 0.f;
#pragma unroll
  for (int i = 0; i < 16; ++i) s += v[i];
  const float mf = wave_sum(s) * (1.0f / DM);
  float ss = 0.f;
#pragma unroll
  for (int i = 0; i < 16; ++i) { const float d = v[i] - mf; ss = fmaf(d, d, ss); }
  const float var = rbf(wave_sum(ss) * (1.0f / DM));
  m = rbf(mf);
  inv = __builtin_amdgcn_rcpf(rbf(__builtin_amdgcn_sqrtf(rbf(var + 1e-5f))));
}
__device__ __forceinline__ unsigned ln_pair(float a, float b, float m, float inv) {
  const unsigned d = pk(a - m, b - m);
  return pk(blo(d) * inv, bhi(d) * inv);
}
__device__ __forceinline__ unsigned lds_arrive(LAS unsigned* c, int lane) {
  unsigned old = 0;
  REREAD();
  if (lane == 0) old = __hip_atomic_fetch_add(c, 1u, __ATOMIC_RELAXED, __HIP_MEMORY_SCOPE_WORKGROUP);
  return __builtin_amdgcn_readfirstlane(old);
}
template <int SL = 1> __device__ __forceinline__ void class_bar(LAS unsigned* c, int lane) {
  PIN();
  if (lds_arrive(c, lane) != 3u) {
    _Pragma("nounroll") while (__builtin_amdgcn_readfirstlane(__hip_atomic_load(c, __ATOMIC_RELAXED, __HIP_MEMORY_SCOPE_WORKGROUP)) < 4u)
      __builtin_amdgcn_s_sleep(SL);
  }
  REREAD();
  PIN();
}
template <int X> __device__ __forceinline__ void dyn_pace(int lag) {
  asm volatile("s_cmp_gt_i32 %0, %1\n\ts_cbranch_scc1 1f\n\ts_waitcnt vmcnt(%3)\n1:\n\ts_cmp_gt_i32 %0, %2\n\ts_cbranch_scc1 2f\n\ts_waitcnt vmcnt(%4)\n2:\n\ts_waitcnt vmcnt(%5)"
               :: "s"(lag), "n"(TH1), "n"(TH2), "n"(W0 - 1 + X), "n"(W0 + WS1 - 1 + X), "n"(W0 + WS2 - 1 + X) : "memory", "scc");
}
__device__ __forceinline__ int now32() { return (int)(unsigned)__builtin_amdgcn_s_memrealtime(); }
template <int N> __device__ __forceinline__ void pace() { asm volatile("s_waitcnt vmcnt(%0)" :: "n"(N) : "memory"); }

#define STAMP(i)
#define STAMPV(i, v)
#define STAMP_FLUSH()

__global__ void __launch_bounds__(NTHR, 2) fused_decode(Params p) {
  __shared__ __attribute__((aligned(16))) unsigned char smem[L_END];
  LAS unsigned char* L = (LAS unsigned char*)smem;
  LAS unsigned* ctr = (LAS unsigned*)(L + L_CTR);
  const int tid = threadIdx.x, lane = tid & 63;
  const int w = __builtin_amdgcn_readfirstlane(tid >> 6);
  const int wl = w & 3;
  const bool isA = w < 4;
  const int xs = blockIdx.x & 7, ci = blockIdx.x >> 3;
  const int n16 = lane & 15, g = lane >> 4;
  const int tbase = DYN ? now32() + SB : 0;
  int lag = 0;
  unsigned cpf = 0, tpf = 0;
  {
    unsigned long long pc; int len;
    asm volatile("s_getpc_b64 %0\n.Lpf_base%=:\n\ts_mov_b32 %1, .Lfd_code_end-.Lpf_base%=" : "=s"(pc), "=s"(len));
    if (tid < 5) cpf = __builtin_amdgcn_raw_buffer_load_b32(mk((const void*)pc, len), (tid * 32 + ci) * 128, 0, 0);
  }
  PIN();
  asm volatile("" :: "s"(p.x), "s"(p.kc), "s"(p.vc), "s"(p.wq), "s"(p.wo), "s"(p.w1), "s"(p.b1), "s"(p.w2), "s"(p.b2), "s"(p.out), "s"(p.ws));
  v2u xr[4];
  if (isA) {
    const rsrc_t rx = mk(p.x, 4 * DM * 2);
#pragma unroll
    for (int i = 0; i < 4; ++i) xr[i] = ld8<0>(rx, lane * 8, wl * 2048 + ((i + (xs >> 1)) & 3) * 512);
  }
  PIN();
  const rsrc_t rws = mk(p.ws, WS_END);
  const int gidx = 2 * xs + (ci >> 4), ab = gidx >> 2, kvh = gidx & 3, sp = ci & 15;
  const int kq = xs >> 1, isq = ci < 16, cb = isq ? 16 * (xs & 1) + ci : 14 * (xs & 1) + ci - 16;
  const int hd = 4 * xs + (ci >> 3), cg = ci & 7;
  const int cu = xs * 32 + ci;
  const size_t kvbase = ((size_t)gidx * 16 + sp) * 262144;
  unsigned long long gen = *(const __attribute__((address_space(4))) unsigned long long*)p.ws;
#define MAKE_TAG() const unsigned long long tag64 = (gen + 1ull) * 0x9E3779B97F4A7C15ull; \
  const unsigned tag0 = (unsigned)tag64, tag1 = (unsigned)(tag64 >> 32)

  const int hc = lane & 7, hbt = (lane >> 3) & 3, hb = 112 * hd + 28 * wl + 4 * hc;
  const int fb = FMODE == 2 ? 2 * (lane & 1) + ((lane >> 3) & 1) : (lane >> 2) & 3, fc = 4 * cu + (FMODE == 2 ? (lane >> 1) & 3 : lane & 3);
  v2u sm;
  if (isA) {
    if (tid < 8) ctr[tid] = 0u;
    PIN();
    float v[16];
#pragma unroll
    for (int i = 0; i < 8; ++i) { v[2 * i] = blo(xr[i >> 1][i & 1]); v[2 * i + 1] = bhi(xr[i >> 1][i & 1]); }
    STAMP(1);
    float m, inv;
    ln_stats(v, m, inv);
    if (!isq) {
#pragma unroll
      for (int i = 0; i < 16; i += 2) { const unsigned o = ln_pair(v[i], v[i + 1], m, inv); v[i] = blo(o); v[i + 1] = bhi(o); }
      ln_stats(v, m, inv);
    }
    *(LAS v2u*)(L + L_XN + wl * 512 + lane * 8) = v2u{ln_pair(v[0], v[1], m, inv), ln_pair(v[2], v[3], m, inv)};
    asm volatile("" : "+s"(gen));
    STAMP(2);
    BAR();
    asm volatile("" :: "v"(cpf), "v"(tpf));
  } else {
    const int ldw = isq ? NQ * 2 : FF * 2;
    const rsrc_t rw = mk(isq ? p.wq : p.w1, DM * ldw);
    const int wo_ = ci < 30 ? (256 * kq + 64 * wl + 8 * g) * ldw + cb * 256 + n16 * 16 : OOB;
    v4u wv[16];
#pragma unroll
    for (int j = 0; j < 16; ++j) {
      if (PZPOS && j == PZPOS && !(ABL & 1)) {
        const v4u pz = {POISON, POISON, POISON, POISON};
        stz(pz, rws, wl < 2 ? a_o(8 * kvh + 4 * wl + g, ab, sp) + n16 * 16
                             : wl == 2 && lane < 4 ? a_ml(gidx, sp) + lane * 16 : OOB, 0);
        if (FMODE == 0) st16<PZF>(pz, rws, wl >= 2 ? a_f(32 * cg + 16 * (wl & 1) + (lane >> 2), hd) + (lane & 3) * 16 : OOB, 0);
        PIN();
      }
      if (PZPOS && j >= WINW && j < WINW + PZPOS) pace<WINW + (FMODE == 0)>();
      else if (j >= WINW) pace<WINW - 1>();
      wv[j] = ld16<AUX_NT>(rw, wo_, ((j >> 3) * 32 + (j & 7)) * ldw);
      PIN();
    }
    asm volatile("" : "+s"(gen));
    STAMP(1);
    BAR();
    STAMP(2);
    f32x4 d[8];
#pragma unroll
    for (int t = 0; t < 2; ++t) {
      const v4u act = *(const LAS v4u*)(L + L_XN + (n16 & 3) * 512 + (64 * wl + 32 * t + 8 * g) * 2);
#pragma unroll
      for (int j0 = 0; j0 < 8; ++j0) d[j0] = mfma(act, tr8(wv + 8 * t, j0), t ? d[j0] : f32x4{0.f, 0.f, 0.f, 0.f});
      PIN();
    }
    if (lane < 16) {
#pragma unroll
      for (int j0 = 0; j0 < 8; ++j0) *(LAS f32x4*)(L + L_RED + wl * RED_W + (j0 * 17 + lane) * 16) = d[j0];
    }
    STAMP(3);
    class_bar(ctr + 0, lane);
    if (wl < 2) {
      const int b = g, jh = lane & 1, nn = wl * 8 + (n16 >> 1);
      f32x4 sum = {0.f, 0.f, 0.f, 0.f};
#pragma unroll
      for (int ww = 0; ww < 4; ++ww)
#pragma unroll
        for (int k = 0; k < 4; ++k) sum[k] += *(const LAS float*)(L + L_RED + ww * RED_W + ((4 * jh + k) * 17 + nn) * 16 + b * 4);
      const int co = (8 * nn + 4 * jh) * 4;
      const int off = isq ? a_q(b, cb, kq) + co : (ci < 30 ? a_h(kq, b) + cb * 512 + co : OOB);
      __builtin_amdgcn_raw_buffer_store_b128(__builtin_bit_cast(v4u, sum), rws, (ABL & 4) ? OOB : off, 0, QST);
    }
    if (!(ABL & 1) && !PZPOS) {
        const v4u pz = {POISON, POISON, POISON, POISON};
        stz(pz, rws, wl < 2 ? a_o(8 * kvh + 4 * wl + g, ab, sp) + n16 * 16
                             : wl == 2 && lane < 4 ? a_ml(gidx, sp) + lane * 16 : OOB, 0);
        if (FMODE == 0) st16<PZF>(pz, rws, wl >= 2 ? a_f(32 * cg + 16 * (wl & 1) + (lane >> 2), hd) + (lane & 3) * 16 : OOB, 0);
        PIN();
      }
    STAMP(4);
    sm.x = __builtin_amdgcn_raw_buffer_load_b16(mk(p.x, 4 * DM * 2), wl == 2 && !(ABL & 2) ? (fb * DM + fc) * 2 : OOB, 0, 0);
    sm.y = __builtin_amdgcn_raw_buffer_load_b16(mk(p.b2, DM * 2), wl == 2 && !(ABL & 2) ? fc * 2 : OOB, 0, 0);
    pace<0>();
    if (lds_arrive(ctr + 1, lane) == 3u) {
      MAKE_TAG();
      st8<FLST>(v2u{tag0, tag1}, rws, lane == 0 ? WS_FLAG + cu * 8 : lane == 1 ? a_line(0, cu) : OOB, 0);
    }
    STAMP(5);
    BAR();
  }
  const rsrc_t rk = mk((const char*)p.kc + ((size_t)gidx << 22), 1 << 22), rv = mk((const char*)p.vc + ((size_t)gidx << 22), 1 << 22);
  constexpr int PU = KVORD == 0 ? 16 : KVORD == 1 ? 2 : KVORD == 2 ? 1 : 4;
  const int cbase = sp * PU * 16384 + (w >> 2) * (KVORD == 0 ? 131072 : 2097152) + wl * 4096;
#define KVOFF(i) ((((i) >> 2) / PU) * (PU * 262144) + (((i) >> 2) % PU) * 16384 + ((i) & 3) * 1024)
  const int koff = cbase + ((lane >> 2) & 3) * 256 + g * 64 + (lane & 3) * 16;
  const int voff = cbase + lane * 16;
  v4u r[32];
  v2u fq;
  const int fhd = 8 * kvh + 2 * wl + ((lane >> 2) & 1);
  const int fqo = a_line(0, (2 * (lane & 3) + (fhd >> 4)) * 32 + (fhd & 15));
  __builtin_amdgcn_s_setprio(1);
#pragma unroll
  for (int i = 0; i < 32; ++i) {
    if (i == FLPOS) { fq = ld8<AUX_SC1>(rws, isA && lane < 8 ? fqo : OOB, 0); PIN(); }
    if (DYN && i >= W0) { if (i >= FLPOS) dyn_pace<1>(lag); else dyn_pace<0>(lag); }
    else if (i >= FLPOS && i < FLPOS + WINK) pace<WINK>();
    else if (i >= WINK) pace<WINK - 1>();
    else if (i > 0 && RAMP) __builtin_amdgcn_s_sleep(RAMP);
    r[i] = ld16<AUX_NT>(rk, koff, KVOFF(i));
    PIN();
    if (DYN && (i % LSTEP) == 0) { lag = now32() - tbase - (isA ? S2 + (S3 - S2) * i / 32 : S3 + T3 * i / 32); asm volatile("" : "+s"(lag)); PIN(); }
  }
  __builtin_amdgcn_s_setprio(0);
  STAMP(6);
  BAR();
  MAKE_TAG();
  if (DYN == 2 && wl == 0) __builtin_amdgcn_raw_buffer_store_b32((unsigned)now32() ^ tag0, rws, lane == 0 ? a_line(1 + (w >> 2), cu) : OOB, 0, AUX_SC1);
  if (isA) {
    int spins = 0;
    if (__any(lane < 8 && ((fq.x ^ tag0) | (fq.y ^ tag1)) != 0u)) {
      _Pragma("nounroll") while (true) {
        REREAD();
        const v2u a = ld8<AUX_SC1>(rws, lane < 8 ? fqo : OOB, 0);
        if (++spins > SPIN_CAP || !__any(lane < 8 && ((a.x ^ tag0) | (a.y ^ tag1)) != 0u)) break;
        __builtin_amdgcn_s_sleep(2);
      }
    }
    REREAD();
    STAMPV(18, spins);
    STAMP(3);
    const int qo = a_q(ab, 8 * kvh + 2 * wl + (lane >> 5), 0) + (lane & 31) * 16;
    v4u qp[4];
#pragma unroll
    for (int s = 0; s < 4; ++s) qp[s] = ld16<QLD>(rws, (ABL & 8) ? OOB : qo, s * 512);
    PIN();
    const f32x4 qs = (__builtin_bit_cast(f32x4, qp[0]) + __builtin_bit_cast(f32x4, qp[1])) + (__builtin_bit_cast(f32x4, qp[2]) + __builtin_bit_cast(f32x4, qp[3]));
    *(LAS v2u*)(L + L_Q + (2 * wl + (lane >> 5)) * 256 + (lane & 31) * 8) = v2u{pk(qs[0], qs[1]), pk(qs[2], qs[3])};
    class_bar(ctr + 2, lane);
    STAMP(4);
  }
  const int hl = n16 & 7, wb = n16 >> 3;
  const v4u qb0 = *(const LAS v4u*)(L + L_Q + hl * 256 + (4 * g + wb) * 16);
  const v4u qb1 = *(const LAS v4u*)(L + L_Q + hl * 256 + (4 * g + 2 + wb) * 16);
#define PRLOAD() pr = __builtin_amdgcn_raw_buffer_load_b32(rws, wl == 1 && lane < NSMP ? a_line(1 + (w >> 2), lane * (256 / NSMP) + (ci & (256 / NSMP - 1))) : OOB, 0, AUX_SC1)
  float s[16];
#pragma unroll
  for (int u = 0; u < 8; ++u) {
    float t0[4], t1[4];
#pragma unroll
    for (int k = 0; k < 4; ++k) {
      const f32x4 d0 = mfma(r[4 * u + k], qb0, f32x4{0.f, 0.f, 0.f, 0.f});
      const f32x4 d1 = mfma(r[4 * u + k], qb1, f32x4{0.f, 0.f, 0.f, 0.f});
      t0[k] = d0[0] + d1[2];
      t1[k] = d0[1] + d1[3];
    }
#pragma unroll
    for (int k = 0; k < 4; k += 2) {
      const float xx = wb ? t1[k + 1] : t0[k], yy = wb ? t1[k] : t0[k + 1];
      s[2 * u + (k >> 1)] = xx + ror<8>(yy);
    }
    if (QKPIN) PIN();
  }
  unsigned pr = 0u;
  if (DYN == 2 && PRPOS == 0) { PIN(); PRLOAD(); PIN(); }
  float m = -INFINITY;
#pragma unroll
  for (int i = 0; i < 16; i += 2) {
    const unsigned a = pk(s[i], s[i + 1]);
    const unsigned c = pk(blo(a) * 0.08838834764831845f, bhi(a) * 0.08838834764831845f);
    s[i] = blo(c); s[i + 1] = bhi(c);
    m = fmaxf(m, fmaxf(s[i], s[i + 1]));
  }
  m = fmaxf(m, ror<8>(m));
  m = xrow_max(m);
  if (lane < 8) *(LAS float*)(L + L_M + (w * 8 + lane) * 4) = m;
  constexpr float LOG2E = 1.4426950408889634f;
  if (!isA) {
    if (wl == 0) {
      int spins = 0;
      _Pragma("nounroll") while (true) {
        REREAD();
        const v4u a = ld16<AUX_SC1>(rws, WS_FLAG + lane * 16, 0), b = ld16<AUX_SC1>(rws, WS_FLAG + lane * 16, 1024);
        const unsigned bad = (a.x ^ tag0) | (a.y ^ tag1) | (a.z ^ tag0) | (a.w ^ tag1) | (b.x ^ tag0) | (b.y ^ tag1) | (b.z ^ tag0) | (b.w ^ tag1);
        if (!__any(bad != 0u) || ++spins > SPIN_CAP) break;
        __builtin_amdgcn_s_sleep(4);
      }
      REREAD();
      if (blockIdx.x == 0) st8(v2u{(unsigned)(gen + 1ull), (unsigned)((gen + 1ull) >> 32)}, rws, lane == 0 ? WS_E : OOB, 0);
    }
    class_bar(ctr + 3, lane);
#pragma unroll
    for (int ww = 0; ww < 8; ++ww) m = fmaxf(m, *(const LAS float*)(L + L_M + (ww * 8 + hl) * 4));
  }
  unsigned pb[16];
  float lsum = 0.f;
  {
    const unsigned msk = wb ? 0xffff0000u : 0x0000ffffu;
#pragma unroll
    for (int i = 0; i < 16; ++i) {
      const float e = __builtin_amdgcn_exp2f((s[i] - m) * LOG2E);
      lsum += e;
      const unsigned t = pk(e, e) & msk;
      pb[i] = t | roru<8>(t);
      asm volatile("" : "+v"(pb[i]));
    }
    lsum += ror<8>(lsum);
    lsum = xrow_sum(lsum);
    if (lane < 8) *(LAS float*)(L + L_L + (w * 8 + lane) * 4) = lsum;
  }
  f32x4 acc[8];
#pragma unroll
  for (int j = 0; j < 8; ++j) acc[j] = f32x4{0.f, 0.f, 0.f, 0.f};
  int tref = tbase + S3 + T3 + (isA ? 0 : T4);
  if (DYN == 2 && wl == 1) {
    if (PRPOS == 1) { if (!isA) __builtin_amdgcn_s_sleep(PRSL); PRLOAD(); }
    const unsigned tn = (unsigned)now32();
    const unsigned age = tn - (pr ^ tag0);
    const bool ok = age < 800u && lane < NSMP;
    const float mean = wave_sum(ok ? (float)age : 0.f) * __builtin_amdgcn_rcpf(fmaxf(wave_sum(ok ? 1.f : 0.f), 1.f));
    if (lane == 0) ctr[8 + (w >> 2)] = tn - (int)mean + (isA ? T3 : T4) + SB2;
  }
  STAMP(7);
  BAR();
  STAMP(15);
constexpr int PVLAG = DYN ? W0 + WS2 : WINV;
#define PV(n) acc[(n) & 7] = mfma(tr8(&r[(n) & ~7], (n) & 7), v4u{pb[((n) >> 3) * 4], pb[((n) >> 3) * 4 + 1], pb[((n) >> 3) * 4 + 2], pb[((n) >> 3) * 4 + 3]}, acc[(n) & 7])
  __builtin_amdgcn_s_setprio(1);
#pragma unroll
  for (int i = 0; i < 32; ++i) {
    if (DYN && i >= W0) dyn_pace<0>(lag);
    else if (i >= WINV) pace<WINV - 1>();
    else if (i > 0 && RAMP) __builtin_amdgcn_s_sleep(RAMP);
    r[i] = ld16<AUX_NT>(rv, voff, KVOFF(i));
    PIN();
    if (DYN && (i % LSTEP) == 0) { if (i == 0 && DYN == 2) tref = __builtin_amdgcn_readfirstlane(ctr[8 + (w >> 2)]); lag = now32() - tref - (isA ? T4 * i / 32 : T5 * i / 32); asm volatile("" : "+s"(lag)); PIN(); }
    if (i >= 7 + PVLAG) { PV(i - 7 - PVLAG); PIN(); }
  }
  const int mo = a_ml(wl * 4 + (hd >> 3), (n16 + 4 * g) & 15) + (hd & 7) * 8;
  v2u ml = ld8<AUX_SC1>(rws, isA ? OOB : mo, 0);
  STAMP(16);
  BAR();
#pragma unroll
  for (int n = 32 - 7 - PVLAG; n < 32; ++n) { PV(n); if ((n & 7) == 7) PIN(); }
  STAMP(8);
#define PUT_ACC(f) _Pragma("unroll") for (int i = 0; i < 4; ++i) { \
      const f32x4 t = {wb ? acc[4][i] : acc[0][i], wb ? acc[5][i] : acc[1][i], wb ? acc[6][i] : acc[2][i], wb ? acc[7][i] : acc[3][i]}; \
      *(LAS f32x4*)(L + L_O + wl * O_W + (hl * 132 + 8 * (4 * g + i) + 4 * wb) * 4) = f t; }
  const int oro = ((2 * wl + (g & 1)) * 132 + 8 * n16 + 4 * (g >> 1)) * 4;
  if (isA) {
    __builtin_amdgcn_s_setprio(0);
    float M = m;
#pragma unroll
    for (int ww = 0; ww < 8; ++ww) M = fmaxf(M, *(const LAS float*)(L + L_M + (ww * 8 + hl) * 4));
    const float fa = __builtin_amdgcn_exp2f((m - M) * LOG2E);
    PUT_ACC(fa *);
    if (lane < 8) *(LAS float*)(L + L_L + (w * 8 + lane) * 4) = fa * lsum;
    class_bar(ctr + 4, lane);
    {
      f32x4 t = *(const LAS f32x4*)(L + L_O + oro);
#pragma unroll
      for (int ww = 1; ww < 4; ++ww) t += *(const LAS f32x4*)(L + L_O + ww * O_W + oro);
      *(LAS f32x4*)(L + L_O + 4 * O_W + oro) = t;
    }
    if (w == 0) {
      float l = 0.f;
#pragma unroll
      for (int ww = 0; ww < 8; ++ww) l += *(const LAS float*)(L + L_L + (ww * 8 + hl) * 4);
      st8<MLST>(v2u{max(f2u(M), 1u), max(f2u(l), 1u)}, rws, lane < 8 ? a_ml(gidx, sp) + lane * 8 : OOB, 0);
    }
    {
    const int ho = hc < 7 ? a_h(2 * (lane >> 5), hbt) + hb * 4 : OOB;
    v4u hp[2];
#pragma unroll
    for (int s = 0; s < 2; ++s) hp[s] = ld16<QLD>(rws, (ABL & 8) ? OOB : ho, s * FF * 4);
    sm = ld8<0>(mk(p.b1, FF * 2), hc < 7 && !(ABL & 2) ? hb * 2 : OOB, 0);
    PIN();
    float hv[4];
#pragma unroll
    for (int j = 0; j < 4; ++j) {
      const float t0 = u2f(hp[0][j]) + u2f(hp[1][j]);
      auto sw = __builtin_amdgcn_permlane32_swap(f2u(t0), f2u(t0), false, false);
      const float t = u2f(sw[0]) + u2f(sw[1]);
      const float u = rbf(rbf(t) + ((j & 1) ? bhi(sm[j >> 1]) : blo(sm[j >> 1])));
      const float z2 = u * fmaf(u * u, 0.044715f, 1.0f) * (-2.0f * 0.7978845608028654f * 1.4426950408889634f);
      hv[j] = u * __builtin_amdgcn_rcpf(1.0f + __builtin_amdgcn_exp2f(z2));
    }
    if (lane < 32) *(LAS v2u*)(L + L_H + wl * 256 + hbt * 64 + hc * 8) = v2u{pk(hv[0], hv[1]), pk(hv[2], hv[3])};
    }
    const rsrc_t r2 = mk(p.w2, FF * DM * 2), ro = mk(p.wo, NQ * DM * 2);
    const int o2 = (112 * hd + 28 * wl + 8 * g) * 2048 + cg * 256 + n16 * 16;
    const int o2b = g < 3 ? o2 : OOB;
    const int oo = (128 * hd + 32 * wl + 8 * g) * 2048 + cg * 256 + n16 * 16;
    const v4u hact = *(const LAS v4u*)(L + L_H + wl * 256 + (n16 & 3) * 64 + g * 16);
    v4u tw[16];
#define TLD(j) do { if ((j) >= WINT) pace<WINT - 1>(); else if ((j) > 0 && (j) < NPRE && RAMP) __builtin_amdgcn_s_sleep(RAMP); tw[j] = (j) < 8 ? ld16<AUX_NT>(r2, (j) < 4 ? o2 : o2b, (j) * 2048) : ld16<AUX_NT>(ro, oo, ((j) - 8) * 2048); PIN(); } while (0)
    STAMP(5);
    BAR();
#pragma unroll
    for (int j = 0; j < NPRE; ++j) TLD(j);
    STAMP(9);
    if (!CB4) BAR();
#pragma unroll
    for (int j = NPRE; j < 16; ++j) TLD(j);
    f32x4 d[8];
#pragma unroll
    for (int j0 = 0; j0 < 8; ++j0) d[j0] = mfma(hact, tr8(tw, j0), f32x4{0.f, 0.f, 0.f, 0.f});
    v4u wt[8];
#pragma unroll
    for (int j0 = 0; j0 < 8; ++j0) { wt[j0] = tr8(tw + 8, j0); asm volatile("" : "+v"(wt[j0])); }
    STAMP(10);
    BAR();
    STAMP(11);
    const v4u oact = *(const LAS v4u*)(L + L_OA + (n16 & 3) * 256 + (32 * wl + 8 * g) * 2);
#pragma unroll
    for (int j0 = 0; j0 < 8; ++j0) d[j0] = mfma(oact, wt[j0], d[j0]);
    if (lane < 16) {
#pragma unroll
      for (int j0 = 0; j0 < 8; ++j0) *(LAS f32x4*)(L + L_RED + wl * RED_W + (j0 * 18 + lane) * 16) = d[j0];
    }
    BAR();
    STAMP(12);
    asm volatile(".Lfd_code_end:");
  } else {
    PUT_ACC();
    if (CB4) class_bar<0>(ctr + 6, lane); else BAR();
    STAMP(9);
    {
      f32x4 t = *(const LAS f32x4*)(L + L_O + 4 * O_W + oro);
#pragma unroll
      for (int ww = 0; ww < 4; ++ww) t += *(const LAS f32x4*)(L + L_O + ww * O_W + oro);
      const unsigned a0 = max(pk(t[0], t[1]), 1u), a1 = max(pk(t[2], t[3]), 1u);
      auto s0 = __builtin_amdgcn_permlane32_swap(a0, a0, false, false);
      auto s1 = __builtin_amdgcn_permlane32_swap(a1, a1, false, false);
      st16<OST>(v4u{s0[0], s1[0], s0[1], s1[1]}, rws, lane < 32 ? a_o(8 * kvh + 2 * wl + g, ab, sp) + n16 * 16 : OOB, 0);
    }
    PIN();
    STAMP(10);
    __builtin_amdgcn_s_setprio(0);
    {
      int spins = 0;
      if (__any(min(ml.x, ml.y) == POISON)) {
        _Pragma("nounroll") while (true) {
          REREAD();
          ml = ld8<AUX_SC1>(rws, mo, 0);
          if (!__any(min(ml.x, ml.y) == POISON) || ++spins > SPIN_CAP) break;
          __builtin_amdgcn_s_sleep(2);
        }
      }
      STAMPV(19, spins);
      float M = u2f(ml.x);
      M = fmaxf(M, ror<8>(M)); M = fmaxf(M, ror<4>(M)); M = fmaxf(M, ror<2>(M)); M = fmaxf(M, ror<1>(M));
      const float e = __builtin_amdgcn_exp2f((u2f(ml.x) - M) * LOG2E);
      float Ls = e * u2f(ml.y);
      Ls += ror<8>(Ls); Ls += ror<4>(Ls); Ls += ror<2>(Ls); Ls += ror<1>(Ls);
      float f = e * __builtin_amdgcn_rcpf(Ls);
      asm volatile("" : "+v"(f));
      if (ODELAY) __builtin_amdgcn_s_sleep(ODELAY);
      const int oo_ = a_o(hd, wl, 4 * g) + n16 * 16;
      v4u op[4];
      int of[4];
#pragma unroll
      for (int k = 0; k < 4; ++k) { op[k] = v4u{0u, 0u, 0u, 0u}; of[k] = oo_; }
      spins = 0;
      _Pragma("nounroll") while (true) {
        REREAD();
        v4u nw[4];
#pragma unroll
        for (int k = 0; k < 4; ++k) nw[k] = ld16<OLD>(rws, of[k], k * 256);
        PIN();
        bool bad = false;
#pragma unroll
        for (int k = 0; k < 4; ++k) {
          op[k] |= nw[k];
          const bool b = umin4(op[k]) == POISON;
          of[k] = b ? oo_ : OOB;
          bad |= b;
        }
        if (!__any(bad) || ++spins > SPIN_CAP) break;
        __builtin_amdgcn_s_sleep(OSLEEP);
      }
      STAMPV(18, spins);
      STAMP(13);
      float t[8] = {0.f, 0.f, 0.f, 0.f, 0.f, 0.f, 0.f, 0.f};
#define OACC(k) do { const float fk = u2f(__builtin_amdgcn_mov_dpp(f2u(f), 0x150 + (k), 0xf, 0xf, true)); \
        _Pragma("unroll") for (int j = 0; j < 4; ++j) { t[2 * j] = fmaf(fk, blo(op[k][j]), t[2 * j]); t[2 * j + 1] = fmaf(fk, bhi(op[k][j]), t[2 * j + 1]); } } while (0)
      OACC(0); OACC(1); OACC(2); OACC(3);
      float u[4];
#pragma unroll
      for (int j = 0; j < 4; ++j) { auto sw = __builtin_amdgcn_permlane32_swap(f2u(t[j]), f2u(t[4 + j]), false, false); u[j] = u2f(sw[0]) + u2f(sw[1]); }
      auto s0 = __builtin_amdgcn_permlane16_swap(f2u(u[0]), f2u(u[2]), false, false);
      auto s1 = __builtin_amdgcn_permlane16_swap(f2u(u[1]), f2u(u[3]), false, false);
      *(LAS unsigned*)(L + L_OA + wl * 256 + n16 * 16 + g * 4) = pk(u2f(s0[0]) + u2f(s0[1]), u2f(s1[0]) + u2f(s1[1]));
      if (wl != 2) __builtin_amdgcn_s_setprio(2);
    }
    BAR();
    BAR();
    if (FMODE == 2) {
      const int bp = lane & 1, j0 = (lane >> 1) & 7, nn = wl * 4 + g;
      f32x2 t = *(const LAS f32x2*)(L + L_RED + (j0 * 18 + nn) * 16 + bp * 8);
#pragma unroll
      for (int ww = 1; ww < 4; ++ww) t += *(const LAS f32x2*)(L + L_RED + ww * RED_W + (j0 * 18 + nn) * 16 + bp * 8);
      st16(v4u{f2u(t[0]), tag0, f2u(t[1]), tag1}, rws, WS_F + ((32 * cg + 2 * nn + (j0 >> 2)) * 32 + hd) * 128 + (lane & 7) * 16, 0);
    }
    if (FMODE != 2 && wl < 2) {
      const int j0 = lane & 7, nn = wl * 8 + (lane >> 3);
      f32x4 t = *(const LAS f32x4*)(L + L_RED + (j0 * 18 + nn) * 16);
#pragma unroll
      for (int ww = 1; ww < 4; ++ww) t += *(const LAS f32x4*)(L + L_RED + ww * RED_W + (j0 * 18 + nn) * 16);
      st16<FST>(FMODE == 1 ? v4u{pk(t[0], t[1]), tag0, pk(t[2], t[3]), tag1} : nopoison(t), rws, a_f(32 * cg + 2 * nn + (j0 >> 2), hd) + (j0 & 3) * 16, 0);
      STAMP(11);
    } else if (wl == 2) {
      v4u f0, f1, f2, f3;
      int spins = 0;
      if (FDELAY) __builtin_amdgcn_s_sleep(FDELAY);
      _Pragma("nounroll") while (true) {
        REREAD();
        f0 = ld16<AUX_SC1>(rws, a_f(cu, lane >> 2) + (lane & 3) * 16, 0);
        f1 = ld16<AUX_SC1>(rws, a_f(cu, lane >> 2) + (lane & 3) * 16, F128 ? 2048 : 1024);
        if (FMODE == 2) {
          f2 = ld16<AUX_SC1>(rws, WS_F + cu * 4096 + lane * 16, 2048);
          f3 = ld16<AUX_SC1>(rws, WS_F + cu * 4096 + lane * 16, 3072);
        }
        bool bad;
        if (FMODE == 0) bad = min(umin4(f0), umin4(f1)) == POISON;
        else bad = ((f0.y ^ tag0) | (f0.w ^ tag1) | (f1.y ^ tag0) | (f1.w ^ tag1)) != 0u;
        if (FMODE == 2) bad |= ((f2.y ^ tag0) | (f2.w ^ tag1) | (f3.y ^ tag0) | (f3.w ^ tag1)) != 0u;
        if (!__any(bad) || ++spins > SPIN_CAP) break;
        __builtin_amdgcn_s_sleep(FSLEEP);
      }
      STAMPV(17, spins);
      STAMP(11);
      float y;
      if (FMODE == 2) {
        const float y0 = (u2f(f0.x) + u2f(f1.x)) + (u2f(f2.x) + u2f(f3.x)), y1 = (u2f(f0.z) + u2f(f1.z)) + (u2f(f2.z) + u2f(f3.z));
        y = xrow_sum((wb ? y1 : y0) + ror<8>(wb ? y0 : y1));
      } else {
        f32x4 t = FMODE == 1 ? f32x4{blo(f0.x) + blo(f1.x), bhi(f0.x) + bhi(f1.x), blo(f0.z) + blo(f1.z), bhi(f0.z) + bhi(f1.z)}
                             : __builtin_bit_cast(f32x4, f0) + __builtin_bit_cast(f32x4, f1);
        const bool h8 = lane & 8, h4 = lane & 4;
        const float a0 = (h8 ? t[2] : t[0]) + ror<8>(h8 ? t[0] : t[2]), a1 = (h8 ? t[3] : t[1]) + ror<8>(h8 ? t[1] : t[3]);
        const float k = h4 ? a1 : a0, o = h4 ? a0 : a1;
        const float o4 = u2f(__builtin_amdgcn_mov_dpp(f2u(o), 0x141, 0xf, 0xf, true));
        y = xrow_sum(k + u2f(__builtin_amdgcn_mov_dpp(f2u(o4), 0x1B, 0xf, 0xf, true)));
      }
      y += blo(sm.x) + blo(sm.y);
      __builtin_amdgcn_raw_buffer_store_b16((unsigned short)pk(y, y), mk(p.out, 4 * DM * 2), lane < 16 ? (fb * DM + fc) * 2 : OOB, 0, OUTAUX);
      STAMP(12);
    }
  }
  STAMP_FLUSH();
}
}

extern "C" void kernel_launch(void* const* d_in, const int* in_sizes, int n_in, void* d_out, int out_size, void* d_ws, size_t ws_size,
                              hipStream_t stream) {
  Params p;
  p.x = d_in[0]; p.kc = d_in[1]; p.vc = d_in[2]; p.wq = d_in[3]; p.wo = d_in[4];
  p.w1 = d_in[6]; p.b1 = d_in[7]; p.w2 = d_in[8]; p.b2 = d_in[9];
  p.out = d_out; p.ws = (char*)d_ws + (size_t)WSOFF * 1048576;
  hipLaunchKernelGGL(fused_decode, dim3(NWG), dim3(NTHR), 0, stream, p);
}
```

```cpp
#include <hip/hip_runtime.h>
#include <hip/hip_bf16.h>
#include <stdint.h>

#define STAMPS 0
#define KNOB(name, val) constexpr int name = val
#define K_WINW 6
#define K_WINT 6
#define K_W0 4
#define K_W1 5
#define K_W2 7
#define K_TH1 -30
#define K_TH2 20
#define K_SB -10
#define K_S2 235
#define K_T2 465
#define K_T3 460
#define K_T4 430
#define K_T5 445
#define K_T6 215
#define K_TO 220
#define K_ODMIN 8
#define K_FDELAY 22
#define K_RAMP 3
#define K_FLPOS 28
#define K_TORD 0
#define K_TDYN 0
#define K_DL 50
#define K_MLPOS 0
#define K_MLST 16
#define K_TRAMP 2
#define K_OPRE 0
#define K_OAUX 0
#define K_FT 0
#define K_INL 1
#define K_TPRE 12
#define K_TLB 0
#define K_TABS -1000
#define K_KPRE 0
#define K_VPRE 8
#define K_TOPEN 16
#define K_TGROW 99
#define K_SWAP 0
#define K_ABS 100

namespace {
typedef unsigned v4u __attribute__((ext_vector_type(4)));
typedef unsigned v2u __attribute__((ext_vector_type(2)));
typedef float f32x4 __attribute__((ext_vector_type(4)));
typedef short bf16x8 __attribute__((ext_vector_type(8)));
typedef __bf16 bf16x2 __attribute__((ext_vector_type(2)));
typedef float f32x2 __attribute__((ext_vector_type(2)));
typedef __amdgpu_buffer_rsrc_t rsrc_t;
#define GAS __attribute__((address_space(1)))
#define LAS __attribute__((address_space(3)))

constexpr int NWG = 256, NTHR = 512;
constexpr int DM = 1024, NQ = 4096, FF = 3584;
KNOB(WINW, K_WINW); KNOB(WINT, K_WINT); KNOB(W0, K_W0); KNOB(W1, K_W1); KNOB(W2, K_W2); KNOB(TH1, K_TH1); KNOB(TH2, K_TH2);
KNOB(S2, K_S2 + K_SB); KNOB(T2, K_T2); KNOB(T3, K_T3); KNOB(T4, K_T4); KNOB(T5, K_T5); KNOB(T6, K_T6); KNOB(TO, K_TO);
KNOB(ODMIN, K_ODMIN); KNOB(FDELAY, K_FDELAY); KNOB(RAMP, K_RAMP); KNOB(FLPOS, K_FLPOS); KNOB(TORD, K_TORD); KNOB(TDYN, K_TDYN); KNOB(ABST, K_ABS);
constexpr int WS_E = 0;
constexpr int WS_FLAG = 4096;
constexpr int WS_LINE = 8192;
constexpr int WS_ML = WS_LINE + 32768;
constexpr int WS_Q = WS_ML + 16384;
constexpr int WS_H = WS_Q + 262144;
constexpr int WS_O = WS_H + 16 * FF * 4;
constexpr int WS_F = WS_O + 524288;
constexpr int WS_END = WS_F + 524288;
constexpr int WS_STAMP = 8 << 20;
__device__ __forceinline__ int a_ml(int grp, int s) { return WS_ML + (grp * 16 + s) * 64; }
__device__ __forceinline__ int a_q(int b, int head, int kq) { return WS_Q + ((b * 32 + head) * 4 + kq) * 512; }
__device__ __forceinline__ int a_h(int kq, int b) { return WS_H + (((kq >> 1) * 4 + b) * 2 + (kq & 1)) * FF * 4; }
__device__ __forceinline__ int a_o(int head, int b, int s) { return WS_O + ((head * 4 + b) * 16 + s) * 256; }
__device__ __forceinline__ int a_f(int f, int hd) { return WS_F + (f * 32 + hd) * 64; }
constexpr int OOB = 0x7f000000;
constexpr int NT = 2, SC1 = 16;
constexpr int SPIN_CAP = 1 << 18;
constexpr int L_CTR = 0;
constexpr int L_ZERO = 32;
constexpr int L_XN = 64;
constexpr int L_Q = L_XN + 2048;
constexpr int L_M = L_Q + 2048;
constexpr int L_L = L_M + 256;
constexpr int L_RED = L_L + 256;
constexpr int RED_G = 320, RED_E = 4 * RED_G + 32, RED_W = 2 * RED_E;
constexpr int L_O = L_RED + 4 * RED_W;
constexpr int O_W = 8 * 132 * 4;
constexpr int L_H = L_O + 5 * O_W;
constexpr int L_OA = L_H + 1024;
constexpr int L_END = L_OA + 1024;

struct Params {
  const void *wq, *w1, *x, *kc, *vc, *wo, *w2, *b1, *b2;
  void *out, *ws;
};

__device__ __forceinline__ rsrc_t mk(const void* p, int bytes) { return __builtin_amdgcn_make_buffer_rsrc((void*)p, 0, bytes, 0x00020000); }
template <int AUX> __device__ __forceinline__ v4u ld16(rsrc_t r, int voff, int soff) { return __builtin_amdgcn_raw_buffer_load_b128(r, voff, soff, AUX); }
template <int AUX> __device__ __forceinline__ v2u ld8(rsrc_t r, int voff, int soff) { return __builtin_amdgcn_raw_buffer_load_b64(r, voff, soff, AUX); }
template <int AUX> __device__ __forceinline__ void st16(v4u v, rsrc_t r, int voff) { __builtin_amdgcn_raw_buffer_store_b128(v, r, voff, 0, AUX); }
template <int AUX> __device__ __forceinline__ void st8(v2u v, rsrc_t r, int voff) { __builtin_amdgcn_raw_buffer_store_b64(v, r, voff, 0, AUX); }
__device__ __forceinline__ f32x4 mfma(v4u a, v4u b, f32x4 c) {
  return __builtin_amdgcn_mfma_f32_16x16x32_bf16(__builtin_bit_cast(bf16x8, a), __builtin_bit_cast(bf16x8, b), c, 0, 0, 0);
}
__device__ __forceinline__ float u2f(unsigned u) { return __builtin_bit_cast(float, u); }
__device__ __forceinline__ unsigned f2u(float f) { return __builtin_bit_cast(unsigned, f); }
__device__ __forceinline__ unsigned pk(float lo, float hi) {
  f32x2 v = {lo, hi};
  return __builtin_bit_cast(unsigned, __builtin_convertvector(v, bf16x2));
}
__device__ __forceinline__ float blo(unsigned u) { return u2f(u << 16); }
__device__ __forceinline__ float bhi(unsigned u) { return u2f(u & 0xffff0000u); }
__device__ __forceinline__ float rbf(float f) { return blo(pk(f, f)); }
template <int C> __device__ __forceinline__ float dpp(float x) { return u2f(__builtin_amdgcn_mov_dpp(f2u(x), C, 0xf, 0xf, true)); }
template <int N> __device__ __forceinline__ float ror(float x) { return dpp<0x120 + N>(x); }
__device__ __forceinline__ float xrow_sum(float x) {
  auto s = __builtin_amdgcn_permlane16_swap(f2u(x), f2u(x), false, false);
  x = u2f(s[0]) + u2f(s[1]);
  auto t = __builtin_amdgcn_permlane32_swap(f2u(x), f2u(x), false, false);
  return u2f(t[0]) + u2f(t[1]);
}
__device__ __forceinline__ float xrow_max(float x) {
  auto s = __builtin_amdgcn_permlane16_swap(f2u(x), f2u(x), false, false);
  x = fmaxf(u2f(s[0]), u2f(s[1]));
  auto t = __builtin_amdgcn_permlane32_swap(f2u(x), f2u(x), false, false);
  return fmaxf(u2f(t[0]), u2f(t[1]));
}
__device__ __forceinline__ float wave_sum(float x) {
  x += ror<8>(x); x += ror<4>(x); x += ror<2>(x); x += ror<1>(x);
  return xrow_sum(x);
}
__device__ __forceinline__ unsigned umin4(v4u v) { return min(min(v.x, v.y), min(v.z, v.w)); }
__device__ __forceinline__ unsigned nz(unsigned u) { return max(u, 1u); }
#define PIN() __builtin_amdgcn_sched_barrier(0)
#define BAR() do { PIN(); asm volatile("s_waitcnt lgkmcnt(0)\n\ts_barrier" ::: "memory"); PIN(); } while (0)
#define REREAD() asm volatile("" ::: "memory")
__device__ __forceinline__ v4u tr8(const v4u* w, int j0) {
  const unsigned sel = (j0 & 1) ? 0x07060302u : 0x05040100u;
  const int c = j0 >> 1;
  return v4u{__builtin_amdgcn_perm(w[1][c], w[0][c], sel), __builtin_amdgcn_perm(w[3][c], w[2][c], sel),
             __builtin_amdgcn_perm(w[5][c], w[4][c], sel), __builtin_amdgcn_perm(w[7][c], w[6][c], sel)};
}
__device__ __forceinline__ void ln_stats(const float (&v)[16], float& m, float& inv) {
  float s = 0.f;
#pragma unroll
  for (int i = 0; i < 16; ++i) s += v[i];
  const float mf = wave_sum(s) * (1.0f / DM);
  float ss = 0.f;
#pragma unroll
  for (int i = 0; i < 16; ++i) { const float d = v[i] - mf; ss = fmaf(d, d, ss); }
  const float var = rbf(wave_sum(ss) * (1.0f / DM));
  m = rbf(mf);
  inv = __builtin_amdgcn_rcpf(rbf(__builtin_amdgcn_sqrtf(rbf(var + 1e-5f))));
}
__device__ __forceinline__ unsigned ln_pair(float a, float b, float m, float inv) {
  const unsigned d = pk(a - m, b - m);
  return pk(blo(d) * inv, bhi(d) * inv);
}
__device__ __forceinline__ unsigned lds_arrive(LAS unsigned* c, int lane) {
  unsigned old = 0;
  REREAD();
  if (lane == 0) old = __hip_atomic_fetch_add(c, 1u, __ATOMIC_RELAXED, __HIP_MEMORY_SCOPE_WORKGROUP);
  return __builtin_amdgcn_readfirstlane(old);
}
template <int SL> __device__ __forceinline__ void class_bar(LAS unsigned* c, int lane) {
  PIN();
  if (lds_arrive(c, lane) != 3u) {
    _Pragma("nounroll") while (__builtin_amdgcn_readfirstlane(__hip_atomic_load(c, __ATOMIC_RELAXED, __HIP_MEMORY_SCOPE_WORKGROUP)) < 4u)
      __builtin_amdgcn_s_sleep(SL);
  }
  REREAD();
  PIN();
}
template <int N> __device__ __forceinline__ void pace() { asm volatile("s_waitcnt vmcnt(%0)" :: "n"(N) : "memory"); }
template <int X> __device__ __forceinline__ void dyn_pace(int lag) {
  asm volatile("s_cmp_gt_i32 %0, %1\n\ts_cbranch_scc1 1f\n\ts_waitcnt vmcnt(%3)\n1:\n\ts_cmp_gt_i32 %0, %2\n\ts_cbranch_scc1 2f\n\ts_waitcnt vmcnt(%4)\n2:\n\ts_waitcnt vmcnt(%5)"
               :: "s"(lag), "n"(0), "n"(TH2 - TH1), "n"(W0 - 1 + X), "n"(W1 - 1 + X), "n"(W2 - 1 + X) : "memory", "scc");
}
template <int X> __device__ __forceinline__ void dyn_pace(unsigned long long tgt) { asm volatile("s_swappc_b64 s[98:99], %0" :: "s"(tgt) : "memory", "s98", "s99"); }
__device__ __forceinline__ int now32() { return (int)(unsigned)__builtin_amdgcn_s_memrealtime(); }

#define STAMP(i)
#define STAMPV(i, v)
#define STAMP_FLUSH()

__global__ void __launch_bounds__(NTHR, 2) fused_decode(Params) {
  __shared__ __attribute__((aligned(16))) unsigned char smem[L_END];
  LAS unsigned char* L = (LAS unsigned char*)smem;
  LAS unsigned* ctr = (LAS unsigned*)(L + L_CTR);
  const int tid = threadIdx.x, lane = tid & 63;
  int w = __builtin_amdgcn_readfirstlane(tid >> 6) ^ K_SWAP, bid = blockIdx.x;
  typedef unsigned long long s8p __attribute__((ext_vector_type(8)));
  s8p ka;
  unsigned long long wp, kws, t64, pc;
  int ldw, wbytes, so0, so1, mg, ta;
  asm volatile(
      "s_load_dwordx16 %[ka], %[kp], 0x10\n\t"
      "s_lshr_b32 %[ta], %[bid], 7\n\t"
      "s_lshl_b32 %[ta], %[ta], 3\n\t"
      "s_cmp_lt_u32 %[w], 4\n\t"
      "s_cselect_b32 %[ta], 16, %[ta]\n\t"
      "s_load_dwordx2 %[wp], %[kp], %[ta]\n\t"
      "s_load_dwordx2 %[kws], %[kp], 0x50\n\t"
      "s_memrealtime %[t]\n\t"
      "s_getpc_b64 %[pc]\n"
      ".Lpf_base:\n\t"
      "s_cmp_lg_u32 %[w], %[pw]\n\t"
      "s_cbranch_scc1 1f\n\t"
      "v_mbcnt_lo_u32_b32 v170, -1, 0\n\t"
      "s_lshr_b32 %[mg], %[bid], 3\n\t"
      "s_lshl_b32 %[mg], %[mg], 7\n\t"
      "v_lshl_add_u32 v170, v170, 12, %[mg]\n\t"
      "v_min_u32 v170, .Lfd_code_end-.Lpf_base-4, v170\n\t"
      "global_load_dword v170, v170, %[pc]\n"
      "1:\n\t"
      "s_cmp_lt_u32 %[bid], 0x80\n\t"
      "s_movk_i32 %[ldw], 0x1c00\n\t"
      "s_cselect_b32 %[ldw], 0x2000, %[ldw]\n\t"
      "s_cselect_b32 %[so1], 16, 14\n\t"
      "s_bitcmp1_b32 %[bid], 0\n\t"
      "s_cselect_b32 %[so1], %[so1], 0\n\t"
      "s_bfe_u32 %[so0], %[bid], 0x40003\n\t"
      "s_add_u32 %[so1], %[so1], %[so0]\n\t"
      "s_lshl_b32 %[so1], %[so1], 8\n\t"
      "s_bfe_u32 %[so0], %[bid], 0x20001\n\t"
      "s_lshl_b32 %[so0], %[so0], 2\n\t"
      "s_and_b32 %[mg], %[w], 3\n\t"
      "s_add_u32 %[so0], %[so0], %[mg]\n\t"
      "s_lshl_b32 %[so0], %[so0], 6\n\t"
      "s_mul_i32 %[so0], %[so0], %[ldw]\n\t"
      "s_add_u32 %[so0], %[so0], %[so1]\n\t"
      "s_lshl_b32 %[wb], %[ldw], 10\n\t"
      "s_cmp_lt_u32 %[bid], 0xf0\n\t"
      "s_cselect_b32 %[wb], %[wb], 0\n\t"
      "s_lshl_b32 %[mg], %[ldw], 3\n\t"
      "s_cmp_gt_u32 %[w], 3\n\t"
      "s_cbranch_scc1 2f\n\t"
      "s_bfe_u32 %[so0], %[bid], 0x10002\n\t"
      "s_lshl_b32 %[so1], %[w], 1\n\t"
      "s_add_u32 %[so0], %[so0], %[so1]\n\t"
      "s_lshl_b32 %[so0], %[so0], 10\n\t"
      "s_xor_b32 %[so1], %[so0], 0x400\n\t"
      "s_setprio 1\n\t"
      "s_movk_i32 %[wb], 0x2000\n\t"
      "s_movk_i32 %[mg], 0x100\n"
      "2:\n\t"
      "s_waitcnt lgkmcnt(0)"
      : [ka] "=&s"(ka), [wp] "=&s"(wp), [kws] "=&s"(kws), [t] "=&s"(t64), [pc] "=&s"(pc), [ldw] "=&s"(ldw), [wb] "=&s"(wbytes),
        [so0] "=&s"(so0), [so1] "=&s"(so1), [mg] "=&s"(mg), [ta] "=&s"(ta), [w] "+s"(w), [bid] "+s"(bid)
      : [kp] "s"(__builtin_amdgcn_kernarg_segment_ptr()), [pw] "n"(K_SWAP)
      : "scc", "v170", "memory");
  const int wl = w & 3;
  const bool isA = w < 4;
  const int xs = bid & 7, ci = bid >> 3;
  const int n16 = lane & 15, g = lane >> 4;
  const int kq = xs >> 1, isq = ci < 16, cb = (isq ? 16 : 14) * (xs & 1) + (ci & 15);
  const int off0 = __umul24(g, mg) + n16 * 16;
  int tbase = (int)(unsigned)t64;
  v4u wv[16];
  const rsrc_t rw = mk((const void*)wp, wbytes);
  v4u xr[2];
  if (isA) {
    xr[0] = ld16<0>(rw, off0, so0);
    PIN();
    xr[1] = ld16<0>(rw, off0, so1);
  }
  int w2 = w;
  asm volatile("" : "+s"(w2));
  PIN();
  if (w2 >= 4) {
#pragma unroll
    for (int j = 0; j < WINW; ++j) { wv[j] = ld16<NT>(rw, off0, so0 + ((j >> 3) * 32 + (j & 7)) * ldw); PIN(); }
  }
  PIN();
  const struct { const void *x, *kc, *vc, *wo, *w2, *b1, *b2, *out, *ws; } p = {(const void*)ka[0], (const void*)ka[1], (const void*)ka[2], (const void*)ka[3],
      (const void*)ka[4], (const void*)ka[5], (const void*)ka[6], (const void*)ka[7], (const void*)kws};
  const rsrc_t rws = mk(p.ws, WS_END);
  const int gidx = 2 * xs + (ci >> 4), ab = gidx >> 2, kvh = gidx & 3, sp = ci & 15;
  const int hd = 4 * xs + (ci >> 3), cg = ci & 7;
  const int cu = xs * 32 + ci;
  unsigned long long gen = *(const __attribute__((address_space(4))) unsigned long long*)kws;
  const int fb = K_FT ? cu & 3 : (lane >> 2) & 3, fc = K_FT ? 16 * (cu >> 2) + 4 * (lane & 3) + ((lane >> 2) & 3) : 4 * cu + (lane & 3);
  v2u sm;
#define ACT(base, q) (*(const LAS v4u*)(L + ((n16 >> 2) == (q) ? (base) : L_ZERO)))
#define PUT_RED(d) do { *(LAS f32x4*)(L + L_RED + wl * RED_W + g * RED_G + n16 * 16) = d[0]; \
                        *(LAS f32x4*)(L + L_RED + wl * RED_W + RED_E + g * RED_G + n16 * 16) = d[1]; } while (0)

  if (isA) {
    if (w == 0 && lane < 16) ctr[lane] = 0u;
    float v[16];
#pragma unroll
    for (int i = 0; i < 8; ++i) { v[2 * i] = blo(xr[i >> 2][i & 3]); v[2 * i + 1] = bhi(xr[i >> 2][i & 3]); }
    STAMP(1);
    float m, inv;
    ln_stats(v, m, inv);
    if (!isq) {
#pragma unroll
      for (int i = 0; i < 16; i += 2) { const unsigned o = ln_pair(v[i], v[i + 1], m, inv); v[i] = blo(o); v[i + 1] = bhi(o); }
      ln_stats(v, m, inv);
    }
    if ((lane >> 5) == (kq & 1))
      *(LAS v4u*)(L + L_XN + wl * 512 + (lane & 31) * 16) = v4u{ln_pair(v[0], v[1], m, inv), ln_pair(v[2], v[3], m, inv), ln_pair(v[4], v[5], m, inv), ln_pair(v[6], v[7], m, inv)};
    asm volatile("" : "+s"(gen));
    STAMP(2);
  } else {
#pragma unroll
    for (int j = WINW; j < 16; ++j) { pace<WINW - 1>(); wv[j] = ld16<NT>(rw, off0, so0 + ((j >> 3) * 32 + (j & 7)) * ldw); PIN(); }
    asm volatile("" : "+s"(gen));
    STAMP(1);
    BAR();
    STAMP(2);
    f32x4 d[2] = {{0.f, 0.f, 0.f, 0.f}, {0.f, 0.f, 0.f, 0.f}};
#pragma unroll
    for (int t = 0; t < 2; ++t) {
#pragma unroll
      for (int q = 0; q < 4; ++q) {
        const v4u act = ACT(L_XN + (n16 & 3) * 512 + (64 * wl + 32 * t + 8 * g) * 2, q);
        d[0] = mfma(act, tr8(wv + 8 * t, 2 * q), d[0]);
        d[1] = mfma(act, tr8(wv + 8 * t, 2 * q + 1), d[1]);
      }
      PIN();
    }
    PUT_RED(d);
    STAMP(3);
    class_bar<1>(ctr + 0, lane);
    if (wl < 2) {
      f32x4 sum = {0.f, 0.f, 0.f, 0.f};
#pragma unroll
      for (int ww = 0; ww < 4; ++ww)
#pragma unroll
        for (int k = 0; k < 4; ++k)
          sum[k] += *(const LAS float*)(L + L_RED + ww * RED_W + (k & 1) * RED_E + (2 * (lane & 1) + (k >> 1)) * RED_G + (wl * 8 + (n16 >> 1)) * 16 + g * 4);
      const int off = (isq ? a_q(g, cb, kq) : a_h(kq, g) + cb * 512) + wl * 256 + n16 * 16;
      st16<SC1 | NT>(__builtin_bit_cast(v4u, sum), rws, ci < 30 ? off : OOB);
    }
    {
      const v4u z = {0u, 0u, 0u, 0u};
      st16<SC1>(z, rws, wl < 2 ? a_o(8 * kvh + 4 * wl + g, ab, sp) + n16 * 16 : a_f(32 * cg + 16 * (wl & 1) + (lane >> 2), hd) + (lane & 3) * 16);
      if (wl == 2) st16<K_MLST>(z, rws, lane < 4 ? a_ml(gidx, sp) + lane * 16 : OOB);
    }
    STAMP(4);
    sm.x = __builtin_amdgcn_raw_buffer_load_b16(mk(p.x, 4 * DM * 2), wl == 2 ? (fb * DM + fc) * 2 : OOB, 0, 0);
    sm.y = __builtin_amdgcn_raw_buffer_load_b16(mk(p.b2, DM * 2), wl == 2 ? fc * 2 : OOB, 0, 0);
    if (K_OPRE == 2) __builtin_amdgcn_raw_buffer_store_b16((unsigned short)0, mk(p.out, 4 * DM * 2), wl == 2 && lane < 16 ? (fb * DM + fc) * 2 : OOB, 0, SC1);
    if (K_OPRE == 3) __builtin_amdgcn_raw_buffer_store_b128(v4u{0u, 0u, 0u, 0u}, mk(p.out, 4 * DM * 2), wl == 2 && lane < 8 && (cu & 3) == 0 ? (cu >> 2) * 128 + lane * 16 : OOB, 0, SC1);
    pace<0>();
    if (lds_arrive(ctr + 1, lane) == 3u) {
      const unsigned long long tg = (gen + 1ull) * 0x9E3779B97F4A7C15ull;
      if (ABST) st8<SC1>(lane == 2 ? v2u{(unsigned)tbase, 0u} : v2u{(unsigned)tg, (unsigned)(tg >> 32)}, rws, lane == 0 ? WS_FLAG + cu * 8 : lane < 3 ? WS_LINE + cu * 128 + lane * 8 - 8 : OOB);
      else st8<SC1>(v2u{(unsigned)tg, (unsigned)(tg >> 32)}, rws, lane == 0 ? WS_FLAG + cu * 8 : lane == 1 ? WS_LINE + cu * 128 : OOB);
    }
    STAMP(5);
  }
  const rsrc_t rk = mk((const char*)p.kc + (size_t)cu * 262144, 262144), rv = mk((const char*)p.vc + (size_t)cu * 262144, 262144);
  const int cbase = (w >> 2) * 131072 + wl * 4096;
  int koff = cbase + ((lane >> 2) & 3) * 256 + g * 64 + (lane & 3) * 16, voff = cbase + lane * 16;
  v4u r[32], fq;
  const int fhd = 8 * kvh + 2 * wl + ((lane >> 2) & 1);
  const int fqo = WS_LINE + ((2 * (lane & 3) + (fhd >> 4)) * 32 + (fhd & 15)) * 128;
  int lag = 0, tab = 0;
#define UPDATE_LAG(t, x) do { lag = now32() - tbase - ((t) + TH1); asm volatile("" : "+s"(lag)); PIN(); } while (0)
  int fq1 = isA && lane < 8 ? fqo : OOB;
  asm volatile("" : "+v"(koff), "+v"(voff), "+v"(fq1));
  asm volatile("" :: "s"(rk), "s"(tab));
  __builtin_amdgcn_s_setprio(1);
  BAR();
  if (K_KPRE && isA) __builtin_amdgcn_s_sleep(K_KPRE);
#pragma unroll
  for (int i = 0; i < 32; ++i) {
    if (i > FLPOS) dyn_pace<1>(lag);
    else if (i >= W0) dyn_pace<0>(lag);
    else if (i > 0) __builtin_amdgcn_s_sleep(RAMP);
    r[i] = ld16<NT>(rk, koff, (i >> 2) * 16384 + (i & 3) * 1024);
    PIN();
    if (i == FLPOS) { fq = ld16<SC1>(rws, fq1, 0); PIN(); }
    if ((i & 3) == 0 && i < 29) UPDATE_LAG(isA ? S2 + T2 * i / 32 : S2 + T2 + T3 * i / 32, i >= FLPOS);
  }
  __builtin_amdgcn_s_setprio(0);
  STAMP(6);
  BAR();
  const unsigned long long tag64 = (gen + 1ull) * 0x9E3779B97F4A7C15ull;
  const unsigned tag0 = (unsigned)tag64, tag1 = (unsigned)(tag64 >> 32);
  if (isA) {
    int spins = 0;
    if (__any(lane < 8 && ((fq.x ^ tag0) | (fq.y ^ tag1)) != 0u)) {
      _Pragma("nounroll") while (true) {
        REREAD();
        fq = ld16<SC1>(rws, fq1, 0);
        if (++spins > SPIN_CAP || !__any(lane < 8 && ((fq.x ^ tag0) | (fq.y ^ tag1)) != 0u)) break;
        __builtin_amdgcn_s_sleep(2);
      }
    }
    REREAD();
    STAMPV(18, spins);
    STAMP(3);
    const int qo = a_q(ab, 8 * kvh + 2 * wl + (lane >> 5), 0) + (lane & 31) * 16;
    v4u qp[4];
#pragma unroll
    for (int s = 0; s < 4; ++s) qp[s] = ld16<SC1>(rws, qo, s * 512);
    PIN();
    if (ABST && w == 0) {
      int d = 0;
#pragma unroll
      for (int k = 0; k < 4; ++k) d += min(max((int)__builtin_amdgcn_readlane(fq.z, k) - tbase, -100), 100);
      if (lane == 0) ctr[12] = tbase + (d >> 2) + (ABST - 100);
    }
    const f32x4 qs = (__builtin_bit_cast(f32x4, qp[0]) + __builtin_bit_cast(f32x4, qp[1])) + (__builtin_bit_cast(f32x4, qp[2]) + __builtin_bit_cast(f32x4, qp[3]));
    *(LAS v2u*)(L + L_Q + (2 * wl + (lane >> 5)) * 256 + (lane & 31) * 8) = v2u{pk(qs[0], qs[1]), pk(qs[2], qs[3])};
    class_bar<1>(ctr + 2, lane);
    STAMP(4);
  }
  if (ABST) tbase = __builtin_amdgcn_readfirstlane(ctr[12]);
  const int hl = n16 & 7, wb = n16 >> 3;
  const v4u qb0 = *(const LAS v4u*)(L + L_Q + hl * 256 + (4 * g + wb) * 16);
  const v4u qb1 = *(const LAS v4u*)(L + L_Q + hl * 256 + (4 * g + 2 + wb) * 16);
  float s[16];
#pragma unroll
  for (int u = 0; u < 8; ++u) {
    float t0[4], t1[4];
#pragma unroll
    for (int k = 0; k < 4; ++k) {
      const f32x4 d0 = mfma(r[4 * u + k], qb0, f32x4{0.f, 0.f, 0.f, 0.f});
      const f32x4 d1 = mfma(r[4 * u + k], qb1, f32x4{0.f, 0.f, 0.f, 0.f});
      t0[k] = d0[0] + d1[2];
      t1[k] = d0[1] + d1[3];
    }
#pragma unroll
    for (int k = 0; k < 4; k += 2) {
      const float xx = wb ? t1[k + 1] : t0[k], yy = wb ? t1[k] : t0[k + 1];
      s[2 * u + (k >> 1)] = xx + ror<8>(yy);
    }
    PIN();
  }
  float m = -3.0e38f;
#pragma unroll
  for (int i = 0; i < 16; i += 2) {
    const unsigned a = pk(s[i], s[i + 1]);
    const unsigned c = pk(blo(a) * 0.08838834764831845f, bhi(a) * 0.08838834764831845f);
    s[i] = blo(c); s[i + 1] = bhi(c);
    m = fmaxf(m, fmaxf(s[i], s[i + 1]));
  }
  m = fmaxf(m, ror<8>(m));
  m = xrow_max(m);
  if (lane < 8) *(LAS float*)(L + L_M + (w * 8 + lane) * 4) = m;
  constexpr float LOG2E = 1.4426950408889634f;
  if (!isA) {
    if (wl == 0) {
      int spins = 0;
      _Pragma("nounroll") while (true) {
        REREAD();
        const v4u a = ld16<SC1>(rws, WS_FLAG + lane * 16, 0), b = ld16<SC1>(rws, WS_FLAG + lane * 16, 1024);
        const unsigned bad = (a.x ^ tag0) | (a.y ^ tag1) | (a.z ^ tag0) | (a.w ^ tag1) | (b.x ^ tag0) | (b.y ^ tag1) | (b.z ^ tag0) | (b.w ^ tag1);
        if (!__any(bad != 0u) || ++spins > SPIN_CAP) break;
        __builtin_amdgcn_s_sleep(4);
      }
      REREAD();
      if (bid == 0) st8<SC1>(v2u{(unsigned)(gen + 1ull), (unsigned)((gen + 1ull) >> 32)}, rws, lane == 0 ? WS_E : OOB);
    }
    class_bar<1>(ctr + 3, lane);
#pragma unroll
    for (int ww = 0; ww < 8; ++ww) m = fmaxf(m, *(const LAS float*)(L + L_M + (ww * 8 + hl) * 4));
  }
  unsigned pb[16];
  float lsum = 0.f;
  {
    const unsigned msk = wb ? 0xffff0000u : 0x0000ffffu;
#pragma unroll
    for (int i = 0; i < 16; ++i) {
      const float e = __builtin_amdgcn_exp2f((s[i] - m) * LOG2E);
      lsum += e;
      const unsigned t = pk(e, e) & msk;
      pb[i] = t | f2u(ror<8>(u2f(t)));
      asm volatile("" : "+v"(pb[i]));
    }
    lsum += ror<8>(lsum);
    lsum = xrow_sum(lsum);
    if (lane < 8) *(LAS float*)(L + L_L + (w * 8 + lane) * 4) = lsum;
  }
  f32x4 acc[8];
#pragma unroll
  for (int j = 0; j < 8; ++j) acc[j] = f32x4{0.f, 0.f, 0.f, 0.f};
  asm volatile("" :: "s"(rv));
  __builtin_amdgcn_s_setprio(1);
  STAMP(7);
  BAR();
  if (K_VPRE && isA) __builtin_amdgcn_s_sleep(K_VPRE);
  STAMP(15);
#define PV(n) acc[(n) & 7] = mfma(tr8(&r[(n) & ~7], (n) & 7), v4u{pb[((n) >> 3) * 4], pb[((n) >> 3) * 4 + 1], pb[((n) >> 3) * 4 + 2], pb[((n) >> 3) * 4 + 3]}, acc[(n) & 7])
#pragma unroll
  for (int i = 0; i < 32; ++i) {
    if (i >= W0) dyn_pace<0>(lag);
    else if (i > 0) __builtin_amdgcn_s_sleep(RAMP);
    r[i] = ld16<NT>(rv, voff, (i >> 2) * 16384 + (i & 3) * 1024);
    PIN();
    if ((i & 3) == 0 && i < 29) UPDATE_LAG(S2 + T2 + T3 + (isA ? T4 * i / 32 : T4 + T5 * i / 32), 0);
    if (i >= 7 + W2) { PV(i - 7 - W2); PIN(); }
  }
  const int mo = a_ml(wl * 4 + (hd >> 3), (n16 + 4 * g) & 15) + (hd & 7) * 8;
  v2u ml = ld8<SC1>(rws, isA || K_MLPOS ? OOB : mo, 0);
  STAMP(16);
  BAR();
#pragma unroll
  for (int n = 32 - 7 - W2; n < 32; ++n) { PV(n); if ((n & 7) == 7) PIN(); }
  STAMP(8);
#define PUT_ACC(f) _Pragma("unroll") for (int i = 0; i < 4; ++i) { \
      const f32x4 t = {wb ? acc[4][i] : acc[0][i], wb ? acc[5][i] : acc[1][i], wb ? acc[6][i] : acc[2][i], wb ? acc[7][i] : acc[3][i]}; \
      *(LAS f32x4*)(L + L_O + wl * O_W + (hl * 132 + 8 * (4 * g + i) + 4 * wb) * 4) = f t; }
  const int oro = ((2 * wl + (g & 1)) * 132 + 8 * n16 + 4 * (g >> 1)) * 4;
  constexpr int S6 = S2 + T2 + T3 + T4 + T5;
  if (isA) {
    __builtin_amdgcn_s_setprio(0);
    float M = m;
#pragma unroll
    for (int ww = 0; ww < 8; ++ww) M = fmaxf(M, *(const LAS float*)(L + L_M + (ww * 8 + hl) * 4));
    const float fa = __builtin_amdgcn_exp2f((m - M) * LOG2E);
    PUT_ACC(fa *);
    if (lane < 8) *(LAS float*)(L + L_L + (w * 8 + lane) * 4) = fa * lsum;
    class_bar<1>(ctr + 4, lane);
    {
      f32x4 t = *(const LAS f32x4*)(L + L_O + oro);
#pragma unroll
      for (int ww = 1; ww < 4; ++ww) t += *(const LAS f32x4*)(L + L_O + ww * O_W + oro);
      *(LAS f32x4*)(L + L_O + 4 * O_W + oro) = t;
    }
    if (w == 0) {
      float l = 0.f;
#pragma unroll
      for (int ww = 0; ww < 8; ++ww) l += *(const LAS float*)(L + L_L + (ww * 8 + hl) * 4);
      if (K_MLST == 16) st8<SC1>(v2u{nz(f2u(M)), nz(f2u(l))}, rws, lane < 8 ? a_ml(gidx, sp) + lane * 8 : OOB);
      else {
        const unsigned a = nz(f2u(M)), b = nz(f2u(l));
        const int src = (lane & 3) * 8;
        st16<K_MLST>(v4u{(unsigned)__builtin_amdgcn_ds_bpermute(src, a), (unsigned)__builtin_amdgcn_ds_bpermute(src, b), (unsigned)__builtin_amdgcn_ds_bpermute(src + 4, a), (unsigned)__builtin_amdgcn_ds_bpermute(src + 4, b)}, rws, lane < 4 ? a_ml(gidx, sp) + lane * 16 : OOB);
      }
    }
    {
      const int hc = lane & 7, hbt = (lane >> 3) & 3, hb = 112 * hd + 28 * wl + 4 * hc;
      const int ho = hc < 7 ? a_h(2 * (lane >> 5), hbt) + hb * 4 : OOB;
      const v4u h0 = ld16<SC1>(rws, ho, 0), h1 = ld16<SC1>(rws, ho, FF * 4);
      const v2u bia = ld8<0>(mk(p.b1, FF * 2), hc < 7 ? hb * 2 : OOB, 0);
      PIN();
      float hv[4];
#pragma unroll
      for (int j = 0; j < 4; ++j) {
        const float t0 = u2f(h0[j]) + u2f(h1[j]);
        auto sw = __builtin_amdgcn_permlane32_swap(f2u(t0), f2u(t0), false, false);
        const float t = u2f(sw[0]) + u2f(sw[1]);
        const float u = rbf(rbf(t) + ((j & 1) ? bhi(bia[j >> 1]) : blo(bia[j >> 1])));
        const float z2 = u * fmaf(u * u, 0.044715f, 1.0f) * (-2.0f * 0.7978845608028654f * LOG2E);
        hv[j] = u * __builtin_amdgcn_rcpf(1.0f + __builtin_amdgcn_exp2f(z2));
      }
      if (lane < 32) *(LAS v2u*)(L + L_H + wl * 256 + hbt * 64 + hc * 8) = v2u{pk(hv[0], hv[1]), pk(hv[2], hv[3])};
    }
    const rsrc_t r2 = mk(p.w2, FF * DM * 2), ro = mk(p.wo, NQ * DM * 2);
    const int o2 = (112 * hd + 28 * wl + 8 * g) * 2048 + cg * 256 + n16 * 16;
    const int o2b = g < 3 ? o2 : OOB;
    const int oo = (128 * hd + 32 * wl + 8 * g) * 2048 + cg * 256 + n16 * 16;
    REREAD();
    v4u hact[4];
#pragma unroll
    for (int q = 0; q < 4; ++q) hact[q] = ACT(L_H + wl * 256 + (n16 & 3) * 64 + g * 16, q);
    int rdo = K_FT ? L_RED + 2 * (lane & 1) * RED_G + (4 * wl + 2 * ((lane >> 4) & 1) + ((lane >> 1) & 1)) * 16 + ((lane >> 2) & 3) * 4
                   : L_RED + (lane & 1) * RED_E + ((lane >> 1) & 3) * RED_G + (4 * wl + ((lane >> 3) & 3)) * 16;
    int fo = lane < 32 ? a_f(32 * cg + 8 * wl + (lane >> 2), hd) + (lane & 3) * 16 : OOB;
    int oa[4];
#pragma unroll
    for (int q = 0; q < 4; ++q) { oa[q] = (n16 >> 2) == q ? L_OA + (n16 & 3) * 256 + (32 * wl + 8 * g) * 2 : L_ZERO; asm volatile("" : "+v"(oa[q])); }
    asm volatile("" : "+v"(rdo), "+v"(fo));
    v4u tw[16];
    asm volatile("" :: "s"(r2), "s"(ro));
    asm volatile("" :: "v"(o2), "v"(o2b), "v"(oo));
    STAMP(5);
    if (K_TLB) {
      const unsigned t2 = __builtin_amdgcn_raw_buffer_load_b32(r2, lane == 0 ? o2 : OOB, 0, NT), to = __builtin_amdgcn_raw_buffer_load_b32(ro, lane == 0 ? oo : OOB, 0, NT);
      asm volatile("" :: "v"(t2), "v"(to));
    }
    BAR();
    if (K_TPRE) __builtin_amdgcn_s_sleep(K_TPRE);
    if (K_TABS > -1000) { _Pragma("nounroll") while (now32() - tbase < S6 + K_TABS) __builtin_amdgcn_s_sleep(1); }
#pragma unroll
    for (int i = 0; i < 16; ++i) {
      const int j = TORD == 0 ? i : TORD == 1 ? i ^ 8 : (i >> 1) + 8 * (i & 1);
      if (TDYN && i >= W0) dyn_pace<0>(lag);
      else if (i >= K_TOPEN) {}
      else if (i >= WINT + 3 * K_TGROW) pace<WINT + 2>();
      else if (i >= WINT + 2 * K_TGROW) pace<WINT + 1>();
      else if (i >= WINT + K_TGROW) pace<WINT>();
      else if (i >= WINT) pace<WINT - 1>();
      else if (i > 0) __builtin_amdgcn_s_sleep(K_TRAMP);
      tw[j] = j < 8 ? ld16<NT>(r2, j < 4 ? o2 : o2b, j * 2048) : ld16<NT>(ro, oo, (j - 8) * 2048);
      PIN();
      if (TDYN && (i & 3) == 0) UPDATE_LAG(S6 + T6 * i / 16, TDYN - 1);
    }
    STAMP(9);
    f32x4 d[2] = {{0.f, 0.f, 0.f, 0.f}, {0.f, 0.f, 0.f, 0.f}};
    v4u wt[8];
    if (TORD != 1) {
#pragma unroll
      for (int j0 = 0; j0 < 8; ++j0) d[j0 & 1] = mfma(hact[j0 >> 1], tr8(tw, j0), d[j0 & 1]);
      PIN();
    }
#pragma unroll
    for (int k = 0; k < 4; ++k) {
#pragma unroll
      for (int j0 = 0; j0 < 8; ++j0) wt[j0][k] = __builtin_amdgcn_perm(tw[9 + 2 * k][j0 >> 1], tw[8 + 2 * k][j0 >> 1], (j0 & 1) ? 0x07060302u : 0x05040100u);
      PIN();
    }
#pragma unroll
    for (int j0 = 0; j0 < 8; ++j0) asm volatile("" : "+v"(wt[j0]));
    if (TORD == 1) {
#pragma unroll
      for (int j0 = 0; j0 < 8; ++j0) d[j0 & 1] = mfma(hact[j0 >> 1], tr8(tw, j0), d[j0 & 1]);
    }
    STAMP(10);
    BAR();
    STAMP(11);
    v4u oact[4];
#pragma unroll
    for (int q = 0; q < 4; ++q) oact[q] = *(const LAS v4u*)(L + oa[q]);
#pragma unroll
    for (int j0 = 0; j0 < 8; ++j0) d[j0 & 1] = mfma(oact[j0 >> 1], wt[j0], d[j0 & 1]);
    PUT_RED(d);
    BAR();
    STAMP(12);
    f32x4 t;
    if (K_FT) {
#pragma unroll
      for (int k = 0; k < 4; ++k) {
        const int o = rdo + (k & 1) * RED_E + (k >> 1) * RED_G;
        t[k] = (*(const LAS float*)(L + o) + *(const LAS float*)(L + o + RED_W)) + (*(const LAS float*)(L + o + 2 * RED_W) + *(const LAS float*)(L + o + 3 * RED_W));
      }
    } else t = (*(const LAS f32x4*)(L + rdo) + *(const LAS f32x4*)(L + rdo + RED_W)) + (*(const LAS f32x4*)(L + rdo + 2 * RED_W) + *(const LAS f32x4*)(L + rdo + 3 * RED_W));
    st16<SC1>(v4u{nz(f2u(t[0])), nz(f2u(t[1])), nz(f2u(t[2])), nz(f2u(t[3]))}, rws, fo);
    STAMP(13);
    asm volatile(".Lfd_code_end:");
  } else {
    PUT_ACC();
    class_bar<0>(ctr + 5, lane);
    STAMP(9);
    {
      f32x4 t = *(const LAS f32x4*)(L + L_O + 4 * O_W + oro);
#pragma unroll
      for (int ww = 0; ww < 4; ++ww) t += *(const LAS f32x4*)(L + L_O + ww * O_W + oro);
      const unsigned a0 = nz(pk(t[0], t[1])), a1 = nz(pk(t[2], t[3]));
      auto s0 = __builtin_amdgcn_permlane32_swap(a0, a0, false, false);
      auto s1 = __builtin_amdgcn_permlane32_swap(a1, a1, false, false);
      st16<SC1>(v4u{s0[0], s1[0], s0[1], s1[1]}, rws, lane < 32 ? a_o(8 * kvh + 2 * wl + g, ab, sp) + n16 * 16 : OOB);
    }
    PIN();
    STAMP(10);
    __builtin_amdgcn_s_setprio(0);
    int spins = 0;
    if (__any(min(ml.x, ml.y) == 0u)) {
      _Pragma("nounroll") while (true) {
        REREAD();
        ml = ld8<SC1>(rws, mo, 0);
        if (!__any(min(ml.x, ml.y) == 0u) || ++spins > SPIN_CAP) break;
        __builtin_amdgcn_s_sleep(2);
      }
    }
    STAMPV(19, spins);
    float M = u2f(ml.x);
    M = fmaxf(M, ror<8>(M)); M = fmaxf(M, ror<4>(M)); M = fmaxf(M, ror<2>(M)); M = fmaxf(M, ror<1>(M));
    const float e = __builtin_amdgcn_exp2f((u2f(ml.x) - M) * LOG2E);
    float Ls = e * u2f(ml.y);
    Ls += ror<8>(Ls); Ls += ror<4>(Ls); Ls += ror<2>(Ls); Ls += ror<1>(Ls);
    const float f = e * __builtin_amdgcn_rcpf(Ls);
    float fk[4] = {dpp<0x150>(f), dpp<0x151>(f), dpp<0x152>(f), dpp<0x153>(f)};
    int wro = L_OA + wl * 256 + n16 * 16 + g * 4, fo = a_f(cu, 0) + lane * 16, oof = lane < 16 ? (fb * DM + fc) * 2 : OOB;
    float res = blo(sm.x) + blo(sm.y);
    asm volatile("" : "+v"(fk[0]), "+v"(fk[1]), "+v"(fk[2]), "+v"(fk[3]), "+v"(wro), "+v"(fo), "+v"(oof), "+v"(res));
    __builtin_amdgcn_s_sleep(ODMIN);
    _Pragma("nounroll") while (now32() - tbase < S6 + TO) __builtin_amdgcn_s_sleep(1);
    const int oo_ = a_o(hd, wl, 4 * g) + n16 * 16;
    v4u op[4];
#pragma unroll
    for (int k = 0; k < 4; ++k) op[k] = ld16<SC1>(rws, oo_, k * 256);
    PIN();
    spins = 0;
    if (__any(min(min(umin4(op[0]), umin4(op[1])), min(umin4(op[2]), umin4(op[3]))) == 0u)) {
      _Pragma("nounroll") while (true) {
        REREAD();
        v4u nw[4];
#pragma unroll
        for (int k = 0; k < 4; ++k) nw[k] = ld16<SC1>(rws, umin4(op[k]) == 0u ? oo_ : OOB, k * 256);
        PIN();
#pragma unroll
        for (int k = 0; k < 4; ++k) op[k] |= nw[k];
        if (++spins > SPIN_CAP || !__any(min(min(umin4(op[0]), umin4(op[1])), min(umin4(op[2]), umin4(op[3]))) == 0u)) break;
        __builtin_amdgcn_s_sleep(1);
      }
    }
    STAMPV(18, spins);
    STAMP(13);
    f32x2 t[4];
#pragma unroll
    for (int j = 0; j < 4; ++j) {
      t[j] = fk[0] * f32x2{blo(op[0][j]), bhi(op[0][j])};
#pragma unroll
      for (int k = 1; k < 4; ++k) t[j] = __builtin_elementwise_fma(f32x2{fk[k], fk[k]}, f32x2{blo(op[k][j]), bhi(op[k][j])}, t[j]);
    }
    float u[4];
#pragma unroll
    for (int j = 0; j < 4; ++j) { auto sw = __builtin_amdgcn_permlane32_swap(f2u(t[j >> 1][j & 1]), f2u(t[2 + (j >> 1)][j & 1]), false, false); u[j] = u2f(sw[0]) + u2f(sw[1]); }
    auto s0 = __builtin_amdgcn_permlane16_swap(f2u(u[0]), f2u(u[2]), false, false);
    auto s1 = __builtin_amdgcn_permlane16_swap(f2u(u[1]), f2u(u[3]), false, false);
    *(LAS unsigned*)(L + wro) = pk(u2f(s0[0]) + u2f(s0[1]), u2f(s1[0]) + u2f(s1[1]));
    BAR();
    BAR();
    if (wl == 2) {
      v4u f0, f1;
      spins = 0;
      __builtin_amdgcn_s_sleep(FDELAY);
      _Pragma("nounroll") while (true) {
        REREAD();
        f0 = ld16<SC1>(rws, fo, 0);
        f1 = ld16<SC1>(rws, fo, 1024);
        if (!__any(min(umin4(f0), umin4(f1)) == 0u) || ++spins > SPIN_CAP) break;
        __builtin_amdgcn_s_sleep(1);
      }
      STAMPV(17, spins);
      STAMP(11);
      const f32x4 y4 = __builtin_bit_cast(f32x4, f0) + __builtin_bit_cast(f32x4, f1);
      const bool h8 = lane & 8, h4 = lane & 4;
      const float a0 = (h8 ? y4[2] : y4[0]) + ror<8>(h8 ? y4[0] : y4[2]), a1 = (h8 ? y4[3] : y4[1]) + ror<8>(h8 ? y4[1] : y4[3]);
      const float y = res + xrow_sum((h4 ? a1 : a0) + dpp<0x1B>(dpp<0x141>(h4 ? a0 : a1)));
      __builtin_amdgcn_raw_buffer_store_b16((unsigned short)pk(y, y), mk(p.out, 4 * DM * 2), oof, 0, K_OAUX);
      STAMP(12);
    }
  }
  STAMP_FLUSH();
}
}

extern "C" void kernel_launch(void* const* d_in, const int* in_sizes, int n_in, void* d_out, int out_size, void* d_ws, size_t ws_size,
                              hipStream_t stream) {
  Params p;
  p.x = d_in[0]; p.kc = d_in[1]; p.vc = d_in[2]; p.wq = d_in[3]; p.wo = d_in[4];
  p.w1 = d_in[6]; p.b1 = d_in[7]; p.w2 = d_in[8]; p.b2 = d_in[9];
  p.out = d_out; p.ws = d_ws;
  hipLaunchKernelGGL(fused_decode, dim3(NWG), dim3(NTHR), 0, stream, p);
}
```

```cpp
#define STAMPS 1
#define DIAG 1
#define DIAGW 0
#define DIAGB -1
#define DIAGS 1
#include <hip/hip_runtime.h>
#include <hip/hip_bf16.h>
#include <stdint.h>

#define KNOB(name, val) constexpr int name = val
#define K_WINW 6
#define K_WINT 6
#define K_W0 4
#define K_W1 5
#define K_W2 7
#define K_TH1 -30
#define K_TH2 20
#define K_SB -10
#define K_S2 235
#define K_T2 465
#define K_T3 460
#define K_T4 430
#define K_T5 445
#define K_T6 215
#define K_TO 220
#define K_ODMIN 8
#define K_FDELAY 22
#define K_RAMP 3
#define K_FLPOS 28
#define K_TORD 0
#define K_TDYN 0
#define K_DL 50
#define K_MLPOS 0
#define K_MLST 16
#define K_TRAMP 2
#define K_OPRE 0
#define K_OAUX 0
#define K_FT 0
#define K_INL 1
#define K_TPRE 12
#define K_TLB 0
#define K_TABS -1000
#define K_KPRE 0
#define K_VPRE 8
#define K_TOPEN 16
#define K_TGROW 99
#define K_SWAP 0
#define K_ABS 100

namespace {
typedef unsigned v4u __attribute__((ext_vector_type(4)));
typedef unsigned v2u __attribute__((ext_vector_type(2)));
typedef float f32x4 __attribute__((ext_vector_type(4)));
typedef short bf16x8 __attribute__((ext_vector_type(8)));
typedef __bf16 bf16x2 __attribute__((ext_vector_type(2)));
typedef float f32x2 __attribute__((ext_vector_type(2)));
typedef __amdgpu_buffer_rsrc_t rsrc_t;
#define GAS __attribute__((address_space(1)))
#define LAS __attribute__((address_space(3)))

constexpr int NWG = 256, NTHR = 512;
constexpr int DM = 1024, NQ = 4096, FF = 3584;
KNOB(WINW, K_WINW); KNOB(WINT, K_WINT); KNOB(W0, K_W0); KNOB(W1, K_W1); KNOB(W2, K_W2); KNOB(TH1, K_TH1); KNOB(TH2, K_TH2);
KNOB(S2, K_S2 + K_SB); KNOB(T2, K_T2); KNOB(T3, K_T3); KNOB(T4, K_T4); KNOB(T5, K_T5); KNOB(T6, K_T6); KNOB(TO, K_TO);
KNOB(ODMIN, K_ODMIN); KNOB(FDELAY, K_FDELAY); KNOB(RAMP, K_RAMP); KNOB(FLPOS, K_FLPOS); KNOB(TORD, K_TORD); KNOB(TDYN, K_TDYN); KNOB(ABST, K_ABS);
constexpr int WS_E = 0;
constexpr int WS_FLAG = 4096;
constexpr int WS_LINE = 8192;
constexpr int WS_ML = WS_LINE + 32768;
constexpr int WS_Q = WS_ML + 16384;
constexpr int WS_H = WS_Q + 262144;
constexpr int WS_O = WS_H + 16 * FF * 4;
constexpr int WS_F = WS_O + 524288;
constexpr int WS_END = WS_F + 524288;
constexpr int WS_STAMP = 8 << 20;
__device__ __forceinline__ int a_ml(int grp, int s) { return WS_ML + (grp * 16 + s) * 64; }
__device__ __forceinline__ int a_q(int b, int head, int kq) { return WS_Q + ((b * 32 + head) * 4 + kq) * 512; }
__device__ __forceinline__ int a_h(int kq, int b) { return WS_H + (((kq >> 1) * 4 + b) * 2 + (kq & 1)) * FF * 4; }
__device__ __forceinline__ int a_o(int head, int b, int s) { return WS_O + ((head * 4 + b) * 16 + s) * 256; }
__device__ __forceinline__ int a_f(int f, int hd) { return WS_F + (f * 32 + hd) * 64; }
constexpr int OOB = 0x7f000000;
constexpr int NT = 2, SC1 = 16;
constexpr int SPIN_CAP = 1 << 18;
constexpr int L_CTR = 0;
constexpr int L_ZERO = 32;
constexpr int L_XN = 64;
constexpr int L_Q = L_XN + 2048;
constexpr int L_M = L_Q + 2048;
constexpr int L_L = L_M + 256;
constexpr int L_RED = L_L + 256;
constexpr int RED_G = 320, RED_E = 4 * RED_G + 32, RED_W = 2 * RED_E;
constexpr int L_O = L_RED + 4 * RED_W;
constexpr int O_W = 8 * 132 * 4;
constexpr int L_H = L_O + 5 * O_W;
constexpr int L_OA = L_H + 1024;
constexpr int L_END = L_OA + 1024;

struct Params {
  const void *wq, *w1, *x, *kc, *vc, *wo, *w2, *b1, *b2;
  void *out, *ws;
};

__device__ __forceinline__ rsrc_t mk(const void* p, int bytes) { return __builtin_amdgcn_make_buffer_rsrc((void*)p, 0, bytes, 0x00020000); }
template <int AUX> __device__ __forceinline__ v4u ld16(rsrc_t r, int voff, int soff) { return __builtin_amdgcn_raw_buffer_load_b128(r, voff, soff, AUX); }
template <int AUX> __device__ __forceinline__ v2u ld8(rsrc_t r, int voff, int soff) { return __builtin_amdgcn_raw_buffer_load_b64(r, voff, soff, AUX); }
template <int AUX> __device__ __forceinline__ void st16(v4u v, rsrc_t r, int voff) { __builtin_amdgcn_raw_buffer_store_b128(v, r, voff, 0, AUX); }
template <int AUX> __device__ __forceinline__ void st8(v2u v, rsrc_t r, int voff) { __builtin_amdgcn_raw_buffer_store_b64(v, r, voff, 0, AUX); }
__device__ __forceinline__ f32x4 mfma(v4u a, v4u b, f32x4 c) {
  return __builtin_amdgcn_mfma_f32_16x16x32_bf16(__builtin_bit_cast(bf16x8, a), __builtin_bit_cast(bf16x8, b), c, 0, 0, 0);
}
__device__ __forceinline__ float u2f(unsigned u) { return __builtin_bit_cast(float, u); }
__device__ __forceinline__ unsigned f2u(float f) { return __builtin_bit_cast(unsigned, f); }
__device__ __forceinline__ unsigned pk(float lo, float hi) {
  f32x2 v = {lo, hi};
  return __builtin_bit_cast(unsigned, __builtin_convertvector(v, bf16x2));
}
__device__ __forceinline__ float blo(unsigned u) { return u2f(u << 16); }
__device__ __forceinline__ float bhi(unsigned u) { return u2f(u & 0xffff0000u); }
__device__ __forceinline__ float rbf(float f) { return blo(pk(f, f)); }
template <int C> __device__ __forceinline__ float dpp(float x) { return u2f(__builtin_amdgcn_mov_dpp(f2u(x), C, 0xf, 0xf, true)); }
template <int N> __device__ __forceinline__ float ror(float x) { return dpp<0x120 + N>(x); }
__device__ __forceinline__ float xrow_sum(float x) {
  auto s = __builtin_amdgcn_permlane16_swap(f2u(x), f2u(x), false, false);
  x = u2f(s[0]) + u2f(s[1]);
  auto t = __builtin_amdgcn_permlane32_swap(f2u(x), f2u(x), false, false);
  return u2f(t[0]) + u2f(t[1]);
}
__device__ __forceinline__ float xrow_max(float x) {
  auto s = __builtin_amdgcn_permlane16_swap(f2u(x), f2u(x), false, false);
  x = fmaxf(u2f(s[0]), u2f(s[1]));
  auto t = __builtin_amdgcn_permlane32_swap(f2u(x), f2u(x), false, false);
  return fmaxf(u2f(t[0]), u2f(t[1]));
}
__device__ __forceinline__ float wave_sum(float x) {
  x += ror<8>(x); x += ror<4>(x); x += ror<2>(x); x += ror<1>(x);
  return xrow_sum(x);
}
__device__ __forceinline__ unsigned umin4(v4u v) { return min(min(v.x, v.y), min(v.z, v.w)); }
__device__ __forceinline__ unsigned nz(unsigned u) { return max(u, 1u); }
#define PIN() __builtin_amdgcn_sched_barrier(0)
#define BAR() do { PIN(); asm volatile("s_waitcnt lgkmcnt(0)\n\ts_barrier" ::: "memory"); PIN(); } while (0)
#define REREAD() asm volatile("" ::: "memory")
__device__ __forceinline__ v4u tr8(const v4u* w, int j0) {
  const unsigned sel = (j0 & 1) ? 0x07060302u : 0x05040100u;
  const int c = j0 >> 1;
  return v4u{__builtin_amdgcn_perm(w[1][c], w[0][c], sel), __builtin_amdgcn_perm(w[3][c], w[2][c], sel),
             __builtin_amdgcn_perm(w[5][c], w[4][c], sel), __builtin_amdgcn_perm(w[7][c], w[6][c], sel)};
}
__device__ __forceinline__ void ln_stats(const float (&v)[16], float& m, float& inv) {
  float s = 0.f;
#pragma unroll
  for (int i = 0; i < 16; ++i) s += v[i];
  const float mf = wave_sum(s) * (1.0f / DM);
  float ss = 0.f;
#pragma unroll
  for (int i = 0; i < 16; ++i) { const float d = v[i] - mf; ss = fmaf(d, d, ss); }
  const float var = rbf(wave_sum(ss) * (1.0f / DM));
  m = rbf(mf);
  inv = __builtin_amdgcn_rcpf(rbf(__builtin_amdgcn_sqrtf(rbf(var + 1e-5f))));
}
__device__ __forceinline__ unsigned ln_pair(float a, float b, float m, float inv) {
  const unsigned d = pk(a - m, b - m);
  return pk(blo(d) * inv, bhi(d) * inv);
}
__device__ __forceinline__ unsigned lds_arrive(LAS unsigned* c, int lane) {
  unsigned old = 0;
  REREAD();
  if (lane == 0) old = __hip_atomic_fetch_add(c, 1u, __ATOMIC_RELAXED, __HIP_MEMORY_SCOPE_WORKGROUP);
  return __builtin_amdgcn_readfirstlane(old);
}
template <int SL> __device__ __forceinline__ void class_bar(LAS unsigned* c, int lane) {
  PIN();
  if (lds_arrive(c, lane) != 3u) {
    _Pragma("nounroll") while (__builtin_amdgcn_readfirstlane(__hip_atomic_load(c, __ATOMIC_RELAXED, __HIP_MEMORY_SCOPE_WORKGROUP)) < 4u)
      __builtin_amdgcn_s_sleep(SL);
  }
  REREAD();
  PIN();
}
template <int N> __device__ __forceinline__ void pace() { asm volatile("s_waitcnt vmcnt(%0)" :: "n"(N) : "memory"); }
template <int X> __device__ __forceinline__ void dyn_pace(int lag) {
  asm volatile("s_cmp_gt_i32 %0, %1\n\ts_cbranch_scc1 1f\n\ts_waitcnt vmcnt(%3)\n1:\n\ts_cmp_gt_i32 %0, %2\n\ts_cbranch_scc1 2f\n\ts_waitcnt vmcnt(%4)\n2:\n\ts_waitcnt vmcnt(%5)"
               :: "s"(lag), "n"(0), "n"(TH2 - TH1), "n"(W0 - 1 + X), "n"(W1 - 1 + X), "n"(W2 - 1 + X) : "memory", "scc");
}
template <int X> __device__ __forceinline__ void dyn_pace(unsigned long long tgt) { asm volatile("s_swappc_b64 s[98:99], %0" :: "s"(tgt) : "memory", "s98", "s99"); }
__device__ __forceinline__ int now32() { return (int)(unsigned)__builtin_amdgcn_s_memrealtime(); }

#define STAMP(i) do { PIN(); ts_[i] = (unsigned)__builtin_amdgcn_s_memrealtime(); PIN(); } while (0)
#define STAMPV(i, v) do { ts_[i] = (unsigned)(v); } while (0)
#define STAMP_FLUSH() do { if (lane == 0) { _Pragma("unroll") for (int i_ = 0; i_ < 20; ++i_) ((GAS unsigned long long*)((GAS char*)p.ws + WS_STAMP))[(bid * 8 + w) * 20 + i_] = ts_[i_]; } } while (0)
#undef STAMP_FLUSH
#define STAMP_FLUSH() do { if (w == DIAGW && (DIAGB < 0 ? ci < 30 : bid == DIAGB)) { const unsigned lim_ = DIAGS > 0 ? 6000u + 8u * (ts_[DIAG] - ts_[0]) : 30000u - 8u * (ts_[DIAG] - ts_[0]); \
    while ((unsigned)__builtin_amdgcn_s_memrealtime() - ts_[0] < lim_ && lim_ < 100000u) __builtin_amdgcn_s_sleep(1); } } while (0)

__global__ void __launch_bounds__(NTHR, 2) fused_decode(Params) {
  __shared__ __attribute__((aligned(16))) unsigned char smem[L_END];
  LAS unsigned char* L = (LAS unsigned char*)smem;
  LAS unsigned* ctr = (LAS unsigned*)(L + L_CTR);
  const int tid = threadIdx.x, lane = tid & 63;
  int w = __builtin_amdgcn_readfirstlane(tid >> 6) ^ K_SWAP, bid = blockIdx.x;
  typedef unsigned long long s8p __attribute__((ext_vector_type(8)));
  s8p ka;
  unsigned long long wp, kws, t64, pc;
  int ldw, wbytes, so0, so1, mg, ta;
  asm volatile(
      "s_load_dwordx16 %[ka], %[kp], 0x10\n\t"
      "s_lshr_b32 %[ta], %[bid], 7\n\t"
      "s_lshl_b32 %[ta], %[ta], 3\n\t"
      "s_cmp_lt_u32 %[w], 4\n\t"
      "s_cselect_b32 %[ta], 16, %[ta]\n\t"
      "s_load_dwordx2 %[wp], %[kp], %[ta]\n\t"
      "s_load_dwordx2 %[kws], %[kp], 0x50\n\t"
      "s_memrealtime %[t]\n\t"
      "s_getpc_b64 %[pc]\n"
      ".Lpf_base:\n\t"
      "s_cmp_lg_u32 %[w], %[pw]\n\t"
      "s_cbranch_scc1 1f\n\t"
      "v_mbcnt_lo_u32_b32 v170, -1, 0\n\t"
      "s_lshr_b32 %[mg], %[bid], 3\n\t"
      "s_lshl_b32 %[mg], %[mg], 7\n\t"
      "v_lshl_add_u32 v170, v170, 12, %[mg]\n\t"
      "v_min_u32 v170, .Lfd_code_end-.Lpf_base-4, v170\n\t"
      "global_load_dword v170, v170, %[pc]\n"
      "1:\n\t"
      "s_cmp_lt_u32 %[bid], 0x80\n\t"
      "s_movk_i32 %[ldw], 0x1c00\n\t"
      "s_cselect_b32 %[ldw], 0x2000, %[ldw]\n\t"
      "s_cselect_b32 %[so1], 16, 14\n\t"
      "s_bitcmp1_b32 %[bid], 0\n\t"
      "s_cselect_b32 %[so1], %[so1], 0\n\t"
      "s_bfe_u32 %[so0], %[bid], 0x40003\n\t"
      "s_add_u32 %[so1], %[so1], %[so0]\n\t"
      "s_lshl_b32 %[so1], %[so1], 8\n\t"
      "s_bfe_u32 %[so0], %[bid], 0x20001\n\t"
      "s_lshl_b32 %[so0], %[so0], 2\n\t"
      "s_and_b32 %[mg], %[w], 3\n\t"
      "s_add_u32 %[so0], %[so0], %[mg]\n\t"
      "s_lshl_b32 %[so0], %[so0], 6\n\t"
      "s_mul_i32 %[so0], %[so0], %[ldw]\n\t"
      "s_add_u32 %[so0], %[so0], %[so1]\n\t"
      "s_lshl_b32 %[wb], %[ldw], 10\n\t"
      "s_cmp_lt_u32 %[bid], 0xf0\n\t"
      "s_cselect_b32 %[wb], %[wb], 0\n\t"
      "s_lshl_b32 %[mg], %[ldw], 3\n\t"
      "s_cmp_gt_u32 %[w], 3\n\t"
      "s_cbranch_scc1 2f\n\t"
      "s_bfe_u32 %[so0], %[bid], 0x10002\n\t"
      "s_lshl_b32 %[so1], %[w], 1\n\t"
      "s_add_u32 %[so0], %[so0], %[so1]\n\t"
      "s_lshl_b32 %[so0], %[so0], 10\n\t"
      "s_xor_b32 %[so1], %[so0], 0x400\n\t"
      "s_setprio 1\n\t"
      "s_movk_i32 %[wb], 0x2000\n\t"
      "s_movk_i32 %[mg], 0x100\n"
      "2:\n\t"
      "s_waitcnt lgkmcnt(0)"
      : [ka] "=&s"(ka), [wp] "=&s"(wp), [kws] "=&s"(kws), [t] "=&s"(t64), [pc] "=&s"(pc), [ldw] "=&s"(ldw), [wb] "=&s"(wbytes),
        [so0] "=&s"(so0), [so1] "=&s"(so1), [mg] "=&s"(mg), [ta] "=&s"(ta), [w] "+s"(w), [bid] "+s"(bid)
      : [kp] "s"(__builtin_amdgcn_kernarg_segment_ptr()), [pw] "n"(K_SWAP)
      : "scc", "v170", "memory");
  const int wl = w & 3;
  const bool isA = w < 4;
  const int xs = bid & 7, ci = bid >> 3;
  const int n16 = lane & 15, g = lane >> 4;
  const int kq = xs >> 1, isq = ci < 16, cb = (isq ? 16 : 14) * (xs & 1) + (ci & 15);
  const int off0 = __umul24(g, mg) + n16 * 16;
  unsigned ts_[20] = {(unsigned)t64, 0, 0, 0, 0, 0, 0, 0, 0, 0, 0, 0, 0, 0, 0, 0, 0, 0, 0, 0};
  int tbase = (int)(unsigned)t64;
  v4u wv[16];
  const rsrc_t rw = mk((const void*)wp, wbytes);
  v4u xr[2];
  if (isA) {
    xr[0] = ld16<0>(rw, off0, so0);
    PIN();
    xr[1] = ld16<0>(rw, off0, so1);
  }
  int w2 = w;
  asm volatile("" : "+s"(w2));
  PIN();
  if (w2 >= 4) {
#pragma unroll
    for (int j = 0; j < WINW; ++j) { wv[j] = ld16<NT>(rw, off0, so0 + ((j >> 3) * 32 + (j & 7)) * ldw); PIN(); }
  }
  PIN();
  const struct { const void *x, *kc, *vc, *wo, *w2, *b1, *b2, *out, *ws; } p = {(const void*)ka[0], (const void*)ka[1], (const void*)ka[2], (const void*)ka[3],
      (const void*)ka[4], (const void*)ka[5], (const void*)ka[6], (const void*)ka[7], (const void*)kws};
  const rsrc_t rws = mk(p.ws, WS_END);
  const int gidx = 2 * xs + (ci >> 4), ab = gidx >> 2, kvh = gidx & 3, sp = ci & 15;
  const int hd = 4 * xs + (ci >> 3), cg = ci & 7;
  const int cu = xs * 32 + ci;
  unsigned long long gen = *(const __attribute__((address_space(4))) unsigned long long*)kws;
  const int fb = K_FT ? cu & 3 : (lane >> 2) & 3, fc = K_FT ? 16 * (cu >> 2) + 4 * (lane & 3) + ((lane >> 2) & 3) : 4 * cu + (lane & 3);
  v2u sm;
#define ACT(base, q) (*(const LAS v4u*)(L + ((n16 >> 2) == (q) ? (base) : L_ZERO)))
#define PUT_RED(d) do { *(LAS f32x4*)(L + L_RED + wl * RED_W + g * RED_G + n16 * 16) = d[0]; \
                        *(LAS f32x4*)(L + L_RED + wl * RED_W + RED_E + g * RED_G + n16 * 16) = d[1]; } while (0)

  if (isA) {
    if (w == 0 && lane < 16) ctr[lane] = 0u;
    float v[16];
#pragma unroll
    for (int i = 0; i < 8; ++i) { v[2 * i] = blo(xr[i >> 2][i & 3]); v[2 * i + 1] = bhi(xr[i >> 2][i & 3]); }
    asm volatile("" : "+v"(v[0]), "+v"(v[15]));
    STAMP(1);
    float m, inv;
    ln_stats(v, m, inv);
    if (!isq) {
#pragma unroll
      for (int i = 0; i < 16; i += 2) { const unsigned o = ln_pair(v[i], v[i + 1], m, inv); v[i] = blo(o); v[i + 1] = bhi(o); }
      ln_stats(v, m, inv);
    }
    if ((lane >> 5) == (kq & 1))
      *(LAS v4u*)(L + L_XN + wl * 512 + (lane & 31) * 16) = v4u{ln_pair(v[0], v[1], m, inv), ln_pair(v[2], v[3], m, inv), ln_pair(v[4], v[5], m, inv), ln_pair(v[6], v[7], m, inv)};
    asm volatile("" : "+s"(gen));
    STAMP(2);
  } else {
#pragma unroll
    for (int j = WINW; j < 16; ++j) { pace<WINW - 1>(); wv[j] = ld16<NT>(rw, off0, so0 + ((j >> 3) * 32 + (j & 7)) * ldw); PIN(); }
    asm volatile("" : "+s"(gen));
    STAMP(1);
    BAR();
    STAMP(2);
    f32x4 d[2] = {{0.f, 0.f, 0.f, 0.f}, {0.f, 0.f, 0.f, 0.f}};
#pragma unroll
    for (int t = 0; t < 2; ++t) {
#pragma unroll
      for (int q = 0; q < 4; ++q) {
        const v4u act = ACT(L_XN + (n16 & 3) * 512 + (64 * wl + 32 * t + 8 * g) * 2, q);
        d[0] = mfma(act, tr8(wv + 8 * t, 2 * q), d[0]);
        d[1] = mfma(act, tr8(wv + 8 * t, 2 * q + 1), d[1]);
      }
      PIN();
    }
    PUT_RED(d);
    STAMP(3);
    class_bar<1>(ctr + 0, lane);
    if (wl < 2) {
      f32x4 sum = {0.f, 0.f, 0.f, 0.f};
#pragma unroll
      for (int ww = 0; ww < 4; ++ww)
#pragma unroll
        for (int k = 0; k < 4; ++k)
          sum[k] += *(const LAS float*)(L + L_RED + ww * RED_W + (k & 1) * RED_E + (2 * (lane & 1) + (k >> 1)) * RED_G + (wl * 8 + (n16 >> 1)) * 16 + g * 4);
      const int off = (isq ? a_q(g, cb, kq) : a_h(kq, g) + cb * 512) + wl * 256 + n16 * 16;
      st16<SC1 | NT>(__builtin_bit_cast(v4u, sum), rws, ci < 30 ? off : OOB);
    }
    {
      const v4u z = {0u, 0u, 0u, 0u};
      st16<SC1>(z, rws, wl < 2 ? a_o(8 * kvh + 4 * wl + g, ab, sp) + n16 * 16 : a_f(32 * cg + 16 * (wl & 1) + (lane >> 2), hd) + (lane & 3) * 16);
      if (wl == 2) st16<K_MLST>(z, rws, lane < 4 ? a_ml(gidx, sp) + lane * 16 : OOB);
    }
    STAMP(4);
    sm.x = __builtin_amdgcn_raw_buffer_load_b16(mk(p.x, 4 * DM * 2), wl == 2 ? (fb * DM + fc) * 2 : OOB, 0, 0);
    sm.y = __builtin_amdgcn_raw_buffer_load_b16(mk(p.b2, DM * 2), wl == 2 ? fc * 2 : OOB, 0, 0);
    if (K_OPRE == 2) __builtin_amdgcn_raw_buffer_store_b16((unsigned short)0, mk(p.out, 4 * DM * 2), wl == 2 && lane < 16 ? (fb * DM + fc) * 2 : OOB, 0, SC1);
    if (K_OPRE == 3) __builtin_amdgcn_raw_buffer_store_b128(v4u{0u, 0u, 0u, 0u}, mk(p.out, 4 * DM * 2), wl == 2 && lane < 8 && (cu & 3) == 0 ? (cu >> 2) * 128 + lane * 16 : OOB, 0, SC1);
    pace<0>();
    if (lds_arrive(ctr + 1, lane) == 3u) {
      const unsigned long long tg = (gen + 1ull) * 0x9E3779B97F4A7C15ull;
      if (ABST) st8<SC1>(lane == 2 ? v2u{(unsigned)tbase, 0u} : v2u{(unsigned)tg, (unsigned)(tg >> 32)}, rws, lane == 0 ? WS_FLAG + cu * 8 : lane < 3 ? WS_LINE + cu * 128 + lane * 8 - 8 : OOB);
      else st8<SC1>(v2u{(unsigned)tg, (unsigned)(tg >> 32)}, rws, lane == 0 ? WS_FLAG + cu * 8 : lane == 1 ? WS_LINE + cu * 128 : OOB);
    }
    STAMP(5);
  }
  const rsrc_t rk = mk((const char*)p.kc + (size_t)cu * 262144, 262144), rv = mk((const char*)p.vc + (size_t)cu * 262144, 262144);
  const int cbase = (w >> 2) * 131072 + wl * 4096;
  int koff = cbase + ((lane >> 2) & 3) * 256 + g * 64 + (lane & 3) * 16, voff = cbase + lane * 16;
  v4u r[32], fq;
  const int fhd = 8 * kvh + 2 * wl + ((lane >> 2) & 1);
  const int fqo = WS_LINE + ((2 * (lane & 3) + (fhd >> 4)) * 32 + (fhd & 15)) * 128;
  int lag = 0, tab = 0;
#define UPDATE_LAG(t, x) do { lag = now32() - tbase - ((t) + TH1); asm volatile("" : "+s"(lag)); PIN(); } while (0)
  int fq1 = isA && lane < 8 ? fqo : OOB;
  asm volatile("" : "+v"(koff), "+v"(voff), "+v"(fq1));
  asm volatile("" :: "s"(rk), "s"(tab));
  __builtin_amdgcn_s_setprio(1);
  BAR();
  if (K_KPRE && isA) __builtin_amdgcn_s_sleep(K_KPRE);
#pragma unroll
  for (int i = 0; i < 32; ++i) {
    if (i > FLPOS) dyn_pace<1>(lag);
    else if (i >= W0) dyn_pace<0>(lag);
    else if (i > 0) __builtin_amdgcn_s_sleep(RAMP);
    r[i] = ld16<NT>(rk, koff, (i >> 2) * 16384 + (i & 3) * 1024);
    PIN();
    if (i == FLPOS) { fq = ld16<SC1>(rws, fq1, 0); PIN(); }
    if ((i & 3) == 0 && i < 29) UPDATE_LAG(isA ? S2 + T2 * i / 32 : S2 + T2 + T3 * i / 32, i >= FLPOS);
  }
  __builtin_amdgcn_s_setprio(0);
  STAMP(6);
  BAR();
  const unsigned long long tag64 = (gen + 1ull) * 0x9E3779B97F4A7C15ull;
  const unsigned tag0 = (unsigned)tag64, tag1 = (unsigned)(tag64 >> 32);
  if (isA) {
    int spins = 0;
    if (__any(lane < 8 && ((fq.x ^ tag0) | (fq.y ^ tag1)) != 0u)) {
      _Pragma("nounroll") while (true) {
        REREAD();
        fq = ld16<SC1>(rws, fq1, 0);
        if (++spins > SPIN_CAP || !__any(lane < 8 && ((fq.x ^ tag0) | (fq.y ^ tag1)) != 0u)) break;
        __builtin_amdgcn_s_sleep(2);
      }
    }
    REREAD();
    STAMPV(18, spins);
    STAMP(3);
    const int qo = a_q(ab, 8 * kvh + 2 * wl + (lane >> 5), 0) + (lane & 31) * 16;
    v4u qp[4];
#pragma unroll
    for (int s = 0; s < 4; ++s) qp[s] = ld16<SC1>(rws, qo, s * 512);
    PIN();
    if (ABST && w == 0) {
      int d = 0;
#pragma unroll
      for (int k = 0; k < 4; ++k) d += min(max((int)__builtin_amdgcn_readlane(fq.z, k) - tbase, -400), 400);
      if (lane == 0) ctr[12] = tbase + (d >> 2) + (ABST - 100);
    }
    const f32x4 qs = (__builtin_bit_cast(f32x4, qp[0]) + __builtin_bit_cast(f32x4, qp[1])) + (__builtin_bit_cast(f32x4, qp[2]) + __builtin_bit_cast(f32x4, qp[3]));
    *(LAS v2u*)(L + L_Q + (2 * wl + (lane >> 5)) * 256 + (lane & 31) * 8) = v2u{pk(qs[0], qs[1]), pk(qs[2], qs[3])};
    class_bar<1>(ctr + 2, lane);
    STAMP(4);
  }
  if (ABST) tbase = __builtin_amdgcn_readfirstlane(ctr[12]);
  STAMPV(14, ts_[0] + 2 * ((int)ts_[0] - tbase + 200));
  const int hl = n16 & 7, wb = n16 >> 3;
  const v4u qb0 = *(const LAS v4u*)(L + L_Q + hl * 256 + (4 * g + wb) * 16);
  const v4u qb1 = *(const LAS v4u*)(L + L_Q + hl * 256 + (4 * g + 2 + wb) * 16);
  float s[16];
#pragma unroll
  for (int u = 0; u < 8; ++u) {
    float t0[4], t1[4];
#pragma unroll
    for (int k = 0; k < 4; ++k) {
      const f32x4 d0 = mfma(r[4 * u + k], qb0, f32x4{0.f, 0.f, 0.f, 0.f});
      const f32x4 d1 = mfma(r[4 * u + k], qb1, f32x4{0.f, 0.f, 0.f, 0.f});
      t0[k] = d0[0] + d1[2];
      t1[k] = d0[1] + d1[3];
    }
#pragma unroll
    for (int k = 0; k < 4; k += 2) {
      const float xx = wb ? t1[k + 1] : t0[k], yy = wb ? t1[k] : t0[k + 1];
      s[2 * u + (k >> 1)] = xx + ror<8>(yy);
    }
    PIN();
  }
  float m = -3.0e38f;
#pragma unroll
  for (int i = 0; i < 16; i += 2) {
    const unsigned a = pk(s[i], s[i + 1]);
    const unsigned c = pk(blo(a) * 0.08838834764831845f, bhi(a) * 0.08838834764831845f);
    s[i] = blo(c); s[i + 1] = bhi(c);
    m = fmaxf(m, fmaxf(s[i], s[i + 1]));
  }
  m = fmaxf(m, ror<8>(m));
  m = xrow_max(m);
  if (lane < 8) *(LAS float*)(L + L_M + (w * 8 + lane) * 4) = m;
  constexpr float LOG2E = 1.4426950408889634f;
  if (!isA) {
    if (wl == 0) {
      int spins = 0;
      _Pragma("nounroll") while (true) {
        REREAD();
        const v4u a = ld16<SC1>(rws, WS_FLAG + lane * 16, 0), b = ld16<SC1>(rws, WS_FLAG + lane * 16, 1024);
        const unsigned bad = (a.x ^ tag0) | (a.y ^ tag1) | (a.z ^ tag0) | (a.w ^ tag1) | (b.x ^ tag0) | (b.y ^ tag1) | (b.z ^ tag0) | (b.w ^ tag1);
        if (!__any(bad != 0u) || ++spins > SPIN_CAP) break;
        __builtin_amdgcn_s_sleep(4);
      }
      REREAD();
      if (bid == 0) st8<SC1>(v2u{(unsigned)(gen + 1ull), (unsigned)((gen + 1ull) >> 32)}, rws, lane == 0 ? WS_E : OOB);
    }
    class_bar<1>(ctr + 3, lane);
#pragma unroll
    for (int ww = 0; ww < 8; ++ww) m = fmaxf(m, *(const LAS float*)(L + L_M + (ww * 8 + hl) * 4));
  }
  unsigned pb[16];
  float lsum = 0.f;
  {
    const unsigned msk = wb ? 0xffff0000u : 0x0000ffffu;
#pragma unroll
    for (int i = 0; i < 16; ++i) {
      const float e = __builtin_amdgcn_exp2f((s[i] - m) * LOG2E);
      lsum += e;
      const unsigned t = pk(e, e) & msk;
      pb[i] = t | f2u(ror<8>(u2f(t)));
      asm volatile("" : "+v"(pb[i]));
    }
    lsum += ror<8>(lsum);
    lsum = xrow_sum(lsum);
    if (lane < 8) *(LAS float*)(L + L_L + (w * 8 + lane) * 4) = lsum;
  }
  f32x4 acc[8];
#pragma unroll
  for (int j = 0; j < 8; ++j) acc[j] = f32x4{0.f, 0.f, 0.f, 0.f};
  asm volatile("" :: "s"(rv));
  __builtin_amdgcn_s_setprio(1);
  STAMP(7);
  BAR();
  if (K_VPRE && isA) __builtin_amdgcn_s_sleep(K_VPRE);
  STAMP(15);
#define PV(n) acc[(n) & 7] = mfma(tr8(&r[(n) & ~7], (n) & 7), v4u{pb[((n) >> 3) * 4], pb[((n) >> 3) * 4 + 1], pb[((n) >> 3) * 4 + 2], pb[((n) >> 3) * 4 + 3]}, acc[(n) & 7])
#pragma unroll
  for (int i = 0; i < 32; ++i) {
    if (i >= W0) dyn_pace<0>(lag);
    else if (i > 0) __builtin_amdgcn_s_sleep(RAMP);
    r[i] = ld16<NT>(rv, voff, (i >> 2) * 16384 + (i & 3) * 1024);
    PIN();
    if ((i & 3) == 0 && i < 29) UPDATE_LAG(S2 + T2 + T3 + (isA ? T4 * i / 32 : T4 + T5 * i / 32), 0);
    if (i >= 7 + W2) { PV(i - 7 - W2); PIN(); }
  }
  const int mo = a_ml(wl * 4 + (hd >> 3), (n16 + 4 * g) & 15) + (hd & 7) * 8;
  v2u ml = ld8<SC1>(rws, isA || K_MLPOS ? OOB : mo, 0);
  STAMP(16);
  BAR();
#pragma unroll
  for (int n = 32 - 7 - W2; n < 32; ++n) { PV(n); if ((n & 7) == 7) PIN(); }
  STAMP(8);
#define PUT_ACC(f) _Pragma("unroll") for (int i = 0; i < 4; ++i) { \
      const f32x4 t = {wb ? acc[4][i] : acc[0][i], wb ? acc[5][i] : acc[1][i], wb ? acc[6][i] : acc[2][i], wb ? acc[7][i] : acc[3][i]}; \
      *(LAS f32x4*)(L + L_O + wl * O_W + (hl * 132 + 8 * (4 * g + i) + 4 * wb) * 4) = f t; }
  const int oro = ((2 * wl + (g & 1)) * 132 + 8 * n16 + 4 * (g >> 1)) * 4;
  constexpr int S6 = S2 + T2 + T3 + T4 + T5;
  if (isA) {
    __builtin_amdgcn_s_setprio(0);
    float M = m;
#pragma unroll
    for (int ww = 0; ww < 8; ++ww) M = fmaxf(M, *(const LAS float*)(L + L_M + (ww * 8 + hl) * 4));
    const float fa = __builtin_amdgcn_exp2f((m - M) * LOG2E);
    PUT_ACC(fa *);
    if (lane < 8) *(LAS float*)(L + L_L + (w * 8 + lane) * 4) = fa * lsum;
    class_bar<1>(ctr + 4, lane);
    {
      f32x4 t = *(const LAS f32x4*)(L + L_O + oro);
#pragma unroll
      for (int ww = 1; ww < 4; ++ww) t += *(const LAS f32x4*)(L + L_O + ww * O_W + oro);
      *(LAS f32x4*)(L + L_O + 4 * O_W + oro) = t;
    }
    if (w == 0) {
      float l = 0.f;
#pragma unroll
      for (int ww = 0; ww < 8; ++ww) l += *(const LAS float*)(L + L_L + (ww * 8 + hl) * 4);
      if (K_MLST == 16) st8<SC1>(v2u{nz(f2u(M)), nz(f2u(l))}, rws, lane < 8 ? a_ml(gidx, sp) + lane * 8 : OOB);
      else {
        const unsigned a = nz(f2u(M)), b = nz(f2u(l));
        const int src = (lane & 3) * 8;
        st16<K_MLST>(v4u{(unsigned)__builtin_amdgcn_ds_bpermute(src, a), (unsigned)__builtin_amdgcn_ds_bpermute(src, b), (unsigned)__builtin_amdgcn_ds_bpermute(src + 4, a), (unsigned)__builtin_amdgcn_ds_bpermute(src + 4, b)}, rws, lane < 4 ? a_ml(gidx, sp) + lane * 16 : OOB);
      }
    }
    {
      const int hc = lane & 7, hbt = (lane >> 3) & 3, hb = 112 * hd + 28 * wl + 4 * hc;
      const int ho = hc < 7 ? a_h(2 * (lane >> 5), hbt) + hb * 4 : OOB;
      const v4u h0 = ld16<SC1>(rws, ho, 0), h1 = ld16<SC1>(rws, ho, FF * 4);
      const v2u bia = ld8<0>(mk(p.b1, FF * 2), hc < 7 ? hb * 2 : OOB, 0);
      PIN();
      float hv[4];
#pragma unroll
      for (int j = 0; j < 4; ++j) {
        const float t0 = u2f(h0[j]) + u2f(h1[j]);
        auto sw = __builtin_amdgcn_permlane32_swap(f2u(t0), f2u(t0), false, false);
        const float t = u2f(sw[0]) + u2f(sw[1]);
        const float u = rbf(rbf(t) + ((j & 1) ? bhi(bia[j >> 1]) : blo(bia[j >> 1])));
        const float z2 = u * fmaf(u * u, 0.044715f, 1.0f) * (-2.0f * 0.7978845608028654f * LOG2E);
        hv[j] = u * __builtin_amdgcn_rcpf(1.0f + __builtin_amdgcn_exp2f(z2));
      }
      if (lane < 32) *(LAS v2u*)(L + L_H + wl * 256 + hbt * 64 + hc * 8) = v2u{pk(hv[0], hv[1]), pk(hv[2], hv[3])};
    }
    const rsrc_t r2 = mk(p.w2, FF * DM * 2), ro = mk(p.wo, NQ * DM * 2);
    const int o2 = (112 * hd + 28 * wl + 8 * g) * 2048 + cg * 256 + n16 * 16;
    const int o2b = g < 3 ? o2 : OOB;
    const int oo = (128 * hd + 32 * wl + 8 * g) * 2048 + cg * 256 + n16 * 16;
    REREAD();
    v4u hact[4];
#pragma unroll
    for (int q = 0; q < 4; ++q) hact[q] = ACT(L_H + wl * 256 + (n16 & 3) * 64 + g * 16, q);
    int rdo = K_FT ? L_RED + 2 * (lane & 1) * RED_G + (4 * wl + 2 * ((lane >> 4) & 1) + ((lane >> 1) & 1)) * 16 + ((lane >> 2) & 3) * 4
                   : L_RED + (lane & 1) * RED_E + ((lane >> 1) & 3) * RED_G + (4 * wl + ((lane >> 3) & 3)) * 16;
    int fo = lane < 32 ? a_f(32 * cg + 8 * wl + (lane >> 2), hd) + (lane & 3) * 16 : OOB;
    int oa[4];
#pragma unroll
    for (int q = 0; q < 4; ++q) { oa[q] = (n16 >> 2) == q ? L_OA + (n16 & 3) * 256 + (32 * wl + 8 * g) * 2 : L_ZERO; asm volatile("" : "+v"(oa[q])); }
    asm volatile("" : "+v"(rdo), "+v"(fo));
    v4u tw[16];
    asm volatile("" :: "s"(r2), "s"(ro));
    asm volatile("" :: "v"(o2), "v"(o2b), "v"(oo));
    STAMP(5);
    if (K_TLB) {
      const unsigned t2 = __builtin_amdgcn_raw_buffer_load_b32(r2, lane == 0 ? o2 : OOB, 0, NT), to = __builtin_amdgcn_raw_buffer_load_b32(ro, lane == 0 ? oo : OOB, 0, NT);
      asm volatile("" :: "v"(t2), "v"(to));
    }
    BAR();
    if (K_TPRE) __builtin_amdgcn_s_sleep(K_TPRE);
    if (K_TABS > -1000) { _Pragma("nounroll") while (now32() - tbase < S6 + K_TABS) __builtin_amdgcn_s_sleep(1); }
#pragma unroll
    for (int i = 0; i < 16; ++i) {
      const int j = TORD == 0 ? i : TORD == 1 ? i ^ 8 : (i >> 1) + 8 * (i & 1);
      if (TDYN && i >= W0) dyn_pace<0>(lag);
      else if (i >= K_TOPEN) {}
      else if (i >= WINT + 3 * K_TGROW) pace<WINT + 2>();
      else if (i >= WINT + 2 * K_TGROW) pace<WINT + 1>();
      else if (i >= WINT + K_TGROW) pace<WINT>();
      else if (i >= WINT) pace<WINT - 1>();
      else if (i > 0) __builtin_amdgcn_s_sleep(K_TRAMP);
      tw[j] = j < 8 ? ld16<NT>(r2, j < 4 ? o2 : o2b, j * 2048) : ld16<NT>(ro, oo, (j - 8) * 2048);
      PIN();
      if (TDYN && (i & 3) == 0) UPDATE_LAG(S6 + T6 * i / 16, TDYN - 1);
    }
    STAMP(9);
    f32x4 d[2] = {{0.f, 0.f, 0.f, 0.f}, {0.f, 0.f, 0.f, 0.f}};
    v4u wt[8];
    if (TORD != 1) {
#pragma unroll
      for (int j0 = 0; j0 < 8; ++j0) d[j0 & 1] = mfma(hact[j0 >> 1], tr8(tw, j0), d[j0 & 1]);
      PIN();
    }
#pragma unroll
    for (int k = 0; k < 4; ++k) {
#pragma unroll
      for (int j0 = 0; j0 < 8; ++j0) wt[j0][k] = __builtin_amdgcn_perm(tw[9 + 2 * k][j0 >> 1], tw[8 + 2 * k][j0 >> 1], (j0 & 1) ? 0x07060302u : 0x05040100u);
      PIN();
    }
#pragma unroll
    for (int j0 = 0; j0 < 8; ++j0) asm volatile("" : "+v"(wt[j0]));
    if (TORD == 1) {
#pragma unroll
      for (int j0 = 0; j0 < 8; ++j0) d[j0 & 1] = mfma(hact[j0 >> 1], tr8(tw, j0), d[j0 & 1]);
    }
    STAMP(10);
    BAR();
    STAMP(11);
    v4u oact[4];
#pragma unroll
    for (int q = 0; q < 4; ++q) oact[q] = *(const LAS v4u*)(L + oa[q]);
#pragma unroll
    for (int j0 = 0; j0 < 8; ++j0) d[j0 & 1] = mfma(oact[j0 >> 1], wt[j0], d[j0 & 1]);
    PUT_RED(d);
    BAR();
    STAMP(12);
    f32x4 t;
    if (K_FT) {
#pragma unroll
      for (int k = 0; k < 4; ++k) {
        const int o = rdo + (k & 1) * RED_E + (k >> 1) * RED_G;
        t[k] = (*(const LAS float*)(L + o) + *(const LAS float*)(L + o + RED_W)) + (*(const LAS float*)(L + o + 2 * RED_W) + *(const LAS float*)(L + o + 3 * RED_W));
      }
    } else t = (*(const LAS f32x4*)(L + rdo) + *(const LAS f32x4*)(L + rdo + RED_W)) + (*(const LAS f32x4*)(L + rdo + 2 * RED_W) + *(const LAS f32x4*)(L + rdo + 3 * RED_W));
    st16<SC1>(v4u{nz(f2u(t[0])), nz(f2u(t[1])), nz(f2u(t[2])), nz(f2u(t[3]))}, rws, fo);
    STAMP(13);
    asm volatile(".Lfd_code_end:");
  } else {
    PUT_ACC();
    class_bar<0>(ctr + 5, lane);
    STAMP(9);
    {
      f32x4 t = *(const LAS f32x4*)(L + L_O + 4 * O_W + oro);
#pragma unroll
      for (int ww = 0; ww < 4; ++ww) t += *(const LAS f32x4*)(L + L_O + ww * O_W + oro);
      const unsigned a0 = nz(pk(t[0], t[1])), a1 = nz(pk(t[2], t[3]));
      auto s0 = __builtin_amdgcn_permlane32_swap(a0, a0, false, false);
      auto s1 = __builtin_amdgcn_permlane32_swap(a1, a1, false, false);
      st16<SC1>(v4u{s0[0], s1[0], s0[1], s1[1]}, rws, lane < 32 ? a_o(8 * kvh + 2 * wl + g, ab, sp) + n16 * 16 : OOB);
    }
    PIN();
    STAMP(10);
    __builtin_amdgcn_s_setprio(0);
    int spins = 0;
    if (__any(min(ml.x, ml.y) == 0u)) {
      _Pragma("nounroll") while (true) {
        REREAD();
        ml = ld8<SC1>(rws, mo, 0);
        if (!__any(min(ml.x, ml.y) == 0u) || ++spins > SPIN_CAP) break;
        __builtin_amdgcn_s_sleep(2);
      }
    }
    STAMPV(19, spins);
    float M = u2f(ml.x);
    M = fmaxf(M, ror<8>(M)); M = fmaxf(M, ror<4>(M)); M = fmaxf(M, ror<2>(M)); M = fmaxf(M, ror<1>(M));
    const float e = __builtin_amdgcn_exp2f((u2f(ml.x) - M) * LOG2E);
    float Ls = e * u2f(ml.y);
    Ls += ror<8>(Ls); Ls += ror<4>(Ls); Ls += ror<2>(Ls); Ls += ror<1>(Ls);
    const float f = e * __builtin_amdgcn_rcpf(Ls);
    float fk[4] = {dpp<0x150>(f), dpp<0x151>(f), dpp<0x152>(f), dpp<0x153>(f)};
    int wro = L_OA + wl * 256 + n16 * 16 + g * 4, fo = a_f(cu, 0) + lane * 16, oof = lane < 16 ? (fb * DM + fc) * 2 : OOB;
    float res = blo(sm.x) + blo(sm.y);
    asm volatile("" : "+v"(fk[0]), "+v"(fk[1]), "+v"(fk[2]), "+v"(fk[3]), "+v"(wro), "+v"(fo), "+v"(oof), "+v"(res));
    __builtin_amdgcn_s_sleep(ODMIN);
    _Pragma("nounroll") while (now32() - tbase < S6 + TO) __builtin_amdgcn_s_sleep(1);
    const int oo_ = a_o(hd, wl, 4 * g) + n16 * 16;
    v4u op[4];
#pragma unroll
    for (int k = 0; k < 4; ++k) op[k] = ld16<SC1>(rws, oo_, k * 256);
    PIN();
    spins = 0;
    if (__any(min(min(umin4(op[0]), umin4(op[1])), min(umin4(op[2]), umin4(op[3]))) == 0u)) {
      _Pragma("nounroll") while (true) {
        REREAD();
        v4u nw[4];
#pragma unroll
        for (int k = 0; k < 4; ++k) nw[k] = ld16<SC1>(rws, umin4(op[k]) == 0u ? oo_ : OOB, k * 256);
        PIN();
#pragma unroll
        for (int k = 0; k < 4; ++k) op[k] |= nw[k];
        if (++spins > SPIN_CAP || !__any(min(min(umin4(op[0]), umin4(op[1])), min(umin4(op[2]), umin4(op[3]))) == 0u)) break;
        __builtin_amdgcn_s_sleep(1);
      }
    }
    STAMPV(18, spins);
    STAMP(13);
    f32x2 t[4];
#pragma unroll
    for (int j = 0; j < 4; ++j) {
      t[j] = fk[0] * f32x2{blo(op[0][j]), bhi(op[0][j])};
#pragma unroll
      for (int k = 1; k < 4; ++k) t[j] = __builtin_elementwise_fma(f32x2{fk[k], fk[k]}, f32x2{blo(op[k][j]), bhi(op[k][j])}, t[j]);
    }
    float u[4];
#pragma unroll
    for (int j = 0; j < 4; ++j) { auto sw = __builtin_amdgcn_permlane32_swap(f2u(t[j >> 1][j & 1]), f2u(t[2 + (j >> 1)][j & 1]), false, false); u[j] = u2f(sw[0]) + u2f(sw[1]); }
    auto s0 = __builtin_amdgcn_permlane16_swap(f2u(u[0]), f2u(u[2]), false, false);
    auto s1 = __builtin_amdgcn_permlane16_swap(f2u(u[1]), f2u(u[3]), false, false);
    *(LAS unsigned*)(L + wro) = pk(u2f(s0[0]) + u2f(s0[1]), u2f(s1[0]) + u2f(s1[1]));
    BAR();
    BAR();
    if (wl == 2) {
      v4u f0, f1;
      spins = 0;
      __builtin_amdgcn_s_sleep(FDELAY);
      _Pragma("nounroll") while (true) {
        REREAD();
        f0 = ld16<SC1>(rws, fo, 0);
        f1 = ld16<SC1>(rws, fo, 1024);
        if (!__any(min(umin4(f0), umin4(f1)) == 0u) || ++spins > SPIN_CAP) break;
        __builtin_amdgcn_s_sleep(1);
      }
      STAMPV(17, spins);
      STAMP(11);
      const f32x4 y4 = __builtin_bit_cast(f32x4, f0) + __builtin_bit_cast(f32x4, f1);
      const bool h8 = lane & 8, h4 = lane & 4;
      const float a0 = (h8 ? y4[2] : y4[0]) + ror<8>(h8 ? y4[0] : y4[2]), a1 = (h8 ? y4[3] : y4[1]) + ror<8>(h8 ? y4[1] : y4[3]);
      const float y = res + xrow_sum((h4 ? a1 : a0) + dpp<0x1B>(dpp<0x141>(h4 ? a0 : a1)));
      __builtin_amdgcn_raw_buffer_store_b16((unsigned short)pk(y, y), mk(p.out, 4 * DM * 2), oof, 0, K_OAUX);
      STAMP(12);
    }
  }
  STAMP_FLUSH();
}
}

extern "C" void kernel_launch(void* const* d_in, const int* in_sizes, int n_in, void* d_out, int out_size, void* d_ws, size_t ws_size,
                              hipStream_t stream) {
  Params p;
  p.x = d_in[0]; p.kc = d_in[1]; p.vc = d_in[2]; p.wq = d_in[3]; p.wo = d_in[4];
  p.w1 = d_in[6]; p.b1 = d_in[7]; p.w2 = d_in[8]; p.b2 = d_in[9];
  p.out = d_out; p.ws = d_ws;
  hipLaunchKernelGGL(fused_decode, dim3(NWG), dim3(NTHR), 0, stream, p);
}
```

```cpp
#define STAMPS 1
#define DIAG 0
#define DIAGW 4
#define DIAGB -1
#define DIAGS 1
#include <hip/hip_runtime.h>
#include <hip/hip_bf16.h>
#include <stdint.h>

#define KNOB(name, val) constexpr int name = val
#define K_WINW 6
#define K_WINT 5
#define K_W0 4
#define K_W1 5
#define K_W2 7
#define K_TH1 -22
#define K_TH2 20
#define K_SB -10
#define K_S2 235
#define K_T2 465
#define K_T3 450
#define K_T4 440
#define K_T5 445
#define K_T6 215
#define K_TO 225
#define K_ODMIN 4
#define K_FDELAY 18
#define K_RAMP 5
#define K_FLPOS 30
#define K_TORD 4
#define K_TDYN 0
#define K_DL 50
#define K_MLPOS 0
#define K_MLST 16
#define K_TRAMP 3
#define K_OPRE 0
#define K_OAUX 0
#define K_FT 0
#define K_INL 1
#define K_TPRE 6
#define K_TLB 0
#define K_TABS -1000
#define K_KPRE 2
#define K_VPRE 4
#define K_TOPEN 16
#define K_TGROW 99
#define K_SWAP 4
#define K_ANCH 85
#define K_TLB0 0
#define K_FN 0
#define K_TLB2 8
#define K_EFF 0
#define K_MID 12
#define K_QPF 0
#define K_HPF 0
#define K_STLB 1
#define K_RW 3
#define K_RT 1
#define K_TLB1 0
#define K_RO 0
#define K_ANCB 0
#define K_APRIO 1
#define K_PFW K_SWAP
#define K_ABS 100

namespace {
typedef unsigned v4u __attribute__((ext_vector_type(4)));
typedef unsigned v2u __attribute__((ext_vector_type(2)));
typedef float f32x4 __attribute__((ext_vector_type(4)));
typedef short bf16x8 __attribute__((ext_vector_type(8)));
typedef __bf16 bf16x2 __attribute__((ext_vector_type(2)));
typedef float f32x2 __attribute__((ext_vector_type(2)));
typedef __amdgpu_buffer_rsrc_t rsrc_t;
#define GAS __attribute__((address_space(1)))
#define LAS __attribute__((address_space(3)))

constexpr int NWG = 256, NTHR = 512;
constexpr int DM = 1024, NQ = 4096, FF = 3584;
KNOB(WINW, K_WINW); KNOB(WINT, K_WINT); KNOB(W0, K_W0); KNOB(W1, K_W1); KNOB(W2, K_W2); KNOB(TH1, K_TH1); KNOB(TH2, K_TH2);
KNOB(S2, K_S2 + K_SB); KNOB(T2, K_T2); KNOB(T3, K_T3); KNOB(T4, K_T4); KNOB(T5, K_T5); KNOB(T6, K_T6); KNOB(TO, K_TO);
KNOB(ODMIN, K_ODMIN); KNOB(FDELAY, K_FDELAY); KNOB(RAMP, K_RAMP); KNOB(FLPOS, K_FLPOS); KNOB(TORD, K_TORD); KNOB(TDYN, K_TDYN); KNOB(ABST, K_ABS);
constexpr int WS_E = 0;
constexpr int WS_FLAG = 4096;
constexpr int WS_LINE = 8192;
constexpr int WS_ML = WS_LINE + 32768;
constexpr int WS_Q = WS_ML + 16384;
constexpr int WS_H = WS_Q + 262144;
constexpr int WS_O = WS_H + 16 * FF * 4;
constexpr int WS_F = WS_O + 524288;
constexpr int WS_END = WS_F + 524288;
constexpr int WS_STAMP = 8 << 20;
__device__ __forceinline__ int a_ml(int grp, int s) { return WS_ML + (grp * 16 + s) * 64; }
__device__ __forceinline__ int a_q(int b, int head, int kq) { return WS_Q + ((b * 32 + head) * 4 + kq) * 512; }
__device__ __forceinline__ int a_h(int kq, int b) { return WS_H + (((kq >> 1) * 4 + b) * 2 + (kq & 1)) * FF * 4; }
__device__ __forceinline__ int a_o(int head, int b, int s) { return WS_O + ((head * 4 + b) * 16 + s) * 256; }
__device__ __forceinline__ int a_f(int f, int hd) { return WS_F + (f * 32 + hd) * 64; }
constexpr int OOB = 0x7f000000;
constexpr int NT = 2, SC1 = 16;
constexpr int SPIN_CAP = 1 << 18;
constexpr int L_CTR = 0;
constexpr int L_ZERO = 32;
constexpr int L_XN = 64;
constexpr int L_Q = L_XN + 2048;
constexpr int L_M = L_Q + 2048;
constexpr int L_L = L_M + 256;
constexpr int L_RED = L_L + 256;
constexpr int RED_G = 320, RED_E = 4 * RED_G + 32, RED_W = 2 * RED_E;
constexpr int L_O = L_RED + 4 * RED_W;
constexpr int O_W = 8 * 132 * 4;
constexpr int L_H = L_O + 5 * O_W;
constexpr int L_OA = L_H + 1024;
constexpr int L_END = L_OA + 1024;

struct Params {
  const void *wq, *w1, *x, *kc, *vc, *wo, *w2, *b1, *b2;
  void *out, *ws;
};

__device__ __forceinline__ rsrc_t mk(const void* p, int bytes) { return __builtin_amdgcn_make_buffer_rsrc((void*)p, 0, bytes, 0x00020000); }
template <int AUX> __device__ __forceinline__ v4u ld16(rsrc_t r, int voff, int soff) { return __builtin_amdgcn_raw_buffer_load_b128(r, voff, soff, AUX); }
template <int AUX> __device__ __forceinline__ v2u ld8(rsrc_t r, int voff, int soff) { return __builtin_amdgcn_raw_buffer_load_b64(r, voff, soff, AUX); }
template <int AUX> __device__ __forceinline__ void st16(v4u v, rsrc_t r, int voff) { __builtin_amdgcn_raw_buffer_store_b128(v, r, voff, 0, AUX); }
template <int AUX> __device__ __forceinline__ void st8(v2u v, rsrc_t r, int voff) { __builtin_amdgcn_raw_buffer_store_b64(v, r, voff, 0, AUX); }
__device__ __forceinline__ f32x4 mfma(v4u a, v4u b, f32x4 c) {
  return __builtin_amdgcn_mfma_f32_16x16x32_bf16(__builtin_bit_cast(bf16x8, a), __builtin_bit_cast(bf16x8, b), c, 0, 0, 0);
}
__device__ __forceinline__ float u2f(unsigned u) { return __builtin_bit_cast(float, u); }
__device__ __forceinline__ unsigned f2u(float f) { return __builtin_bit_cast(unsigned, f); }
__device__ __forceinline__ unsigned pk(float lo, float hi) {
  f32x2 v = {lo, hi};
  return __builtin_bit_cast(unsigned, __builtin_convertvector(v, bf16x2));
}
__device__ __forceinline__ float blo(unsigned u) { return u2f(u << 16); }
__device__ __forceinline__ float bhi(unsigned u) { return u2f(u & 0xffff0000u); }
__device__ __forceinline__ float rbf(float f) { return blo(pk(f, f)); }
template <int C> __device__ __forceinline__ float dpp(float x) { return u2f(__builtin_amdgcn_mov_dpp(f2u(x), C, 0xf, 0xf, true)); }
template <int N> __device__ __forceinline__ float ror(float x) { return dpp<0x120 + N>(x); }
__device__ __forceinline__ float xrow_sum(float x) {
  auto s = __builtin_amdgcn_permlane16_swap(f2u(x), f2u(x), false, false);
  x = u2f(s[0]) + u2f(s[1]);
  auto t = __builtin_amdgcn_permlane32_swap(f2u(x), f2u(x), false, false);
  return u2f(t[0]) + u2f(t[1]);
}
__device__ __forceinline__ float xrow_max(float x) {
  auto s = __builtin_amdgcn_permlane16_swap(f2u(x), f2u(x), false, false);
  x = fmaxf(u2f(s[0]), u2f(s[1]));
  auto t = __builtin_amdgcn_permlane32_swap(f2u(x), f2u(x), false, false);
  return fmaxf(u2f(t[0]), u2f(t[1]));
}
__device__ __forceinline__ float wave_sum(float x) {
  x += ror<8>(x); x += ror<4>(x); x += ror<2>(x); x += ror<1>(x);
  return xrow_sum(x);
}
__device__ __forceinline__ unsigned umin4(v4u v) { return min(min(v.x, v.y), min(v.z, v.w)); }
__device__ __forceinline__ unsigned nz(unsigned u) { return max(u, 1u); }
#define PIN() __builtin_amdgcn_sched_barrier(0)
#define BAR() do { PIN(); asm volatile("s_waitcnt lgkmcnt(0)\n\ts_barrier" ::: "memory"); PIN(); } while (0)
#define REREAD() asm volatile("" ::: "memory")
__device__ __forceinline__ v4u tr8(const v4u* w, int j0) {
  const unsigned sel = (j0 & 1) ? 0x07060302u : 0x05040100u;
  const int c = j0 >> 1;
  return v4u{__builtin_amdgcn_perm(w[1][c], w[0][c], sel), __builtin_amdgcn_perm(w[3][c], w[2][c], sel),
             __builtin_amdgcn_perm(w[5][c], w[4][c], sel), __builtin_amdgcn_perm(w[7][c], w[6][c], sel)};
}
__device__ __forceinline__ void ln_stats(const float (&v)[16], float& m, float& inv) {
  float s = 0.f;
#pragma unroll
  for (int i = 0; i < 16; ++i) s += v[i];
  const float mf = wave_sum(s) * (1.0f / DM);
  float ss = 0.f;
#pragma unroll
  for (int i = 0; i < 16; ++i) { const float d = v[i] - mf; ss = fmaf(d, d, ss); }
  const float var = rbf(wave_sum(ss) * (1.0f / DM));
  m = rbf(mf);
  inv = __builtin_amdgcn_rcpf(rbf(__builtin_amdgcn_sqrtf(rbf(var + 1e-5f))));
}
__device__ __forceinline__ unsigned ln_pair(float a, float b, float m, float inv) {
  const unsigned d = pk(a - m, b - m);
  return pk(blo(d) * inv, bhi(d) * inv);
}
__device__ __forceinline__ unsigned lds_arrive(LAS unsigned* c, int lane) {
  unsigned old = 0;
  REREAD();
  if (lane == 0) old = __hip_atomic_fetch_add(c, 1u, __ATOMIC_RELAXED, __HIP_MEMORY_SCOPE_WORKGROUP);
  return __builtin_amdgcn_readfirstlane(old);
}
template <int SL> __device__ __forceinline__ void class_bar(LAS unsigned* c, int lane) {
  PIN();
  if (lds_arrive(c, lane) != 3u) {
    _Pragma("nounroll") while (__builtin_amdgcn_readfirstlane(__hip_atomic_load(c, __ATOMIC_RELAXED, __HIP_MEMORY_SCOPE_WORKGROUP)) < 4u)
      __builtin_amdgcn_s_sleep(SL);
  }
  REREAD();
  PIN();
}
template <int N> __device__ __forceinline__ void pace() { asm volatile("s_waitcnt vmcnt(%0)" :: "n"(N) : "memory"); }
template <int X> __device__ __forceinline__ void dyn_pace(int lag) {
  asm volatile("s_cmp_gt_i32 %0, %1\n\ts_cbranch_scc1 1f\n\ts_waitcnt vmcnt(%3)\n1:\n\ts_cmp_gt_i32 %0, %2\n\ts_cbranch_scc1 2f\n\ts_waitcnt vmcnt(%4)\n2:\n\ts_waitcnt vmcnt(%5)"
               :: "s"(lag), "n"(0), "n"(TH2 - TH1), "n"(W0 - 1 + X), "n"(W1 - 1 + X), "n"(W2 - 1 + X) : "memory", "scc");
}
template <int X> __device__ __forceinline__ void dyn_pace(unsigned long long tgt) { asm volatile("s_swappc_b64 s[98:99], %0" :: "s"(tgt) : "memory", "s98", "s99"); }
__device__ __forceinline__ unsigned touch(const void* base, int off) {
  asm volatile("" : "+s"(off));
  return *(const __attribute__((address_space(4))) unsigned*)((unsigned long long)base + (unsigned)off);
}
__device__ __forceinline__ int now32() { return (int)(unsigned)__builtin_amdgcn_s_memrealtime(); }

#define STAMP(i) do { PIN(); ts_[i] = (unsigned)__builtin_amdgcn_s_memrealtime(); PIN(); } while (0)
#define STAMPV(i, v) do { ts_[i] = (unsigned)(v); } while (0)
#define STAMP_FLUSH() do { if (lane == 0) { _Pragma("unroll") for (int i_ = 0; i_ < 20; ++i_) ((GAS unsigned long long*)((GAS char*)p.ws + WS_STAMP))[(bid * 8 + w) * 20 + i_] = ts_[i_]; } } while (0)
#undef STAMP_FLUSH
#define STAMP_FLUSH() do { if (w == DIAGW && (DIAGB < 0 ? ci < 30 : bid == DIAGB)) { const unsigned lim_ = DIAGS > 0 ? 6000u + 8u * (ts_[DIAG] - ts_[0]) : 30000u - 8u * (ts_[DIAG] - ts_[0]); \
    while ((unsigned)__builtin_amdgcn_s_memrealtime() - ts_[0] < lim_ && lim_ < 100000u) __builtin_amdgcn_s_sleep(1); } } while (0)

__global__ void __launch_bounds__(NTHR, 2) fused_decode(Params) {
  __shared__ __attribute__((aligned(16))) unsigned char smem[L_END];
  LAS unsigned char* L = (LAS unsigned char*)smem;
  LAS unsigned* ctr = (LAS unsigned*)(L + L_CTR);
  const int tid = threadIdx.x, lane = tid & 63;
  int w = __builtin_amdgcn_readfirstlane(tid >> 6) ^ K_SWAP, bid = blockIdx.x;
  typedef unsigned long long s8p __attribute__((ext_vector_type(8)));
  s8p ka;
  unsigned long long wp, kws, t64, pc;
  int ldw, wbytes, so0, so1, mg, ta;
  asm volatile(
      "s_load_dwordx16 %[ka], %[kp], 0x10\n\t"
      "s_lshr_b32 %[ta], %[bid], 7\n\t"
      "s_lshl_b32 %[ta], %[ta], 3\n\t"
      "s_cmp_lt_u32 %[w], 4\n\t"
      "s_cselect_b32 %[ta], 16, %[ta]\n\t"
      "s_load_dwordx2 %[wp], %[kp], %[ta]\n\t"
      "s_load_dwordx2 %[kws], %[kp], 0x50\n\t"
      "s_memrealtime %[t]\n\t"
      "s_getpc_b64 %[pc]\n"
      ".Lpf_base:\n\t"
      "s_cmp_lg_u32 %[w], %[pw]\n\t"
      "s_cbranch_scc1 1f\n\t"
      "v_mbcnt_lo_u32_b32 v170, -1, 0\n\t"
      "s_lshr_b32 %[mg], %[bid], 3\n\t"
      "s_lshl_b32 %[mg], %[mg], 7\n\t"
      "v_lshl_add_u32 v170, v170, 12, %[mg]\n\t"
      "v_min_u32 v170, .Lfd_code_end-.Lpf_base-4, v170\n\t"
      "global_load_dword v170, v170, %[pc]\n"
      "1:\n\t"
      "s_cmp_lt_u32 %[bid], 0x80\n\t"
      "s_movk_i32 %[ldw], 0x1c00\n\t"
      "s_cselect_b32 %[ldw], 0x2000, %[ldw]\n\t"
      "s_cselect_b32 %[so1], 16, 14\n\t"
      "s_bitcmp1_b32 %[bid], 0\n\t"
      "s_cselect_b32 %[so1], %[so1], 0\n\t"
      "s_bfe_u32 %[so0], %[bid], 0x40003\n\t"
      "s_add_u32 %[so1], %[so1], %[so0]\n\t"
      "s_lshl_b32 %[so1], %[so1], 8\n\t"
      "s_bfe_u32 %[so0], %[bid], 0x20001\n\t"
      "s_lshl_b32 %[so0], %[so0], %[s1]\n\t"
      "s_and_b32 %[mg], %[w], 3\n\t"
      "s_add_u32 %[so0], %[so0], %[mg]\n\t"
      "s_lshl_b32 %[so0], %[so0], %[s2]\n\t"
      "s_mul_i32 %[so0], %[so0], %[ldw]\n\t"
      "s_add_u32 %[so0], %[so0], %[so1]\n\t"
      "s_lshl_b32 %[wb], %[ldw], 10\n\t"
      "s_cmp_lt_u32 %[bid], 0xf0\n\t"
      "s_cselect_b32 %[wb], %[wb], 0\n\t"
      "s_lshl_b32 %[mg], %[ldw], %[sh]\n\t"
      "s_cmp_gt_u32 %[w], 3\n\t"
      "s_cbranch_scc1 2f\n\t"
      "s_bfe_u32 %[so0], %[bid], 0x10002\n\t"
      "s_lshl_b32 %[so1], %[w], 1\n\t"
      "s_add_u32 %[so0], %[so0], %[so1]\n\t"
      "s_lshl_b32 %[so0], %[so0], 10\n\t"
      "s_xor_b32 %[so1], %[so0], 0x400\n\t"
      "s_setprio %[ap]\n\t"
      "s_movk_i32 %[wb], 0x2000\n\t"
      "s_movk_i32 %[mg], 0x100\n"
      "2:\n\t"
      "s_waitcnt lgkmcnt(0)"
      : [ka] "=&s"(ka), [wp] "=&s"(wp), [kws] "=&s"(kws), [t] "=&s"(t64), [pc] "=&s"(pc), [ldw] "=&s"(ldw), [wb] "=&s"(wbytes),
        [so0] "=&s"(so0), [so1] "=&s"(so1), [mg] "=&s"(mg), [ta] "=&s"(ta), [w] "+s"(w), [bid] "+s"(bid)
      : [kp] "s"(__builtin_amdgcn_kernarg_segment_ptr()), [pw] "n"(K_PFW), [ap] "n"(K_APRIO), [sh] "n"(K_RW == 1 ? 0 : K_RW == 2 ? 4 : K_RW == 3 ? 6 : 3), [s1] "n"(K_RW == 3 ? 4 : 2), [s2] "n"(K_RW == 3 ? 4 : 6)
      : "scc", "v170", "memory");
  const int wl = w & 3;
  const bool isA = w < 4;
  const int xs = bid & 7, ci = bid >> 3;
  const int n16 = lane & 15, g = lane >> 4;
  const int kq = xs >> 1, isq = ci < 16, cb = (isq ? 16 : 14) * (xs & 1) + (ci & 15);
  const int off0 = __umul24(g, mg) + n16 * 16;
  unsigned ts_[20] = {(unsigned)t64, 0, 0, 0, 0, 0, 0, 0, 0, 0, 0, 0, 0, 0, 0, 0, 0, 0, 0, 0};
  STAMP(14);
  int tbase = (int)(unsigned)t64;
  v4u wv[16];
  const rsrc_t rw = mk((const void*)wp, wbytes);
  v4u xr[2];
  unsigned tpf = 0;
  if (isA) {
    xr[0] = ld16<0>(rw, off0, so0);
    PIN();
    xr[1] = ld16<0>(rw, off0, so1);
    if (K_TLB0) {
      PIN();
      const unsigned long long tb = wl == 0 ? ka[1] : wl == 1 ? ka[2] : wl == 2 ? ka[3] : ka[4];
      const int to = wl < 2 ? (xs * 32 + ci) * 262144 : (4 * xs + (ci >> 3)) * (wl == 2 ? 262144 : 229376);
      tpf = __builtin_amdgcn_raw_buffer_load_b32(mk((const void*)tb, to + 4), lane * 4, to, NT);
    }
  }
  int w2 = w;
  asm volatile("" : "+s"(w2));
  PIN();
  if (w2 >= 4) {
#pragma unroll
    for (int j = 0; j < WINW; ++j) { wv[j] = ld16<NT>(rw, off0, so0 + (K_RW == 1 ? 4 * j : K_RW ? j : (j >> 3) * 32 + (j & 7)) * ldw); PIN(); }
  }
  PIN();
  const struct { const void *x, *kc, *vc, *wo, *w2, *b1, *b2, *out, *ws; } p = {(const void*)ka[0], (const void*)ka[1], (const void*)ka[2], (const void*)ka[3],
      (const void*)ka[4], (const void*)ka[5], (const void*)ka[6], (const void*)ka[7], (const void*)kws};
  const rsrc_t rws = mk(p.ws, WS_END);
  const int gidx = 2 * xs + (ci >> 4), ab = gidx >> 2, kvh = gidx & 3, sp = ci & 15;
  const int hd = 4 * xs + (ci >> 3), cg = ci & 7;
  const int cu = xs * 32 + ci;
  unsigned long long gen = *(const __attribute__((address_space(4))) unsigned long long*)kws;
  const int fb = K_FT ? cu & 3 : (lane >> 2) & 3, fc = K_FT ? 16 * (cu >> 2) + 4 * (lane & 3) + ((lane >> 2) & 3) : 4 * cu + (lane & 3);
  v2u sm;
#define ACT(base, q) (*(const LAS v4u*)(L + ((n16 >> 2) == (q) ? (base) : L_ZERO)))
#define PUT_RED(d) do { *(LAS f32x4*)(L + L_RED + wl * RED_W + g * RED_G + n16 * 16) = d[0]; \
                        *(LAS f32x4*)(L + L_RED + wl * RED_W + RED_E + g * RED_G + n16 * 16) = d[1]; } while (0)

  if (isA) {
    if (w == 0 && lane < 12) ctr[lane] = 0u;
    float v[16];
#pragma unroll
    for (int i = 0; i < 8; ++i) { v[2 * i] = blo(xr[i >> 2][i & 3]); v[2 * i + 1] = bhi(xr[i >> 2][i & 3]); }
    asm volatile("" : "+v"(v[0]), "+v"(v[15]));
    if (K_ANCH && !K_ANCB) {
      PIN();
      tbase = now32() - K_ANCH;
      if (w == 0 && lane == 0) { ctr[13] = tbase; ctr[14] = -TH1; }
    }
    STAMP(1);
    float m, inv;
    ln_stats(v, m, inv);
    if (!isq) {
#pragma unroll
      for (int i = 0; i < 16; i += 2) { const unsigned o = ln_pair(v[i], v[i + 1], m, inv); v[i] = blo(o); v[i + 1] = bhi(o); }
      ln_stats(v, m, inv);
    }
    if (K_RW == 1) {
      if ((lane >> 5) == (kq & 1)) {
#pragma unroll
        for (int k = 0; k < 4; ++k) *(LAS unsigned*)(L + L_XN + wl * 512 + ((lane & 31) >> 2) * 64 + k * 16 + (lane & 3) * 4) = ln_pair(v[k], v[4 + k], m, inv);
      }
    } else if ((lane >> 5) == (kq & 1))
      *(LAS v4u*)(L + L_XN + wl * 512 + (lane & 31) * 16) = v4u{ln_pair(v[0], v[1], m, inv), ln_pair(v[2], v[3], m, inv), ln_pair(v[4], v[5], m, inv), ln_pair(v[6], v[7], m, inv)};
    if (K_TLB1 && (ci & (K_TLB1 - 1)) == 0) {
      const void* tb = wl == 0 ? p.kc : wl == 1 ? p.vc : wl == 2 ? p.wo : p.w2;
      const void* tc = wl == 0 ? p.b1 : wl == 1 ? p.b2 : p.out;
      const unsigned t0 = touch(tb, wl < 2 ? cu * 262144 : hd * (wl == 2 ? 262144 : 229376)), t1 = touch(tc, 0);
      asm volatile("" :: "s"(t0), "s"(t1));
    }
    asm volatile("" : "+s"(gen));
    if (K_TLB0) asm volatile("" :: "v"(tpf));
    STAMP(2);
  } else {
#pragma unroll
    for (int j = WINW; j < 16; ++j) {
      pace<WINW - 1>(); wv[j] = ld16<NT>(rw, off0, so0 + (K_RW == 1 ? 4 * j : K_RW ? j : (j >> 3) * 32 + (j & 7)) * ldw); PIN();
      if (j == WINW) STAMP(12);
      if (K_ANCB && j == WINW && w == 4) { tbase = now32() - K_ANCH; if (lane == 0) { ctr[13] = tbase; ctr[14] = -TH1; } PIN(); }
    }
    asm volatile("" : "+s"(gen));
    STAMP(1);
    BAR();
    if (K_ANCH) tbase = __builtin_amdgcn_readfirstlane(ctr[13]);
    STAMP(2);
    f32x4 d[2] = {{0.f, 0.f, 0.f, 0.f}, {0.f, 0.f, 0.f, 0.f}};
#pragma unroll
    for (int t = 0; t < 2; ++t) {
#pragma unroll
      for (int q = 0; q < 4; ++q) {
        const v4u act = ACT(L_XN + (n16 & 3) * 512 + (K_RW == 2 ? 64 * wl + 16 * g + 8 * t : K_RW == 3 ? 64 * g + 16 * wl + 8 * t : 64 * wl + 32 * t + 8 * g) * 2, q);
        d[0] = mfma(act, tr8(wv + 8 * t, 2 * q), d[0]);
        d[1] = mfma(act, tr8(wv + 8 * t, 2 * q + 1), d[1]);
      }
      PIN();
    }
    PUT_RED(d);
    STAMP(3);
    class_bar<1>(ctr + 0, lane);
    if (wl < 2) {
      f32x4 sum = {0.f, 0.f, 0.f, 0.f};
#pragma unroll
      for (int ww = 0; ww < 4; ++ww)
#pragma unroll
        for (int k = 0; k < 4; ++k)
          sum[k] += *(const LAS float*)(L + L_RED + ww * RED_W + (k & 1) * RED_E + (2 * (lane & 1) + (k >> 1)) * RED_G + (wl * 8 + (n16 >> 1)) * 16 + g * 4);
      const int off = (isq ? a_q(g, cb, kq) : a_h(kq, g) + cb * 512) + wl * 256 + n16 * 16;
      st16<SC1 | NT>(__builtin_bit_cast(v4u, sum), rws, ci < 30 ? off : OOB);
    }
    {
      const v4u z = {0u, 0u, 0u, 0u};
      st16<SC1>(z, rws, wl < 2 ? a_o(8 * kvh + 4 * wl + g, ab, sp) + n16 * 16 : a_f(32 * cg + 16 * (wl & 1) + (lane >> 2), hd) + (lane & 3) * 16);
      if (wl == 2) st16<K_MLST>(z, rws, lane < 4 ? a_ml(gidx, sp) + lane * 16 : OOB);
    }
    STAMP(4);
    sm.x = __builtin_amdgcn_raw_buffer_load_b16(mk(p.x, 4 * DM * 2), wl == 2 ? (fb * DM + fc) * 2 : OOB, 0, 0);
    sm.y = __builtin_amdgcn_raw_buffer_load_b16(mk(p.b2, DM * 2), wl == 2 ? fc * 2 : OOB, 0, 0);
    if (K_OPRE == 2) __builtin_amdgcn_raw_buffer_store_b16((unsigned short)0, mk(p.out, 4 * DM * 2), wl == 2 && lane < 16 ? (fb * DM + fc) * 2 : OOB, 0, SC1);
    if (K_OPRE == 3) __builtin_amdgcn_raw_buffer_store_b128(v4u{0u, 0u, 0u, 0u}, mk(p.out, 4 * DM * 2), wl == 2 && lane < 8 && (cu & 3) == 0 ? (cu >> 2) * 128 + lane * 16 : OOB, 0, SC1);
    if (K_TLB2) {
      const void* tb = wl == 0 ? p.vc : wl == 1 ? p.wo : wl == 2 ? p.w2 : p.b1;
      const int to = wl == 0 ? cu * 262144 : wl == 1 ? hd * 262144 : wl == 2 ? hd * 229376 : 0;
      if (K_STLB) {
        if ((ci & (K_TLB2 - 1)) == 0) { const unsigned tp = touch(tb, to); asm volatile("" :: "s"(tp)); }
      } else {
        const unsigned tp = __builtin_amdgcn_raw_buffer_load_b32(mk(tb, (ci & (K_TLB2 - 1)) == 0 ? to + 4 : 0), lane * 4, to, NT);
        asm volatile("" :: "v"(tp));
      }
    }
    pace<0>();
    if (lds_arrive(ctr + 1, lane) == 3u) {
      const unsigned long long tg = (gen + 1ull) * 0x9E3779B97F4A7C15ull;
      if (ABST) st8<SC1>(lane == 2 ? v2u{(unsigned)(K_EFF > -1000 ? tbase + (int)ctr[14] + TH1 + K_EFF : K_FN ? now32() - K_FN : tbase), 0u} : v2u{(unsigned)tg, (unsigned)(tg >> 32)}, rws, lane == 0 ? WS_FLAG + cu * 8 : lane < 3 ? WS_LINE + cu * 128 + lane * 8 - 8 : OOB);
      else st8<SC1>(v2u{(unsigned)tg, (unsigned)(tg >> 32)}, rws, lane == 0 ? WS_FLAG + cu * 8 : lane == 1 ? WS_LINE + cu * 128 : OOB);
    }
    STAMP(5);
  }
  const rsrc_t rk = mk((const char*)p.kc + (size_t)cu * 262144, 262144), rv = mk((const char*)p.vc + (size_t)cu * 262144, 262144);
  const int cbase = (w >> 2) * 131072 + wl * 4096;
  int koff = cbase + ((lane >> 2) & 3) * 256 + g * 64 + (lane & 3) * 16, voff = cbase + lane * 16;
  v4u r[32], fq;
  const int fhd = 8 * kvh + 2 * wl + ((lane >> 2) & 1);
  const int fqo = WS_LINE + ((2 * (lane & 3) + (fhd >> 4)) * 32 + (fhd & 15)) * 128;
  int lag = 0, tab = 0;
#define UPDATE_LAG(t, x) do { lag = now32() - tbase - ((t) + TH1); asm volatile("" : "+s"(lag)); PIN(); } while (0)
  int plag = 0, adj = 0;
  int fq1 = isA && lane < 8 ? fqo : OOB;
  asm volatile("" : "+v"(koff), "+v"(voff), "+v"(fq1));
  asm volatile("" :: "s"(rk), "s"(tab));
  __builtin_amdgcn_s_setprio(1);
  BAR();
  if (K_KPRE && isA) __builtin_amdgcn_s_sleep(K_KPRE);
#pragma unroll
  for (int i = 0; i < 32; ++i) {
    if (i > FLPOS) dyn_pace<1>(lag);
    else if (i >= W0) dyn_pace<0>(lag);
    else if (i > 0) __builtin_amdgcn_s_sleep(RAMP);
    r[i] = ld16<NT>(rk, koff, (i >> 2) * 16384 + (i & 3) * 1024);
    PIN();
    if (i == FLPOS) { fq = ld16<SC1>(rws, fq1, 0); PIN(); }
    if (K_ANCB && i == 0) { REREAD(); tbase = __builtin_amdgcn_readfirstlane(ctr[13]); }
    if (i == K_MID) {
      REREAD();
      const int a = __builtin_amdgcn_readfirstlane((int)ctr[7]);
      adj = (a >> 24) == 32 ? ((a & 0xffffff) >> 5) - 150 + (ABST - 100) : 0;
    }
    if ((i & 3) == 0 && i < 29) UPDATE_LAG(adj + (isA ? S2 + T2 * i / 32 : S2 + T2 + T3 * i / 32), i >= FLPOS);
    if (K_EFF > -1000 && (i & 3) == 0 && i >= 8 && i <= 24) {
      if (w == 0) ctr[14] = lag + (plag > TH2 - TH1 ? (W2 - W1) * 13 : plag > 0 ? 0 : (W0 - W1) * 13);
      PIN();
    }
    if ((i & 3) == 0) plag = lag;
  }
  __builtin_amdgcn_s_setprio(0);
  STAMP(6);
  BAR();
  const unsigned long long tag64 = (gen + 1ull) * 0x9E3779B97F4A7C15ull;
  const unsigned tag0 = (unsigned)tag64, tag1 = (unsigned)(tag64 >> 32);
  if (isA) {
    int spins = 0;
    if (__any(lane < 8 && ((fq.x ^ tag0) | (fq.y ^ tag1)) != 0u)) {
      _Pragma("nounroll") while (true) {
        REREAD();
        fq = ld16<SC1>(rws, fq1, 0);
        if (++spins > SPIN_CAP || !__any(lane < 8 && ((fq.x ^ tag0) | (fq.y ^ tag1)) != 0u)) break;
        __builtin_amdgcn_s_sleep(2);
      }
    }
    REREAD();
    STAMPV(18, spins);
    STAMP(3);
    if (ABST) {
      if (K_ANCH) tbase = __builtin_amdgcn_readfirstlane(ctr[13]);
      if (lane < 8) __hip_atomic_fetch_add(ctr + 7, (unsigned)(min(max((int)fq.z - tbase, -150), 150) + 150 + (1 << 24)), __ATOMIC_RELAXED, __HIP_MEMORY_SCOPE_WORKGROUP);
    }
    if (K_QPF) {
      const int o = a_q(ab, 8 * kvh + 2 * wl, 0) + sp * 256;
      const unsigned t0 = touch(p.ws, o), t1 = touch(p.ws, o + 128);
      asm volatile("" :: "s"(t0), "s"(t1));
      __builtin_amdgcn_s_sleep(K_QPF - 1);
    }
    const int qo = a_q(ab, 8 * kvh + 2 * wl + (lane >> 5), 0) + (lane & 31) * 16;
    v4u qp[4];
#pragma unroll
    for (int s = 0; s < 4; ++s) qp[s] = ld16<SC1>(rws, qo, s * 512);
    PIN();
    const f32x4 qs = (__builtin_bit_cast(f32x4, qp[0]) + __builtin_bit_cast(f32x4, qp[1])) + (__builtin_bit_cast(f32x4, qp[2]) + __builtin_bit_cast(f32x4, qp[3]));
    *(LAS v2u*)(L + L_Q + (2 * wl + (lane >> 5)) * 256 + (lane & 31) * 8) = v2u{pk(qs[0], qs[1]), pk(qs[2], qs[3])};
    class_bar<1>(ctr + 2, lane);
    STAMP(4);
  }
  if (ABST) tbase += ((__builtin_amdgcn_readfirstlane((int)ctr[7]) & 0xffffff) >> 5) - 150 + (ABST - 100);
  const int hl = n16 & 7, wb = n16 >> 3;
  const v4u qb0 = *(const LAS v4u*)(L + L_Q + hl * 256 + (4 * g + wb) * 16);
  const v4u qb1 = *(const LAS v4u*)(L + L_Q + hl * 256 + (4 * g + 2 + wb) * 16);
  float s[16];
#pragma unroll
  for (int u = 0; u < 8; ++u) {
    float t0[4], t1[4];
#pragma unroll
    for (int k = 0; k < 4; ++k) {
      const f32x4 d0 = mfma(r[4 * u + k], qb0, f32x4{0.f, 0.f, 0.f, 0.f});
      const f32x4 d1 = mfma(r[4 * u + k], qb1, f32x4{0.f, 0.f, 0.f, 0.f});
      t0[k] = d0[0] + d1[2];
      t1[k] = d0[1] + d1[3];
    }
#pragma unroll
    for (int k = 0; k < 4; k += 2) {
      const float xx = wb ? t1[k + 1] : t0[k], yy = wb ? t1[k] : t0[k + 1];
      s[2 * u + (k >> 1)] = xx + ror<8>(yy);
    }
    PIN();
  }
  float m = -3.0e38f;
#pragma unroll
  for (int i = 0; i < 16; i += 2) {
    const unsigned a = pk(s[i], s[i + 1]);
    const unsigned c = pk(blo(a) * 0.08838834764831845f, bhi(a) * 0.08838834764831845f);
    s[i] = blo(c); s[i + 1] = bhi(c);
    m = fmaxf(m, fmaxf(s[i], s[i + 1]));
  }
  m = fmaxf(m, ror<8>(m));
  m = xrow_max(m);
  if (lane < 8) *(LAS float*)(L + L_M + (w * 8 + lane) * 4) = m;
  constexpr float LOG2E = 1.4426950408889634f;
  if (!isA) {
    if (wl == 0) {
      int spins = 0;
      _Pragma("nounroll") while (true) {
        REREAD();
        const v4u a = ld16<SC1>(rws, WS_FLAG + lane * 16, 0), b = ld16<SC1>(rws, WS_FLAG + lane * 16, 1024);
        const unsigned bad = (a.x ^ tag0) | (a.y ^ tag1) | (a.z ^ tag0) | (a.w ^ tag1) | (b.x ^ tag0) | (b.y ^ tag1) | (b.z ^ tag0) | (b.w ^ tag1);
        if (!__any(bad != 0u) || ++spins > SPIN_CAP) break;
        __builtin_amdgcn_s_sleep(4);
      }
      REREAD();
      if (bid == 0) st8<SC1>(v2u{(unsigned)(gen + 1ull), (unsigned)((gen + 1ull) >> 32)}, rws, lane == 0 ? WS_E : OOB);
    }
    class_bar<1>(ctr + 3, lane);
    if (K_HPF) {
      const int o = a_h(cg >> 1, 2 * (cg & 1) + (wl >> 1)) + 448 * hd + (wl & 1) * 224;
      const unsigned t0 = touch(p.ws, o), t1 = touch(p.ws, o + 112), t2 = touch(p.ws, o + 220), t3 = touch(p.b1, 224 * hd + (wl & 1) * 112 + (wl >> 1) * 108);
      asm volatile("" :: "s"(t0), "s"(t1), "s"(t2), "s"(t3));
    }
#pragma unroll
    for (int ww = 0; ww < 8; ++ww) m = fmaxf(m, *(const LAS float*)(L + L_M + (ww * 8 + hl) * 4));
  }
  unsigned pb[16];
  float lsum = 0.f;
  {
    const unsigned msk = wb ? 0xffff0000u : 0x0000ffffu;
#pragma unroll
    for (int i = 0; i < 16; ++i) {
      const float e = __builtin_amdgcn_exp2f((s[i] - m) * LOG2E);
      lsum += e;
      const unsigned t = pk(e, e) & msk;
      pb[i] = t | f2u(ror<8>(u2f(t)));
      asm volatile("" : "+v"(pb[i]));
    }
    lsum += ror<8>(lsum);
    lsum = xrow_sum(lsum);
    if (lane < 8) *(LAS float*)(L + L_L + (w * 8 + lane) * 4) = lsum;
  }
  f32x4 acc[8];
#pragma unroll
  for (int j = 0; j < 8; ++j) acc[j] = f32x4{0.f, 0.f, 0.f, 0.f};
  asm volatile("" :: "s"(rv));
  __builtin_amdgcn_s_setprio(1);
  STAMP(7);
  BAR();
  if (K_VPRE && isA) __builtin_amdgcn_s_sleep(K_VPRE);
  STAMP(15);
#define PV(n) acc[(n) & 7] = mfma(tr8(&r[(n) & ~7], (n) & 7), v4u{pb[((n) >> 3) * 4], pb[((n) >> 3) * 4 + 1], pb[((n) >> 3) * 4 + 2], pb[((n) >> 3) * 4 + 3]}, acc[(n) & 7])
#pragma unroll
  for (int i = 0; i < 32; ++i) {
    if (i >= W0) dyn_pace<0>(lag);
    else if (i > 0) __builtin_amdgcn_s_sleep(RAMP);
    r[i] = ld16<NT>(rv, voff, (i >> 2) * 16384 + (i & 3) * 1024);
    PIN();
    if ((i & 3) == 0 && i < 29) UPDATE_LAG(S2 + T2 + T3 + (isA ? T4 * i / 32 : T4 + T5 * i / 32), 0);
    if (i >= 7 + W2) { PV(i - 7 - W2); PIN(); }
  }
  const int mo = a_ml(wl * 4 + (hd >> 3), (n16 + 4 * g) & 15) + (hd & 7) * 8;
  v2u ml = ld8<SC1>(rws, isA || K_MLPOS ? OOB : mo, 0);
  STAMP(16);
  BAR();
#pragma unroll
  for (int n = 32 - 7 - W2; n < 32; ++n) { PV(n); if ((n & 7) == 7) PIN(); }
  STAMP(8);
#define PUT_ACC(f) _Pragma("unroll") for (int i = 0; i < 4; ++i) { \
      const f32x4 t = {wb ? acc[4][i] : acc[0][i], wb ? acc[5][i] : acc[1][i], wb ? acc[6][i] : acc[2][i], wb ? acc[7][i] : acc[3][i]}; \
      *(LAS f32x4*)(L + L_O + wl * O_W + (hl * 132 + 8 * (4 * g + i) + 4 * wb) * 4) = f t; }
  const int oro = ((2 * wl + (g & 1)) * 132 + 8 * n16 + 4 * (g >> 1)) * 4;
  constexpr int S6 = S2 + T2 + T3 + T4 + T5;
  if (isA) {
    __builtin_amdgcn_s_setprio(0);
    float M = m;
#pragma unroll
    for (int ww = 0; ww < 8; ++ww) M = fmaxf(M, *(const LAS float*)(L + L_M + (ww * 8 + hl) * 4));
    const float fa = __builtin_amdgcn_exp2f((m - M) * LOG2E);
    PUT_ACC(fa *);
    if (lane < 8) *(LAS float*)(L + L_L + (w * 8 + lane) * 4) = fa * lsum;
    class_bar<1>(ctr + 4, lane);
    {
      f32x4 t = *(const LAS f32x4*)(L + L_O + oro);
#pragma unroll
      for (int ww = 1; ww < 4; ++ww) t += *(const LAS f32x4*)(L + L_O + ww * O_W + oro);
      *(LAS f32x4*)(L + L_O + 4 * O_W + oro) = t;
    }
    if (w == 0) {
      float l = 0.f;
#pragma unroll
      for (int ww = 0; ww < 8; ++ww) l += *(const LAS float*)(L + L_L + (ww * 8 + hl) * 4);
      if (K_MLST == 16) st8<SC1>(v2u{nz(f2u(M)), nz(f2u(l))}, rws, lane < 8 ? a_ml(gidx, sp) + lane * 8 : OOB);
      else {
        const unsigned a = nz(f2u(M)), b = nz(f2u(l));
        const int src = (lane & 3) * 8;
        st16<K_MLST>(v4u{(unsigned)__builtin_amdgcn_ds_bpermute(src, a), (unsigned)__builtin_amdgcn_ds_bpermute(src, b), (unsigned)__builtin_amdgcn_ds_bpermute(src + 4, a), (unsigned)__builtin_amdgcn_ds_bpermute(src + 4, b)}, rws, lane < 4 ? a_ml(gidx, sp) + lane * 16 : OOB);
      }
    }
    {
      const int hc = lane & 7, hbt = (lane >> 3) & 3, hb = 112 * hd + 28 * wl + 4 * hc;
      const int ho = hc < 7 ? a_h(2 * (lane >> 5), hbt) + hb * 4 : OOB;
      const v4u h0 = ld16<SC1>(rws, ho, 0), h1 = ld16<SC1>(rws, ho, FF * 4);
      const v2u bia = ld8<0>(mk(p.b1, FF * 2), hc < 7 ? hb * 2 : OOB, 0);
      PIN();
      float hv[4];
#pragma unroll
      for (int j = 0; j < 4; ++j) {
        const float t0 = u2f(h0[j]) + u2f(h1[j]);
        auto sw = __builtin_amdgcn_permlane32_swap(f2u(t0), f2u(t0), false, false);
        const float t = u2f(sw[0]) + u2f(sw[1]);
        const float u = rbf(rbf(t) + ((j & 1) ? bhi(bia[j >> 1]) : blo(bia[j >> 1])));
        const float z2 = u * fmaf(u * u, 0.044715f, 1.0f) * (-2.0f * 0.7978845608028654f * LOG2E);
        hv[j] = u * __builtin_amdgcn_rcpf(1.0f + __builtin_amdgcn_exp2f(z2));
      }
      if (K_RT) {
        if (lane < 32) {
#pragma unroll
          for (int k = 0; k < 4; ++k) *(LAS unsigned short*)(L + L_H + wl * 256 + hbt * 64 + k * 16 + hc * 2) = (unsigned short)pk(hv[k], hv[k]);
        }
      } else if (lane < 32) *(LAS v2u*)(L + L_H + wl * 256 + hbt * 64 + hc * 8) = v2u{pk(hv[0], hv[1]), pk(hv[2], hv[3])};
    }
    const rsrc_t r2 = mk(p.w2, FF * DM * 2), ro = mk(p.wo, NQ * DM * 2);
    const int o2 = (112 * hd + 28 * wl + (K_RT ? 1 : 8) * g) * 2048 + cg * 256 + n16 * 16;
    const int o2b = g < 3 ? o2 : OOB;
    const int oo = (128 * hd + (K_RO ? 32 * g + 8 * wl : 32 * wl + (K_RT ? 1 : 8) * g)) * 2048 + cg * 256 + n16 * 16;
    REREAD();
    v4u hact[4];
#pragma unroll
    for (int q = 0; q < 4; ++q) hact[q] = ACT(L_H + wl * 256 + (n16 & 3) * 64 + g * 16, q);
    int rdo = K_FT ? L_RED + 2 * (lane & 1) * RED_G + (4 * wl + 2 * ((lane >> 4) & 1) + ((lane >> 1) & 1)) * 16 + ((lane >> 2) & 3) * 4
                   : L_RED + (lane & 1) * RED_E + ((lane >> 1) & 3) * RED_G + (4 * wl + ((lane >> 3) & 3)) * 16;
    int fo = lane < 32 ? a_f(32 * cg + 8 * wl + (lane >> 2), hd) + (lane & 3) * 16 : OOB;
    int oa[4];
#pragma unroll
    for (int q = 0; q < 4; ++q) { oa[q] = (n16 >> 2) == q ? L_OA + (n16 & 3) * 256 + (K_RO ? 32 * g + 8 * wl : 32 * wl + 8 * g) * 2 : L_ZERO; asm volatile("" : "+v"(oa[q])); }
    asm volatile("" : "+v"(rdo), "+v"(fo));
    v4u tw[16];
    asm volatile("" :: "s"(r2), "s"(ro));
    asm volatile("" :: "v"(o2), "v"(o2b), "v"(oo));
    STAMP(5);
    if (K_TLB) {
      const unsigned t2 = __builtin_amdgcn_raw_buffer_load_b32(r2, lane == 0 ? o2 : OOB, 0, NT), to = __builtin_amdgcn_raw_buffer_load_b32(ro, lane == 0 ? oo : OOB, 0, NT);
      asm volatile("" :: "v"(t2), "v"(to));
    }
    BAR();
    if (K_TPRE) __builtin_amdgcn_s_sleep(K_TPRE);
    if (K_TABS > -1000) { _Pragma("nounroll") while (now32() - tbase < S6 + K_TABS) __builtin_amdgcn_s_sleep(1); }
#pragma unroll
    for (int i = 0; i < 16; ++i) {
      constexpr int ord3[16] = {0, 1, 8, 2, 9, 3, 10, 4, 11, 5, 12, 6, 13, 7, 14, 15}, ord4[16] = {0, 1, 8, 9, 2, 3, 10, 11, 4, 5, 12, 13, 6, 7, 14, 15},
                    ord5[16] = {0, 8, 1, 2, 9, 3, 4, 10, 5, 6, 11, 7, 12, 13, 14, 15};
      const int j = TORD == 0 ? i : TORD == 1 ? i ^ 8 : TORD == 2 ? (i >> 1) + 8 * (i & 1) : TORD == 3 ? ord3[i] : TORD == 4 ? ord4[i] : ord5[i];
      if (K_RT && j == 7) { tw[7] = v4u{0u, 0u, 0u, 0u}; continue; }
      if (TDYN && i >= W0) dyn_pace<0>(lag);
      else if (i >= K_TOPEN) {}
      else if (i >= WINT + 3 * K_TGROW) pace<WINT + 2>();
      else if (i >= WINT + 2 * K_TGROW) pace<WINT + 1>();
      else if (i >= WINT + K_TGROW) pace<WINT>();
      else if (i >= WINT) pace<WINT - 1>();
      else if (i > 0) __builtin_amdgcn_s_sleep(K_TRAMP);
      tw[j] = j < 8 ? ld16<NT>(r2, j < 4 || K_RT ? o2 : o2b, j * (K_RT ? 8192 : 2048)) : ld16<NT>(ro, oo, (j - 8) * (K_RT ? 8192 : 2048));
      PIN();
      if (TDYN && (i & 3) == 0) UPDATE_LAG(S6 + T6 * i / 16, TDYN - 1);
    }
    STAMP(9);
    f32x4 d[2] = {{0.f, 0.f, 0.f, 0.f}, {0.f, 0.f, 0.f, 0.f}};
    v4u wt[8], w2t[8];
#define TPAIR(dst, base, k) do { _Pragma("unroll") for (int j0 = 0; j0 < 8; ++j0) \
      dst[j0][k] = __builtin_amdgcn_perm(tw[(base) + 1 + 2 * (k)][j0 >> 1], tw[(base) + 2 * (k)][j0 >> 1], (j0 & 1) ? 0x07060302u : 0x05040100u); PIN(); } while (0)
    TPAIR(w2t, 0, 0); TPAIR(wt, 8, 0); TPAIR(w2t, 0, 1); TPAIR(w2t, 0, 2);
    if (TORD >= 2 && TORD <= 4) { TPAIR(wt, 8, 1); TPAIR(wt, 8, 2); }
    TPAIR(w2t, 0, 3);
#pragma unroll
    for (int j0 = 0; j0 < 8; ++j0) d[j0 & 1] = mfma(hact[j0 >> 1], w2t[j0], d[j0 & 1]);
    PIN();
    if (!(TORD >= 2 && TORD <= 4)) { TPAIR(wt, 8, 1); TPAIR(wt, 8, 2); }
    TPAIR(wt, 8, 3);
#pragma unroll
    for (int j0 = 0; j0 < 8; ++j0) asm volatile("" : "+v"(wt[j0]));
    STAMP(10);
    BAR();
    STAMP(11);
    v4u oact[4];
#pragma unroll
    for (int q = 0; q < 4; ++q) oact[q] = *(const LAS v4u*)(L + oa[q]);
#pragma unroll
    for (int j0 = 0; j0 < 8; ++j0) d[j0 & 1] = mfma(oact[j0 >> 1], wt[j0], d[j0 & 1]);
    PUT_RED(d);
    BAR();
    STAMP(12);
    f32x4 t;
    if (K_FT) {
#pragma unroll
      for (int k = 0; k < 4; ++k) {
        const int o = rdo + (k & 1) * RED_E + (k >> 1) * RED_G;
        t[k] = (*(const LAS float*)(L + o) + *(const LAS float*)(L + o + RED_W)) + (*(const LAS float*)(L + o + 2 * RED_W) + *(const LAS float*)(L + o + 3 * RED_W));
      }
    } else t = (*(const LAS f32x4*)(L + rdo) + *(const LAS f32x4*)(L + rdo + RED_W)) + (*(const LAS f32x4*)(L + rdo + 2 * RED_W) + *(const LAS f32x4*)(L + rdo + 3 * RED_W));
    st16<SC1>(v4u{nz(f2u(t[0])), nz(f2u(t[1])), nz(f2u(t[2])), nz(f2u(t[3]))}, rws, fo);
    STAMP(13);
    asm volatile(".Lfd_code_end:");
  } else {
    PUT_ACC();
    class_bar<0>(ctr + 5, lane);
    STAMP(9);
    {
      f32x4 t = *(const LAS f32x4*)(L + L_O + 4 * O_W + oro);
#pragma unroll
      for (int ww = 0; ww < 4; ++ww) t += *(const LAS f32x4*)(L + L_O + ww * O_W + oro);
      const unsigned a0 = nz(pk(t[0], t[1])), a1 = nz(pk(t[2], t[3]));
      auto s0 = __builtin_amdgcn_permlane32_swap(a0, a0, false, false);
      auto s1 = __builtin_amdgcn_permlane32_swap(a1, a1, false, false);
      st16<SC1>(v4u{s0[0], s1[0], s0[1], s1[1]}, rws, lane < 32 ? a_o(8 * kvh + 2 * wl + g, ab, sp) + n16 * 16 : OOB);
    }
    PIN();
    STAMP(10);
    __builtin_amdgcn_s_setprio(0);
    int spins = 0;
    if (__any(min(ml.x, ml.y) == 0u)) {
      _Pragma("nounroll") while (true) {
        REREAD();
        ml = ld8<SC1>(rws, mo, 0);
        if (!__any(min(ml.x, ml.y) == 0u) || ++spins > SPIN_CAP) break;
        __builtin_amdgcn_s_sleep(2);
      }
    }
    STAMPV(19, spins);
    float M = u2f(ml.x);
    M = fmaxf(M, ror<8>(M)); M = fmaxf(M, ror<4>(M)); M = fmaxf(M, ror<2>(M)); M = fmaxf(M, ror<1>(M));
    const float e = __builtin_amdgcn_exp2f((u2f(ml.x) - M) * LOG2E);
    float Ls = e * u2f(ml.y);
    Ls += ror<8>(Ls); Ls += ror<4>(Ls); Ls += ror<2>(Ls); Ls += ror<1>(Ls);
    const float f = e * __builtin_amdgcn_rcpf(Ls);
    float fk[4] = {dpp<0x150>(f), dpp<0x151>(f), dpp<0x152>(f), dpp<0x153>(f)};
    int wro = L_OA + wl * 256 + (K_RT ? (n16 >> 2) * 64 + g * 16 + (n16 & 3) * 4 : n16 * 16 + g * 4), fo = a_f(cu, 0) + lane * 16, oof = lane < 16 ? (fb * DM + fc) * 2 : OOB;
    float res = blo(sm.x) + blo(sm.y);
    asm volatile("" : "+v"(fk[0]), "+v"(fk[1]), "+v"(fk[2]), "+v"(fk[3]), "+v"(wro), "+v"(fo), "+v"(oof), "+v"(res));
    __builtin_amdgcn_s_sleep(ODMIN);
    _Pragma("nounroll") while (now32() - tbase < S6 + TO) __builtin_amdgcn_s_sleep(1);
    const int oo_ = a_o(hd, wl, 4 * g) + n16 * 16;
    v4u op[4];
#pragma unroll
    for (int k = 0; k < 4; ++k) op[k] = ld16<SC1>(rws, oo_, k * 256);
    PIN();
    spins = 0;
    if (__any(min(min(umin4(op[0]), umin4(op[1])), min(umin4(op[2]), umin4(op[3]))) == 0u)) {
      _Pragma("nounroll") while (true) {
        REREAD();
        v4u nw[4];
#pragma unroll
        for (int k = 0; k < 4; ++k) nw[k] = ld16<SC1>(rws, umin4(op[k]) == 0u ? oo_ : OOB, k * 256);
        PIN();
#pragma unroll
        for (int k = 0; k < 4; ++k) op[k] |= nw[k];
        if (++spins > SPIN_CAP || !__any(min(min(umin4(op[0]), umin4(op[1])), min(umin4(op[2]), umin4(op[3]))) == 0u)) break;
        __builtin_amdgcn_s_sleep(1);
      }
    }
    STAMPV(18, spins);
    STAMP(13);
    f32x2 t[4];
#pragma unroll
    for (int j = 0; j < 4; ++j) {
      t[j] = fk[0] * f32x2{blo(op[0][j]), bhi(op[0][j])};
#pragma unroll
      for (int k = 1; k < 4; ++k) t[j] = __builtin_elementwise_fma(f32x2{fk[k], fk[k]}, f32x2{blo(op[k][j]), bhi(op[k][j])}, t[j]);
    }
    float u[4];
#pragma unroll
    for (int j = 0; j < 4; ++j) {
      auto sw = K_RT ? __builtin_amdgcn_permlane32_swap(f2u(t[2 * (j >> 1)][j & 1]), f2u(t[2 * (j >> 1) + 1][j & 1]), false, false)
                     : __builtin_amdgcn_permlane32_swap(f2u(t[j >> 1][j & 1]), f2u(t[2 + (j >> 1)][j & 1]), false, false);
      u[j] = u2f(sw[0]) + u2f(sw[1]);
    }
    auto s0 = __builtin_amdgcn_permlane16_swap(f2u(u[0]), f2u(u[K_RT ? 1 : 2]), false, false);
    auto s1 = __builtin_amdgcn_permlane16_swap(f2u(u[K_RT ? 2 : 1]), f2u(u[3]), false, false);
    *(LAS unsigned*)(L + wro) = pk(u2f(s0[0]) + u2f(s0[1]), u2f(s1[0]) + u2f(s1[1]));
    BAR();
    BAR();
    if (wl == 2) {
      v4u f0, f1;
      spins = 0;
      __builtin_amdgcn_s_sleep(FDELAY);
      _Pragma("nounroll") while (true) {
        REREAD();
        f0 = ld16<SC1>(rws, fo, 0);
        f1 = ld16<SC1>(rws, fo, 1024);
        if (!__any(min(umin4(f0), umin4(f1)) == 0u) || ++spins > SPIN_CAP) break;
        __builtin_amdgcn_s_sleep(1);
      }
      STAMPV(17, spins);
      STAMP(11);
      const f32x4 y4 = __builtin_bit_cast(f32x4, f0) + __builtin_bit_cast(f32x4, f1);
      const bool h8 = lane & 8, h4 = lane & 4;
      const float a0 = (h8 ? y4[2] : y4[0]) + ror<8>(h8 ? y4[0] : y4[2]), a1 = (h8 ? y4[3] : y4[1]) + ror<8>(h8 ? y4[1] : y4[3]);
      const float y = res + xrow_sum((h4 ? a1 : a0) + dpp<0x1B>(dpp<0x141>(h4 ? a0 : a1)));
      __builtin_amdgcn_raw_buffer_store_b16((unsigned short)pk(y, y), mk(p.out, 4 * DM * 2), oof, 0, K_OAUX);
      STAMP(12);
    }
  }
  STAMP_FLUSH();
}
}

extern "C" void kernel_launch(void* const* d_in, const int* in_sizes, int n_in, void* d_out, int out_size, void* d_ws, size_t ws_size,
                              hipStream_t stream) {
  Params p;
  p.x = d_in[0]; p.kc = d_in[1]; p.vc = d_in[2]; p.wq = d_in[3]; p.wo = d_in[4];
  p.w1 = d_in[6]; p.b1 = d_in[7]; p.w2 = d_in[8]; p.b2 = d_in[9];
  p.out = d_out; p.ws = d_ws;
  hipLaunchKernelGGL(fused_decode, dim3(NWG), dim3(NTHR), 0, stream, p);
}
```

```cpp
#include <hip/hip_runtime.h>
#include <hip/hip_bf16.h>
#include <stdint.h>

namespace {
typedef unsigned v4u __attribute__((ext_vector_type(4)));
typedef unsigned v2u __attribute__((ext_vector_type(2)));
typedef float f32x4 __attribute__((ext_vector_type(4)));
typedef float f32x2 __attribute__((ext_vector_type(2)));
typedef short bf16x8 __attribute__((ext_vector_type(8)));
typedef __bf16 bf16x2 __attribute__((ext_vector_type(2)));
typedef __amdgpu_buffer_rsrc_t rsrc_t;
#define LAS __attribute__((address_space(3)))
#define CAS __attribute__((address_space(4)))

constexpr int NWG = 256, NTHR = 512;
constexpr int DM = 1024, NQ = 4096, FF = 3584;
constexpr int WINW = 6, WINT = 5;
constexpr int W0 = 4, W1 = 5, W2 = 7;
constexpr int TH1 = -22, TH2 = 20;
constexpr int S2 = 140, T2 = 465, T3 = 450, T4 = 440, T5 = 445, S6 = S2 + T2 + T3 + T4 + T5;
constexpr int EFF = 5;
constexpr int TW = 15, TO = 205;
constexpr int RAMP = 5, VPRE = 4, TRAMP = 3, ODMIN = 20, FDELAY = 18;
constexpr int FLPOS = 30, MID = 12, VEND = 28;
constexpr int WS_E = 0;
constexpr int WS_PAD = 64;
constexpr int WS_FLAG = 4096;
constexpr int WS_LINE = 8192;
constexpr int WS_ML = WS_LINE + 32768;
constexpr int WS_Q = WS_ML + 16384;
constexpr int WS_H = WS_Q + 262144;
constexpr int WS_O = WS_H + 16 * FF * 4;
constexpr int WS_F = WS_O + 524288;
constexpr int WS_END = WS_F + 524288;
__device__ __forceinline__ int a_ml(int grp, int s) { return WS_ML + (grp * 16 + s) * 64; }
__device__ __forceinline__ int a_q(int b, int head, int kq) { return WS_Q + ((b * 32 + head) * 4 + kq) * 512; }
__device__ __forceinline__ int a_h(int kq, int b) { return WS_H + (((kq >> 1) * 4 + b) * 2 + (kq & 1)) * FF * 4; }
__device__ __forceinline__ int a_o(int head, int b, int s) { return WS_O + ((head * 4 + b) * 16 + s) * 256; }
__device__ __forceinline__ int a_f(int f, int hd) { return WS_F + (f * 32 + hd) * 64; }
constexpr int OOB = 0x7f000000;
constexpr int NT = 2, SC1 = 16;
constexpr int SPIN_CAP = 1 << 18;
__device__ __forceinline__ unsigned long long tag_of(unsigned long long e) { return e * 0x9E3779B97F4A7C15ull + 0xD1B54A32D192ED03ull; }
constexpr int L_CTR = 0;
constexpr int L_ZERO = 32;
constexpr int L_GEN = 64;
constexpr int L_XN = 80;
constexpr int L_Q = L_XN + 2048;
constexpr int L_M = L_Q + 2048;
constexpr int L_L = L_M + 256;
constexpr int L_RED = L_L + 256;
constexpr int RED_G = 320, RED_E = 4 * RED_G + 32, RED_W = 2 * RED_E;
constexpr int L_O = L_RED + 4 * RED_W;
constexpr int O_W = 8 * 132 * 4;
constexpr int L_H = L_O + 5 * O_W;
constexpr int L_OA = L_H + 1024;
constexpr int L_END = L_OA + 1024;

struct Params {
  const void *wq, *w1, *x, *kc, *vc, *wo, *w2, *b1, *b2;
  void *out, *ws;
};
static_assert(sizeof(Params) == 88, "no padding");

__device__ __forceinline__ rsrc_t mk(const void* p, int bytes) { return __builtin_amdgcn_make_buffer_rsrc((void*)p, 0, bytes, 0x00020000); }
template <int AUX> __device__ __forceinline__ v4u ld16(rsrc_t r, int voff, int soff) { return __builtin_amdgcn_raw_buffer_load_b128(r, voff, soff, AUX); }
template <int AUX> __device__ __forceinline__ v2u ld8(rsrc_t r, int voff, int soff) { return __builtin_amdgcn_raw_buffer_load_b64(r, voff, soff, AUX); }
template <int AUX> __device__ __forceinline__ void st16(v4u v, rsrc_t r, int voff) { __builtin_amdgcn_raw_buffer_store_b128(v, r, voff, 0, AUX); }
template <int AUX> __device__ __forceinline__ void st8(v2u v, rsrc_t r, int voff) { __builtin_amdgcn_raw_buffer_store_b64(v, r, voff, 0, AUX); }
__device__ __forceinline__ f32x4 mfma(v4u a, v4u b, f32x4 c) {
  return __builtin_amdgcn_mfma_f32_16x16x32_bf16(__builtin_bit_cast(bf16x8, a), __builtin_bit_cast(bf16x8, b), c, 0, 0, 0);
}
__device__ __forceinline__ float u2f(unsigned u) { return __builtin_bit_cast(float, u); }
__device__ __forceinline__ unsigned f2u(float f) { return __builtin_bit_cast(unsigned, f); }
__device__ __forceinline__ unsigned pk(float lo, float hi) {
  f32x2 v = {lo, hi};
  return __builtin_bit_cast(unsigned, __builtin_convertvector(v, bf16x2));
}
__device__ __forceinline__ float blo(unsigned u) { return u2f(u << 16); }
__device__ __forceinline__ float bhi(unsigned u) { return u2f(u & 0xffff0000u); }
__device__ __forceinline__ float rbf(float f) { return blo(pk(f, f)); }
template <int C> __device__ __forceinline__ float dpp(float x) { return u2f(__builtin_amdgcn_mov_dpp(f2u(x), C, 0xf, 0xf, true)); }
template <int N> __device__ __forceinline__ float ror(float x) { return dpp<0x120 + N>(x); }
__device__ __forceinline__ float xrow_sum(float x) {
  auto s = __builtin_amdgcn_permlane16_swap(f2u(x), f2u(x), false, false);
  x = u2f(s[0]) + u2f(s[1]);
  auto t = __builtin_amdgcn_permlane32_swap(f2u(x), f2u(x), false, false);
  return u2f(t[0]) + u2f(t[1]);
}
__device__ __forceinline__ float xrow_max(float x) {
  auto s = __builtin_amdgcn_permlane16_swap(f2u(x), f2u(x), false, false);
  x = fmaxf(u2f(s[0]), u2f(s[1]));
  auto t = __builtin_amdgcn_permlane32_swap(f2u(x), f2u(x), false, false);
  return fmaxf(u2f(t[0]), u2f(t[1]));
}
__device__ __forceinline__ float wave_sum(float x) {
  x += ror<8>(x); x += ror<4>(x); x += ror<2>(x); x += ror<1>(x);
  return xrow_sum(x);
}
__device__ __forceinline__ unsigned umin4(v4u v) { return min(min(v.x, v.y), min(v.z, v.w)); }
__device__ __forceinline__ unsigned nz(unsigned u) { return max(u, 1u); }
#define PIN() __builtin_amdgcn_sched_barrier(0)
#define BAR() do { PIN(); asm volatile("s_waitcnt lgkmcnt(0)\n\ts_barrier" ::: "memory"); PIN(); } while (0)
#define REREAD() asm volatile("" ::: "memory")
__device__ __forceinline__ unsigned tr2(v4u lo, v4u hi, int j0) { return __builtin_amdgcn_perm(hi[j0 >> 1], lo[j0 >> 1], (j0 & 1) ? 0x07060302u : 0x05040100u); }
__device__ __forceinline__ v4u tr8(const v4u* w, int j0) { return v4u{tr2(w[0], w[1], j0), tr2(w[2], w[3], j0), tr2(w[4], w[5], j0), tr2(w[6], w[7], j0)}; }
__device__ __forceinline__ void ln_stats(const float (&v)[16], float& m, float& inv) {
  float s = 0.f;
#pragma unroll
  for (int i = 0; i < 16; ++i) s += v[i];
  const float mf = wave_sum(s) * (1.0f / DM);
  float ss = 0.f;
#pragma unroll
  for (int i = 0; i < 16; ++i) { const float d = v[i] - mf; ss = fmaf(d, d, ss); }
  const float var = rbf(wave_sum(ss) * (1.0f / DM));
  m = rbf(mf);
  inv = __builtin_amdgcn_rcpf(rbf(__builtin_amdgcn_sqrtf(rbf(var + 1e-5f))));
}
__device__ __forceinline__ unsigned ln_pair(float a, float b, float m, float inv) {
  const unsigned d = pk(a - m, b - m);
  return pk(blo(d) * inv, bhi(d) * inv);
}
__device__ __forceinline__ unsigned lds_arrive(LAS unsigned* c, int lane) {
  unsigned old = 0;
  REREAD();
  if (lane == 0) old = __hip_atomic_fetch_add(c, 1u, __ATOMIC_RELAXED, __HIP_MEMORY_SCOPE_WORKGROUP);
  return __builtin_amdgcn_readfirstlane(old);
}
template <int SL> __device__ __forceinline__ void class_bar(LAS unsigned* c, int lane) {
  PIN();
  if (lds_arrive(c, lane) != 3u) {
    _Pragma("nounroll") while (__builtin_amdgcn_readfirstlane(__hip_atomic_load(c, __ATOMIC_RELAXED, __HIP_MEMORY_SCOPE_WORKGROUP)) < 4u)
      __builtin_amdgcn_s_sleep(SL);
  }
  REREAD();
  PIN();
}
template <int N> __device__ __forceinline__ void pace() { asm volatile("s_waitcnt vmcnt(%0)" :: "n"(N) : "memory"); }
template <int B, int X0, int X1, int X2> __device__ __forceinline__ void dyn_pace(int lag) {
  asm volatile("s_cmp_gt_i32 %0, 0\n\ts_cbranch_scc1 1f\n\ts_waitcnt vmcnt(%2)\n1:\n\t"
               "s_cmp_gt_i32 %0, %1\n\ts_cbranch_scc1 2f\n\ts_waitcnt vmcnt(%3)\n2:\n\ts_waitcnt vmcnt(%4)"
               :: "s"(lag), "n"(B), "n"(X0 - 1), "n"(X1 - 1), "n"(X2 - 1) : "memory", "scc");
}
__device__ __forceinline__ unsigned touch(const void* base, int off) {
  asm volatile("" : "+s"(off));
  return *(const CAS unsigned*)((unsigned long long)base + (unsigned)off);
}
__device__ __forceinline__ int now32() { return (int)(unsigned)__builtin_amdgcn_s_memrealtime(); }

__global__ void __launch_bounds__(NTHR, 2) fused_decode(Params) {
  __shared__ __attribute__((aligned(16))) unsigned char smem[L_END];
  LAS unsigned char* L = (LAS unsigned char*)smem;
  LAS unsigned* ctr = (LAS unsigned*)(L + L_CTR);
  const int lane = threadIdx.x & 63;
  int w = __builtin_amdgcn_readfirstlane(threadIdx.x >> 6) ^ 4, bid = blockIdx.x;
  typedef unsigned long long s8p __attribute__((ext_vector_type(8)));
  s8p ka;
  unsigned long long wp, kws, pc;
  int ldw, wbytes, so0, so1, mg, ta;
  asm volatile(
      "s_load_dwordx16 %[ka], %[kp], 0x10\n\t"
      "s_lshr_b32 %[ta], %[bid], 7\n\t"
      "s_lshl_b32 %[ta], %[ta], 3\n\t"
      "s_cmp_lt_u32 %[w], 4\n\t"
      "s_cselect_b32 %[ta], 16, %[ta]\n\t"
      "s_load_dwordx2 %[wp], %[kp], %[ta]\n\t"
      "s_load_dwordx2 %[kws], %[kp], 0x50\n\t"
      "s_cmp_lg_u32 %[w], 4\n\t"
      "s_cbranch_scc1 1f\n\t"
      "s_getpc_b64 %[pc]\n"
      ".Lfd_code_base:\n\t"
      "v_mbcnt_lo_u32_b32 v255, -1, 0\n\t"
      "s_lshr_b32 %[mg], %[bid], 3\n\t"
      "s_lshl_b32 %[mg], %[mg], 7\n\t"
      "v_lshl_add_u32 v255, v255, 12, %[mg]\n\t"
      "v_min_u32 v255, .Lfd_code_end-.Lfd_code_base-4, v255\n\t"
      "global_load_dword v255, v255, %[pc]\n"
      "1:\n\t"
      "s_cmp_lt_u32 %[bid], 0x80\n\t"
      "s_movk_i32 %[ldw], 0x1c00\n\t"
      "s_cselect_b32 %[ldw], 0x2000, %[ldw]\n\t"
      "s_cselect_b32 %[so1], 16, 14\n\t"
      "s_bitcmp1_b32 %[bid], 0\n\t"
      "s_cselect_b32 %[so1], %[so1], 0\n\t"
      "s_bfe_u32 %[so0], %[bid], 0x40003\n\t"
      "s_add_u32 %[so1], %[so1], %[so0]\n\t"
      "s_lshl_b32 %[so1], %[so1], 8\n\t"
      "s_bfe_u32 %[so0], %[bid], 0x20001\n\t"
      "s_lshl_b32 %[so0], %[so0], 4\n\t"
      "s_and_b32 %[mg], %[w], 3\n\t"
      "s_add_u32 %[so0], %[so0], %[mg]\n\t"
      "s_lshl_b32 %[so0], %[so0], 4\n\t"
      "s_mul_i32 %[so0], %[so0], %[ldw]\n\t"
      "s_add_u32 %[so0], %[so0], %[so1]\n\t"
      "s_lshl_b32 %[wb], %[ldw], 10\n\t"
      "s_cmp_lt_u32 %[bid], 0xf0\n\t"
      "s_cselect_b32 %[wb], %[wb], 0\n\t"
      "s_lshl_b32 %[mg], %[ldw], 6\n\t"
      "s_cmp_gt_u32 %[w], 3\n\t"
      "s_cbranch_scc1 2f\n\t"
      "s_bfe_u32 %[so0], %[bid], 0x10002\n\t"
      "s_lshl_b32 %[so1], %[w], 1\n\t"
      "s_add_u32 %[so0], %[so0], %[so1]\n\t"
      "s_lshl_b32 %[so0], %[so0], 10\n\t"
      "s_xor_b32 %[so1], %[so0], 0x400\n\t"
      "s_setprio 1\n\t"
      "s_movk_i32 %[wb], 0x2000\n\t"
      "s_movk_i32 %[mg], 0x100\n"
      "2:\n\t"
      "s_waitcnt lgkmcnt(0)"
      : [ka] "=&s"(ka), [wp] "=&s"(wp), [kws] "=&s"(kws), [pc] "=&s"(pc), [ldw] "=&s"(ldw), [wb] "=&s"(wbytes),
        [so0] "=&s"(so0), [so1] "=&s"(so1), [mg] "=&s"(mg), [ta] "=&s"(ta), [w] "+s"(w), [bid] "+s"(bid)
      : [kp] "s"(__builtin_amdgcn_kernarg_segment_ptr())
      : "scc", "v255", "memory");
  const int wl = w & 3;
  const bool isA = w < 4;
  const int xs = bid & 7, ci = bid >> 3;
  const int n16 = lane & 15, g = lane >> 4;
  const int kq = xs >> 1, isq = ci < 16, cb = (isq ? 16 : 14) * (xs & 1) + (ci & 15);
  const int off0 = __umul24(g, mg) + n16 * 16;
  const rsrc_t rw = mk((const void*)wp, wbytes);
  v4u xr[2], wv[16];
  if (isA) {
    xr[0] = ld16<0>(rw, off0, so0);
    PIN();
    xr[1] = ld16<0>(rw, off0, so1);
  }
  int w2 = w;
  asm volatile("" : "+s"(w2));
  PIN();
  if (w2 >= 4) {
#pragma unroll
    for (int j = 0; j < WINW; ++j) { wv[j] = ld16<NT>(rw, off0, so0 + j * ldw); PIN(); }
  }
  PIN();
  const struct { const void *x, *kc, *vc, *wo, *w2, *b1, *b2, *out; } p = {(const void*)ka[0], (const void*)ka[1], (const void*)ka[2],
      (const void*)ka[3], (const void*)ka[4], (const void*)ka[5], (const void*)ka[6], (const void*)ka[7]};
  const rsrc_t rws = mk((const void*)kws, WS_END);
  const int gidx = 2 * xs + (ci >> 4), ab = gidx >> 2, kvh = gidx & 3, sp = ci & 15;
  const int hd = 4 * xs + (ci >> 3), cg = ci & 7;
  const int cu = xs * 32 + ci;
  const int fb = (lane >> 2) & 3, fc = 4 * cu + (lane & 3);
  v2u sm;
  int tbase;
#define ACT(base, q) (*(const LAS v4u*)(L + ((n16 >> 2) == (q) ? (base) : L_ZERO)))
#define PUT_RED(d) do { *(LAS f32x4*)(L + L_RED + wl * RED_W + g * RED_G + n16 * 16) = d[0]; \
                        *(LAS f32x4*)(L + L_RED + wl * RED_W + RED_E + g * RED_G + n16 * 16) = d[1]; } while (0)

  if (isA) {
    if (w == 0 && lane < 12) ctr[lane] = 0u;
    float v[16];
#pragma unroll
    for (int i = 0; i < 8; ++i) { v[2 * i] = blo(xr[i >> 2][i & 3]); v[2 * i + 1] = bhi(xr[i >> 2][i & 3]); }
    asm volatile("" : "+v"(v[0]), "+v"(v[15]));
    PIN();
    tbase = now32();
    if (w == 0 && lane == 0) { ctr[13] = tbase; ctr[14] = -TH1; }
    PIN();
    const unsigned tws = touch((const void*)kws, WS_PAD);
    PIN();
    float m, inv;
    ln_stats(v, m, inv);
    if (!isq) {
#pragma unroll
      for (int i = 0; i < 16; i += 2) { const unsigned o = ln_pair(v[i], v[i + 1], m, inv); v[i] = blo(o); v[i + 1] = bhi(o); }
      ln_stats(v, m, inv);
    }
    if ((lane >> 5) == (kq & 1))
      *(LAS v4u*)(L + L_XN + wl * 512 + (lane & 31) * 16) =
          v4u{ln_pair(v[0], v[1], m, inv), ln_pair(v[2], v[3], m, inv), ln_pair(v[4], v[5], m, inv), ln_pair(v[6], v[7], m, inv)};
    asm volatile("" :: "s"(tws));
  } else {
#pragma unroll
    for (int j = WINW; j < 16; ++j) { pace<WINW - 1>(); wv[j] = ld16<NT>(rw, off0, so0 + j * ldw); PIN(); }
    const v2u ge = ld8<SC1>(rws, wl == 0 && lane == 0 ? WS_E : OOB, 0);
    BAR();
    tbase = __builtin_amdgcn_readfirstlane(ctr[13]);
    f32x4 d[2] = {{0.f, 0.f, 0.f, 0.f}, {0.f, 0.f, 0.f, 0.f}};
#pragma unroll
    for (int t = 0; t < 2; ++t) {
#pragma unroll
      for (int q = 0; q < 4; ++q) {
        const v4u act = ACT(L_XN + (n16 & 3) * 512 + (64 * g + 16 * wl + 8 * t) * 2, q);
        d[0] = mfma(act, tr8(wv + 8 * t, 2 * q), d[0]);
        d[1] = mfma(act, tr8(wv + 8 * t, 2 * q + 1), d[1]);
      }
      PIN();
    }
    PUT_RED(d);
    if (wl == 0 && lane == 0) *(LAS v2u*)(L + L_GEN) = ge;
    class_bar<1>(ctr + 0, lane);
    if (wl < 2) {
      f32x4 sum = {0.f, 0.f, 0.f, 0.f};
#pragma unroll
      for (int ww = 0; ww < 4; ++ww)
#pragma unroll
        for (int k = 0; k < 4; ++k)
          sum[k] += *(const LAS float*)(L + L_RED + ww * RED_W + (k & 1) * RED_E + (2 * (lane & 1) + (k >> 1)) * RED_G + (wl * 8 + (n16 >> 1)) * 16 + g * 4);
      const int off = (isq ? a_q(g, cb, kq) : a_h(kq, g) + cb * 512) + wl * 256 + n16 * 16;
      st16<SC1 | NT>(__builtin_bit_cast(v4u, sum), rws, ci < 30 ? off : OOB);
    }
    {
      const v4u z = {0u, 0u, 0u, 0u};
      st16<SC1>(z, rws, wl < 2 ? a_o(8 * kvh + 4 * wl + g, ab, sp) + n16 * 16 : a_f(32 * cg + 16 * (wl & 1) + (lane >> 2), hd) + (lane & 3) * 16);
      if (wl == 2) st16<SC1>(z, rws, lane < 4 ? a_ml(gidx, sp) + lane * 16 : OOB);
    }
    sm.x = __builtin_amdgcn_raw_buffer_load_b16(mk(p.x, 4 * DM * 2), wl == 2 ? (fb * DM + fc) * 2 : OOB, 0, 0);
    sm.y = __builtin_amdgcn_raw_buffer_load_b16(mk(p.b2, DM * 2), wl == 2 ? fc * 2 : OOB, 0, 0);
    if (cg == 0) {
      const unsigned tp = touch(wl == 0 ? p.vc : wl == 1 ? p.wo : wl == 2 ? p.w2 : p.b1, wl == 0 ? cu * 262144 : wl == 1 ? hd * 262144 : wl == 2 ? hd * 229376 : 0);
      asm volatile("" :: "s"(tp));
    }
    pace<0>();
    if (lds_arrive(ctr + 1, lane) == 3u) {
      const unsigned long long tg = tag_of(*(const LAS unsigned long long*)(L + L_GEN));
      st16<SC1>(v4u{(unsigned)tg, (unsigned)(tg >> 32), (unsigned)(tbase + (int)ctr[14] + TH1 + EFF), 0u}, rws, lane == 0 ? WS_LINE + cu * 128 : OOB);
      st8<SC1>(v2u{(unsigned)tg, (unsigned)(tg >> 32)}, rws, lane == 0 ? WS_FLAG + cu * 8 : OOB);
    }
  }
  const rsrc_t rk = mk((const char*)p.kc + (size_t)cu * 262144, 262144), rv = mk((const char*)p.vc + (size_t)cu * 262144, 262144);
  const int cbase = (w >> 2) * 131072 + wl * 4096;
  int koff = cbase + ((lane >> 2) & 3) * 256 + g * 64 + (lane & 3) * 16, voff = cbase + lane * 16;
  const int fhd = 8 * kvh + 2 * wl + ((lane >> 2) & 1);
  int fqo = isA && lane < 8 ? WS_LINE + ((2 * (lane & 3) + (fhd >> 4)) * 32 + (fhd & 15)) * 128 : OOB;
  v4u r[32], fq;
  int lag = 0, plag = 0, adj = 0;
#define UPDATE_LAG(t) do { lag = now32() - tbase - ((t) + TH1); asm volatile("" : "+s"(lag)); PIN(); } while (0)
#define MEAN_STAMP(a) ((((a) & 0xffffff) >> 5) - 150)
  asm volatile("" : "+v"(koff), "+v"(voff), "+v"(fqo));
  asm volatile("" :: "s"(rk));
  __builtin_amdgcn_s_setprio(1);
  BAR();
#pragma unroll
  for (int i = 0; i < 32; ++i) {
    if (i > FLPOS) dyn_pace<TH2 - TH1, W0 + 1, W1 + 1, W2 + 1>(lag);
    else if (i >= W0) dyn_pace<TH2 - TH1, W0, W1, W2>(lag);
    else if (i > 0) __builtin_amdgcn_s_sleep(RAMP);
    r[i] = ld16<NT>(rk, koff, (i >> 2) * 16384 + (i & 3) * 1024);
    PIN();
    if (i == FLPOS) { fq = ld16<SC1>(rws, fqo, 0); PIN(); }
    if (i == MID) {
      REREAD();
      const int a = __builtin_amdgcn_readfirstlane((int)ctr[7]);
      adj = (a >> 24) == 32 ? MEAN_STAMP(a) : 0;
    }
    if ((i & 3) == 0) {
      UPDATE_LAG(adj + (isA ? S2 + T2 * i / 32 : S2 + T2 + T3 * i / 32));
      if (i >= 8 && i <= 24) {
        if (w == 0) ctr[14] = lag + (plag > TH2 - TH1 ? (W2 - W1) * 13 : plag > 0 ? 0 : (W0 - W1) * 13);
        PIN();
      }
      plag = lag;
    }
  }
  __builtin_amdgcn_s_setprio(0);
  BAR();
  const v2u gl = *(const LAS v2u*)(L + L_GEN);
  const unsigned long long gen = (unsigned long long)(unsigned)__builtin_amdgcn_readfirstlane(gl.y) << 32 | (unsigned)__builtin_amdgcn_readfirstlane(gl.x);
  const unsigned long long tag64 = tag_of(gen);
  const unsigned tag0 = (unsigned)tag64, tag1 = (unsigned)(tag64 >> 32);
  if (isA) {
    if (__any(lane < 8 && ((fq.x ^ tag0) | (fq.y ^ tag1)) != 0u)) {
      int spins = 0;
      _Pragma("nounroll") while (true) {
        REREAD();
        fq = ld16<SC1>(rws, fqo, 0);
        if (++spins > SPIN_CAP || !__any(lane < 8 && ((fq.x ^ tag0) | (fq.y ^ tag1)) != 0u)) break;
        __builtin_amdgcn_s_sleep(2);
      }
    }
    REREAD();
    tbase = __builtin_amdgcn_readfirstlane(ctr[13]);
    if (lane < 8) __hip_atomic_fetch_add(ctr + 7, (unsigned)(min(max((int)fq.z - tbase, -150), 150) + 150 + (1 << 24)), __ATOMIC_RELAXED, __HIP_MEMORY_SCOPE_WORKGROUP);
    const int qo = a_q(ab, 8 * kvh + 2 * wl + (lane >> 5), 0) + (lane & 31) * 16;
    v4u qp[4];
#pragma unroll
    for (int s = 0; s < 4; ++s) qp[s] = ld16<SC1>(rws, qo, s * 512);
    PIN();
    const f32x4 qs = (__builtin_bit_cast(f32x4, qp[0]) + __builtin_bit_cast(f32x4, qp[1])) + (__builtin_bit_cast(f32x4, qp[2]) + __builtin_bit_cast(f32x4, qp[3]));
    *(LAS v2u*)(L + L_Q + (2 * wl + (lane >> 5)) * 256 + (lane & 31) * 8) = v2u{pk(qs[0], qs[1]), pk(qs[2], qs[3])};
    class_bar<1>(ctr + 2, lane);
  }
  tbase += MEAN_STAMP(__builtin_amdgcn_readfirstlane((int)ctr[7]));
  const int hl = n16 & 7, wb = n16 >> 3;
  const v4u qb0 = *(const LAS v4u*)(L + L_Q + hl * 256 + (4 * g + wb) * 16);
  const v4u qb1 = *(const LAS v4u*)(L + L_Q + hl * 256 + (4 * g + 2 + wb) * 16);
  float s[16];
#pragma unroll
  for (int u = 0; u < 8; ++u) {
    float t0[4], t1[4];
#pragma unroll
    for (int k = 0; k < 4; ++k) {
      const f32x4 d0 = mfma(r[4 * u + k], qb0, f32x4{0.f, 0.f, 0.f, 0.f});
      const f32x4 d1 = mfma(r[4 * u + k], qb1, f32x4{0.f, 0.f, 0.f, 0.f});
      t0[k] = d0[0] + d1[2];
      t1[k] = d0[1] + d1[3];
    }
#pragma unroll
    for (int k = 0; k < 4; k += 2) {
      const float xx = wb ? t1[k + 1] : t0[k], yy = wb ? t1[k] : t0[k + 1];
      s[2 * u + (k >> 1)] = xx + ror<8>(yy);
    }
    PIN();
  }
  float m = -3.0e38f;
#pragma unroll
  for (int i = 0; i < 16; i += 2) {
    const unsigned a = pk(s[i], s[i + 1]);
    const unsigned c = pk(blo(a) * 0.08838834764831845f, bhi(a) * 0.08838834764831845f);
    s[i] = blo(c); s[i + 1] = bhi(c);
    m = fmaxf(m, fmaxf(s[i], s[i + 1]));
  }
  m = fmaxf(m, ror<8>(m));
  m = xrow_max(m);
  if (lane < 8) *(LAS float*)(L + L_M + (w * 8 + lane) * 4) = m;
  constexpr float LOG2E = 1.4426950408889634f;
  if (!isA) {
    if (wl == 0) {
      int spins = 0;
      _Pragma("nounroll") while (true) {
        REREAD();
        const v4u a = ld16<SC1>(rws, WS_FLAG + lane * 16, 0), b = ld16<SC1>(rws, WS_FLAG + lane * 16, 1024);
        const unsigned bad = (a.x ^ tag0) | (a.y ^ tag1) | (a.z ^ tag0) | (a.w ^ tag1) | (b.x ^ tag0) | (b.y ^ tag1) | (b.z ^ tag0) | (b.w ^ tag1);
        if (!__any(bad != 0u) || ++spins > SPIN_CAP) break;
        __builtin_amdgcn_s_sleep(4);
      }
      REREAD();
      if (bid == 0) st8<SC1>(v2u{(unsigned)(gen + 1ull), (unsigned)((gen + 1ull) >> 32)}, rws, lane == 0 ? WS_E : OOB);
    }
    class_bar<1>(ctr + 3, lane);
#pragma unroll
    for (int ww = 0; ww < 8; ++ww) m = fmaxf(m, *(const LAS float*)(L + L_M + (ww * 8 + hl) * 4));
  }
  unsigned pb[16];
  float lsum = 0.f;
  {
    const unsigned msk = wb ? 0xffff0000u : 0x0000ffffu;
#pragma unroll
    for (int i = 0; i < 16; ++i) {
      const float e = __builtin_amdgcn_exp2f((s[i] - m) * LOG2E);
      lsum += e;
      const unsigned t = pk(e, e) & msk;
      pb[i] = t | f2u(ror<8>(u2f(t)));
      asm volatile("" : "+v"(pb[i]));
    }
    lsum += ror<8>(lsum);
    lsum = xrow_sum(lsum);
    if (lane < 8) *(LAS float*)(L + L_L + (w * 8 + lane) * 4) = lsum;
  }
  f32x4 acc[8];
#pragma unroll
  for (int j = 0; j < 8; ++j) acc[j] = f32x4{0.f, 0.f, 0.f, 0.f};
  asm volatile("" :: "s"(rv));
  __builtin_amdgcn_s_setprio(1);
  BAR();
  if (isA) __builtin_amdgcn_s_sleep(VPRE);
#define PV(n) acc[(n) & 7] = mfma(tr8(&r[(n) & ~7], (n) & 7), v4u{pb[((n) >> 3) * 4], pb[((n) >> 3) * 4 + 1], pb[((n) >> 3) * 4 + 2], pb[((n) >> 3) * 4 + 3]}, acc[(n) & 7])
#pragma unroll
  for (int i = 0; i < 32; ++i) {
    if (i >= VEND) dyn_pace<15, 5, 7, 12>(lag);
    else if (i >= W0) dyn_pace<TH2 - TH1, W0, W1, W2>(lag);
    else if (i > 0) __builtin_amdgcn_s_sleep(RAMP);
    r[i] = ld16<NT>(rv, voff, (i >> 2) * 16384 + (i & 3) * 1024);
    PIN();
    if ((i & 3) == 0 || (i >= VEND - 1 && i < 31)) UPDATE_LAG(S2 + T2 + T3 + (isA ? T4 * i / 32 + (i >= VEND - 1 ? 25 : 0) : T4 + T5 * i / 32));
    if (i >= 7 + W2 && i < 23 + W2) { PV(i - 7 - W2); PIN(); }
  }
  const int mo = a_ml(wl * 4 + (hd >> 3), (n16 + 4 * g) & 15) + (hd & 7) * 8;
  v2u ml = ld8<SC1>(rws, isA ? OOB : mo, 0);
  BAR();
#pragma unroll
  for (int n = 16; n < 32; ++n) { PV(n); if ((n & 7) == 7) PIN(); }
#define PUT_ACC(f) _Pragma("unroll") for (int i = 0; i < 4; ++i) { \
      const f32x4 t = {wb ? acc[4][i] : acc[0][i], wb ? acc[5][i] : acc[1][i], wb ? acc[6][i] : acc[2][i], wb ? acc[7][i] : acc[3][i]}; \
      *(LAS f32x4*)(L + L_O + wl * O_W + (hl * 132 + 8 * (4 * g + i) + 4 * wb) * 4) = f t; }
  const int oro = ((2 * wl + (g & 1)) * 132 + 8 * n16 + 4 * (g >> 1)) * 4;
  if (isA) {
    __builtin_amdgcn_s_setprio(0);
    float M = m;
#pragma unroll
    for (int ww = 0; ww < 8; ++ww) M = fmaxf(M, *(const LAS float*)(L + L_M + (ww * 8 + hl) * 4));
    const float fa = __builtin_amdgcn_exp2f((m - M) * LOG2E);
    PUT_ACC(fa *);
    if (lane < 8) *(LAS float*)(L + L_L + (w * 8 + lane) * 4) = fa * lsum;
    class_bar<1>(ctr + 4, lane);
    {
      f32x4 t = *(const LAS f32x4*)(L + L_O + oro);
#pragma unroll
      for (int ww = 1; ww < 4; ++ww) t += *(const LAS f32x4*)(L + L_O + ww * O_W + oro);
      *(LAS f32x4*)(L + L_O + 4 * O_W + oro) = t;
    }
    if (w == 0) {
      float l = 0.f;
#pragma unroll
      for (int ww = 0; ww < 8; ++ww) l += *(const LAS float*)(L + L_L + (ww * 8 + hl) * 4);
      st8<SC1>(v2u{nz(f2u(M)), nz(f2u(l))}, rws, lane < 8 ? a_ml(gidx, sp) + lane * 8 : OOB);
    }
    {
      const int hc = lane & 7, hbt = (lane >> 3) & 3, hb = 112 * hd + 28 * wl + 4 * hc;
      const int ho = hc < 7 ? a_h(2 * (lane >> 5), hbt) + hb * 4 : OOB;
      const v4u h0 = ld16<SC1>(rws, ho, 0), h1 = ld16<SC1>(rws, ho, FF * 4);
      const v2u bia = ld8<0>(mk(p.b1, FF * 2), hc < 7 ? hb * 2 : OOB, 0);
      PIN();
      float hv[4];
#pragma unroll
      for (int j = 0; j < 4; ++j) {
        const float t0 = u2f(h0[j]) + u2f(h1[j]);
        auto sw = __builtin_amdgcn_permlane32_swap(f2u(t0), f2u(t0), false, false);
        const float t = u2f(sw[0]) + u2f(sw[1]);
        const float u = rbf(rbf(t) + ((j & 1) ? bhi(bia[j >> 1]) : blo(bia[j >> 1])));
        const float z2 = u * fmaf(u * u, 0.044715f, 1.0f) * (-2.0f * 0.7978845608028654f * LOG2E);
        hv[j] = u * __builtin_amdgcn_rcpf(1.0f + __builtin_amdgcn_exp2f(z2));
      }
      if (lane < 32) {
#pragma unroll
        for (int k = 0; k < 4; ++k) *(LAS unsigned short*)(L + L_H + wl * 256 + hbt * 64 + k * 16 + hc * 2) = (unsigned short)pk(hv[k], hv[k]);
      }
    }
    const rsrc_t r2 = mk(p.w2, FF * DM * 2), ro = mk(p.wo, NQ * DM * 2);
    const int o2 = (112 * hd + 28 * wl + g) * 2048 + cg * 256 + n16 * 16;
    const int oo = (128 * hd + 32 * wl + g) * 2048 + cg * 256 + n16 * 16;
    REREAD();
    v4u hact[4];
#pragma unroll
    for (int q = 0; q < 4; ++q) hact[q] = ACT(L_H + wl * 256 + (n16 & 3) * 64 + g * 16, q);
    int rdo = L_RED + (lane & 1) * RED_E + ((lane >> 1) & 3) * RED_G + (4 * wl + ((lane >> 3) & 3)) * 16;
    int fo = lane < 32 ? a_f(32 * cg + 8 * wl + (lane >> 2), hd) + (lane & 3) * 16 : OOB;
    int oa[4];
#pragma unroll
    for (int q = 0; q < 4; ++q) { oa[q] = (n16 >> 2) == q ? L_OA + (n16 & 3) * 256 + (32 * wl + 8 * g) * 2 : L_ZERO; asm volatile("" : "+v"(oa[q])); }
    asm volatile("" : "+v"(rdo), "+v"(fo));
    asm volatile("" :: "s"(r2), "s"(ro));
    asm volatile("" :: "v"(o2), "v"(oo));
    v4u tw[16];
    tw[7] = v4u{0u, 0u, 0u, 0u};
    BAR();
    _Pragma("nounroll") while (now32() - tbase < S6 + TW) __builtin_amdgcn_s_sleep(0);
#pragma unroll
    for (int i = 0; i < 15; ++i) {
      constexpr int ord[15] = {0, 1, 8, 9, 2, 3, 10, 11, 4, 5, 12, 13, 6, 14, 15};
      const int j = ord[i];
      if (i >= WINT) pace<WINT - 1>();
      else if (i > 0) __builtin_amdgcn_s_sleep(TRAMP);
      tw[j] = j < 8 ? ld16<NT>(r2, o2, j * 8192) : ld16<NT>(ro, oo, (j - 8) * 8192);
      PIN();
    }
    f32x4 d[2] = {{0.f, 0.f, 0.f, 0.f}, {0.f, 0.f, 0.f, 0.f}};
    v4u wt[8], w2t[8];
#define TPAIR(dst, base, k) do { _Pragma("unroll") for (int j0 = 0; j0 < 8; ++j0) dst[j0][k] = tr2(tw[(base) + 2 * (k)], tw[(base) + 2 * (k) + 1], j0); PIN(); } while (0)
    TPAIR(w2t, 0, 0); TPAIR(wt, 8, 0); TPAIR(w2t, 0, 1); TPAIR(wt, 8, 1); TPAIR(w2t, 0, 2); TPAIR(wt, 8, 2); TPAIR(w2t, 0, 3);
#pragma unroll
    for (int j0 = 0; j0 < 8; ++j0) d[j0 & 1] = mfma(hact[j0 >> 1], w2t[j0], d[j0 & 1]);
    PIN();
    BAR();
    v4u oact[4];
#pragma unroll
    for (int q = 0; q < 4; ++q) oact[q] = *(const LAS v4u*)(L + oa[q]);
    PIN();
    TPAIR(wt, 8, 3);
#pragma unroll
    for (int j0 = 0; j0 < 8; ++j0) d[j0 & 1] = mfma(oact[j0 >> 1], wt[j0], d[j0 & 1]);
    PUT_RED(d);
    BAR();
    const f32x4 t = (*(const LAS f32x4*)(L + rdo) + *(const LAS f32x4*)(L + rdo + RED_W)) + (*(const LAS f32x4*)(L + rdo + 2 * RED_W) + *(const LAS f32x4*)(L + rdo + 3 * RED_W));
    st16<SC1>(v4u{nz(f2u(t[0])), nz(f2u(t[1])), nz(f2u(t[2])), nz(f2u(t[3]))}, rws, fo);
    asm volatile(".Lfd_code_end:");
  } else {
    PUT_ACC();
    class_bar<0>(ctr + 5, lane);
    {
      f32x4 t = *(const LAS f32x4*)(L + L_O + 4 * O_W + oro);
#pragma unroll
      for (int ww = 0; ww < 4; ++ww) t += *(const LAS f32x4*)(L + L_O + ww * O_W + oro);
      const unsigned a0 = nz(pk(t[0], t[1])), a1 = nz(pk(t[2], t[3]));
      auto s0 = __builtin_amdgcn_permlane32_swap(a0, a0, false, false);
      auto s1 = __builtin_amdgcn_permlane32_swap(a1, a1, false, false);
      st16<SC1>(v4u{s0[0], s1[0], s0[1], s1[1]}, rws, lane < 32 ? a_o(8 * kvh + 2 * wl + g, ab, sp) + n16 * 16 : OOB);
    }
    PIN();
    __builtin_amdgcn_s_setprio(0);
    if (__any(min(ml.x, ml.y) == 0u)) {
      int spins = 0;
      _Pragma("nounroll") while (true) {
        REREAD();
        ml = ld8<SC1>(rws, mo, 0);
        if (!__any(min(ml.x, ml.y) == 0u) || ++spins > SPIN_CAP) break;
        __builtin_amdgcn_s_sleep(2);
      }
    }
    float M = u2f(ml.x);
    M = fmaxf(M, ror<8>(M)); M = fmaxf(M, ror<4>(M)); M = fmaxf(M, ror<2>(M)); M = fmaxf(M, ror<1>(M));
    const float e = __builtin_amdgcn_exp2f((u2f(ml.x) - M) * LOG2E);
    float Ls = e * u2f(ml.y);
    Ls += ror<8>(Ls); Ls += ror<4>(Ls); Ls += ror<2>(Ls); Ls += ror<1>(Ls);
    const float f = e * __builtin_amdgcn_rcpf(Ls);
    float fk[4] = {dpp<0x150>(f), dpp<0x151>(f), dpp<0x152>(f), dpp<0x153>(f)};
    int wro = L_OA + wl * 256 + (n16 >> 2) * 64 + g * 16 + (n16 & 3) * 4, fo = a_f(cu, 0) + lane * 16, oof = lane < 16 ? (fb * DM + fc) * 2 : OOB;
    float res = blo(sm.x) + blo(sm.y);
    asm volatile("" : "+v"(fk[0]), "+v"(fk[1]), "+v"(fk[2]), "+v"(fk[3]), "+v"(wro), "+v"(fo), "+v"(oof), "+v"(res));
    __builtin_amdgcn_s_sleep(ODMIN);
    _Pragma("nounroll") while (now32() - tbase < S6 + TO) __builtin_amdgcn_s_sleep(1);
    const int oo_ = a_o(hd, wl, 4 * g) + n16 * 16;
    v4u op[4];
#pragma unroll
    for (int k = 0; k < 4; ++k) op[k] = ld16<SC1>(rws, oo_, k * 256);
    PIN();
    if (__any(min(min(umin4(op[0]), umin4(op[1])), min(umin4(op[2]), umin4(op[3]))) == 0u)) {
      int spins = 0;
      _Pragma("nounroll") while (true) {
        REREAD();
        v4u nw[4];
#pragma unroll
        for (int k = 0; k < 4; ++k) nw[k] = ld16<SC1>(rws, umin4(op[k]) == 0u ? oo_ : OOB, k * 256);
        PIN();
#pragma unroll
        for (int k = 0; k < 4; ++k) op[k] |= nw[k];
        if (++spins > SPIN_CAP || !__any(min(min(umin4(op[0]), umin4(op[1])), min(umin4(op[2]), umin4(op[3]))) == 0u)) break;
        __builtin_amdgcn_s_sleep(1);
      }
    }
    f32x2 t[4];
#pragma unroll
    for (int j = 0; j < 4; ++j) {
      t[j] = fk[0] * f32x2{blo(op[0][j]), bhi(op[0][j])};
#pragma unroll
      for (int k = 1; k < 4; ++k) t[j] = __builtin_elementwise_fma(f32x2{fk[k], fk[k]}, f32x2{blo(op[k][j]), bhi(op[k][j])}, t[j]);
    }
    float u[4];
#pragma unroll
    for (int j = 0; j < 4; ++j) {
      auto sw = __builtin_amdgcn_permlane32_swap(f2u(t[2 * (j >> 1)][j & 1]), f2u(t[2 * (j >> 1) + 1][j & 1]), false, false);
      u[j] = u2f(sw[0]) + u2f(sw[1]);
    }
    auto s0 = __builtin_amdgcn_permlane16_swap(f2u(u[0]), f2u(u[1]), false, false);
    auto s1 = __builtin_amdgcn_permlane16_swap(f2u(u[2]), f2u(u[3]), false, false);
    *(LAS unsigned*)(L + wro) = pk(u2f(s0[0]) + u2f(s0[1]), u2f(s1[0]) + u2f(s1[1]));
    BAR();
    BAR();
    if (wl == 2) {
      v4u f0, f1;
      int spins = 0;
      __builtin_amdgcn_s_sleep(FDELAY);
      _Pragma("nounroll") while (true) {
        REREAD();
        f0 = ld16<SC1>(rws, fo, 0);
        f1 = ld16<SC1>(rws, fo, 1024);
        if (!__any(min(umin4(f0), umin4(f1)) == 0u) || ++spins > SPIN_CAP) break;
        __builtin_amdgcn_s_sleep(1);
      }
      const f32x4 y4 = __builtin_bit_cast(f32x4, f0) + __builtin_bit_cast(f32x4, f1);
      const bool h8 = lane & 8, h4 = lane & 4;
      const float a0 = (h8 ? y4[2] : y4[0]) + ror<8>(h8 ? y4[0] : y4[2]), a1 = (h8 ? y4[3] : y4[1]) + ror<8>(h8 ? y4[1] : y4[3]);
      const float y = res + xrow_sum((h4 ? a1 : a0) + dpp<0x1B>(dpp<0x141>(h4 ? a0 : a1)));
      __builtin_amdgcn_raw_buffer_store_b16((unsigned short)pk(y, y), mk(p.out, 4 * DM * 2), oof, 0, 0);
    }
  }
}
}

extern "C" void kernel_launch(void* const* d_in, const int* in_sizes, int n_in, void* d_out, int out_size, void* d_ws, size_t ws_size,
                              hipStream_t stream) {
  Params p;
  p.x = d_in[0]; p.kc = d_in[1]; p.vc = d_in[2]; p.wq = d_in[3]; p.wo = d_in[4];
  p.w1 = d_in[6]; p.b1 = d_in[7]; p.w2 = d_in[8]; p.b2 = d_in[9];
  p.out = d_out; p.ws = d_ws;
  hipLaunchKernelGGL(fused_decode, dim3(NWG), dim3(NTHR), 0, stream, p);
}
```
